```python
import torch

def _softmax(x, axis=-1):
    return torch.softmax(x, dim=axis)
def _gelu(x):
    return torch.nn.functional.gelu(x, approximate="tanh")
def _silu(x):
    return torch.nn.functional.silu(x)
def _sigmoid(x):
    return torch.sigmoid(x)
def _layernorm(x, axis=-1, eps=1e-5):
    m = x.mean(dim=axis, keepdim=True)
    v = x.var(dim=axis, keepdim=True, unbiased=False)
    return (x - m) / torch.sqrt(v + eps)
def _scatter_add(tgt, idx, src, axis=0):
    return tgt.clone().index_add_(axis, idx, src)
def _segment_sum(src, seg, n):
    out = torch.zeros((n,) + src.shape[1:], dtype=src.dtype)
    return out.index_add_(0, seg, src)
def _pad(x, pw):
    flat = tuple(v for pair in reversed(pw) for v in pair)
    return torch.nn.functional.pad(x, flat)

kernel_name = 'concat_heads__gqa_block_cached__gqa_block_cached__near_ridge'

def setup_inputs():

    _w = (lambda a: (lambda t: t.to(torch.bfloat16) if t.is_floating_point() else t)(torch.as_tensor(a)))
    def _A_setup():
        B, Sq, D = 8, 160, 640
        Hq, Hkv, Sk, Dh = 32, 32, 512, 128
        assert Hq % Hkv == 0, (Hq, Hkv)
        return {
            "x": _w((lambda *s: torch.randn(*s, dtype=torch.bfloat16) * 0.8)(B, Sq, D)),
            "k_cache": _w((lambda *s: torch.randn(*s, dtype=torch.bfloat16) * 0.8)(B, Hkv, Sk, Dh)),
            "v_cache": _w((lambda *s: torch.randn(*s, dtype=torch.bfloat16) * 0.05)(B, Hkv, Sk, Dh)),
            "wq": _w((lambda *s: torch.randn(*s, dtype=torch.bfloat16) * 0.8)(D, Hq * Dh) * (D ** -0.5)),
            "wo": _w((lambda *s: torch.randn(*s, dtype=torch.bfloat16) * 0.05)(Hq * Dh, D) * ((Hq * Dh) ** -0.5)),
        }
    _A_in = _A_setup()

    def _B_setup():
        B, Sq, D = 8, 160, 640
        Hq, Hkv, Sk, Dh = 32, 32, 512, 128
        assert Hq % Hkv == 0, (Hq, Hkv)
        return {
            "x": _w((lambda *s: torch.randn(*s, dtype=torch.bfloat16) * 0.8)(B, Sq, D)),
            "k_cache": _w((lambda *s: torch.randn(*s, dtype=torch.bfloat16) * 0.8)(B, Hkv, Sk, Dh)),
            "v_cache": _w((lambda *s: torch.randn(*s, dtype=torch.bfloat16) * 0.05)(B, Hkv, Sk, Dh)),
            "wq": _w((lambda *s: torch.randn(*s, dtype=torch.bfloat16) * 0.8)(D, Hq * Dh) * (D ** -0.5)),
            "wo": _w((lambda *s: torch.randn(*s, dtype=torch.bfloat16) * 0.05)(Hq * Dh, D) * ((Hq * Dh) ** -0.5)),
        }
    _B_in = _B_setup()

    return {
        "A_x": _A_in["x"],
        "A_k_cache": _A_in["k_cache"],
        "A_v_cache": _A_in["v_cache"],
        "A_wq": _A_in["wq"],
        "A_wo": _A_in["wo"],
        "B_x": _B_in["x"],
        "B_k_cache": _B_in["k_cache"],
        "B_v_cache": _B_in["v_cache"],
        "B_wq": _B_in["wq"],
        "B_wo": _B_in["wo"],
    }

def reference(A_x, A_k_cache, A_v_cache, A_wq, A_wo, B_x, B_k_cache, B_v_cache, B_wq, B_wo):
    def _A_ref(x, k_cache, v_cache, wq, wo):
        B, Sq, D = x.shape
        Hq, Dh = 32, 128
        Hkv = k_cache.shape[1]
        rep = Hq // Hkv
        q = (lambda x, *d: x.permute(*d))((x @ wq).reshape(B, Sq, Hq, Dh), 0, 2, 1, 3)
        k = (lambda x, n, axis: torch.repeat_interleave(x, n, dim=axis))(k_cache, rep, axis=1)
        v = (lambda x, n, axis: torch.repeat_interleave(x, n, dim=axis))(v_cache, rep, axis=1)
        scores = torch.einsum("bhqd,bhkd->bhqk", q, k) * (Dh ** -0.5)
        p = _softmax(scores, axis=-1)
        o = torch.einsum("bhqk,bhkd->bhqd", p, v)
        return (lambda x, *d: x.permute(*d))(o, 0, 2, 1, 3).reshape(B, Sq, Hq * Dh) @ wo

    def _B_ref(x, k_cache, v_cache, wq, wo):
        B, Sq, D = x.shape
        Hq, Dh = 32, 128
        Hkv = k_cache.shape[1]
        rep = Hq // Hkv
        q = (lambda x, *d: x.permute(*d))((x @ wq).reshape(B, Sq, Hq, Dh), 0, 2, 1, 3)
        k = (lambda x, n, axis: torch.repeat_interleave(x, n, dim=axis))(k_cache, rep, axis=1)
        v = (lambda x, n, axis: torch.repeat_interleave(x, n, dim=axis))(v_cache, rep, axis=1)
        scores = torch.einsum("bhqd,bhkd->bhqk", q, k) * (Dh ** -0.5)
        p = _softmax(scores, axis=-1)
        o = torch.einsum("bhqk,bhkd->bhqd", p, v)
        return (lambda x, *d: x.permute(*d))(o, 0, 2, 1, 3).reshape(B, Sq, Hq * Dh) @ wo

    a = _A_ref(A_x, A_k_cache, A_v_cache, A_wq, A_wo)
    b = _B_ref(B_x, B_k_cache, B_v_cache, B_wq, B_wo)
    return torch.concatenate([a, b], axis=-1)
```

```cpp
#include <hip/hip_runtime.h>
#include <hip/hip_bf16.h>
typedef unsigned short u16;
__device__ __forceinline__ float bf2f(u16 v){ return __uint_as_float(((unsigned)v)<<16); }
__device__ __forceinline__ u16 f2bf(float f){ unsigned u=__float_as_uint(f); u += 0x7fffu + ((u>>16)&1u); return (u16)(u>>16); }
constexpr int B=8,SQ=160,D=640,H=32,SK=512,DH=128,M=B*SQ,HD=H*DH;
__global__ void ngemm(const u16* A, const u16* W, u16* C, int K, int N, int ldc, int coff){
  int n = blockIdx.x*blockDim.x+threadIdx.x; int m = blockIdx.y;
  if(n>=N) return;
  float acc=0.f;
  for(int k=0;k<K;++k) acc += bf2f(A[(long)m*K+k])*bf2f(W[(long)k*N+n]);
  C[(long)m*ldc+coff+n]=f2bf(acc);
}
__global__ void nattn(const u16* Q, const u16* Kc, const u16* Vc, u16* O){
  int s=blockIdx.x, h=blockIdx.y, b=blockIdx.z, t=threadIdx.x;
  __shared__ float q[DH]; __shared__ float p[SK]; __shared__ float red[128];
  q[t]=bf2f(Q[((long)(b*SQ+s))*HD+h*DH+t]);
  __syncthreads();
  const u16* Kb=Kc+((long)(b*H+h))*SK*DH; const u16* Vb=Vc+((long)(b*H+h))*SK*DH;
  float mx=-1e30f;
  for(int j=t;j<SK;j+=128){ float a=0.f; for(int d=0;d<DH;++d) a+=q[d]*bf2f(Kb[(long)j*DH+d]); a*=0.08838834764831845f; p[j]=a; mx=fmaxf(mx,a);}
  red[t]=mx; __syncthreads();
  for(int o=64;o>0;o>>=1){ if(t<o) red[t]=fmaxf(red[t],red[t+o]); __syncthreads(); }
  mx=red[0]; __syncthreads();
  float sm=0.f;
  for(int j=t;j<SK;j+=128){ float e=__expf(p[j]-mx); p[j]=e; sm+=e; }
  red[t]=sm; __syncthreads();
  for(int o=64;o>0;o>>=1){ if(t<o) red[t]+=red[t+o]; __syncthreads(); }
  sm=red[0];
  float acc=0.f;
  for(int j=0;j<SK;++j) acc+=p[j]*bf2f(Vb[(long)j*DH+t]);
  O[((long)(b*SQ+s))*HD+h*DH+t]=f2bf(acc/sm);
}
extern "C" void kernel_launch(void* const* d_in, const int* in_sizes, int n_in, void* d_out, int out_size, void* d_ws, size_t ws_size, hipStream_t stream){
  u16* ws=(u16*)d_ws;
  for(int br=0;br<2;++br){
    const u16* x=(const u16*)d_in[br*5+0]; const u16* k=(const u16*)d_in[br*5+1]; const u16* v=(const u16*)d_in[br*5+2];
    const u16* wq=(const u16*)d_in[br*5+3]; const u16* wo=(const u16*)d_in[br*5+4];
    u16* q=ws+(long)br*2*M*HD; u16* o=q+(long)M*HD;
    ngemm<<<dim3(HD/256,M),256,0,stream>>>(x,wq,q,D,HD,HD,0);
    nattn<<<dim3(SQ,H,B),128,0,stream>>>(q,k,v,o);
    ngemm<<<dim3((D+255)/256,M),256,0,stream>>>(o,wo,(u16*)d_out,HD,D,2*D,br*D);
  }
}
```

```cpp
#include <hip/hip_runtime.h>
#include <hip/hip_bf16.h>
typedef unsigned short u16;
constexpr size_t WS_O = 0, WS_P = 32u<<20, WS_SYN = 64u<<20;
__device__ __forceinline__ float bf2f(u16 v){ return __uint_as_float(((unsigned)v)<<16); }
__device__ __forceinline__ u16 f2bf(float f){ unsigned u=__float_as_uint(f); if((u&0x7fffffffu)>0x7f800000u) return (u16)((u>>16)|0x40); u += 0x7fffu + ((u>>16)&1u); return (u16)(u>>16); }

__global__ __launch_bounds__(256,1) void fused_kernel(const u16* __restrict__ xA, const u16* __restrict__ kA, const u16* __restrict__ vA, const u16* __restrict__ wqA, const u16* __restrict__ woA,
                                                      const u16* __restrict__ xB, const u16* __restrict__ kB, const u16* __restrict__ vB, const u16* __restrict__ wqB, const u16* __restrict__ woB,
                                                      char* __restrict__ wsp, u16* __restrict__ outp){
  extern __shared__ __attribute__((aligned(16))) char lds[];
  typedef const u16* cptr; typedef __attribute__((address_space(4))) const cptr* kptr;
  kptr ka = (kptr)__builtin_amdgcn_kernarg_segment_ptr();
  unsigned flag, nonce, tid=threadIdx.x, ob0, ob1, wb0, wb1, cb0, cb1, pb0, pb1; unsigned long long syn; int w;
  asm volatile(
  "s_getpc_b64 s[62:63]\n\t"
  "s_bfe_u32 s48, %14, 0x10002\n\t"
  "s_mul_i32 s49, s48, 40\n\t"
  "s_load_dwordx8 s[64:71], %13, s49\n\t"
  "s_add_u32 s49, s49, 32\n\t"
  "s_load_dwordx2 s[72:73], %13, s49\n\t"
  "s_load_dwordx2 s[74:75], %13, 0x50\n\t"
  "v_readfirstlane_b32 s76, %2\n\t"
  "v_and_b32 v244, 63, %2\n\t"
  "s_and_b32 s79, %14, 3\n\t"
  "s_lshr_b32 s77, %14, 6\n\t"
  "s_bfe_u32 s57, %14, 0x30003\n\t"
  "s_lshr_b32 s58, %14, 3\n\t"
  "s_lshr_b32 s76, s76, 6\n\t"
  "s_lshl_b32 s59, s79, 2\n\t"
  "s_add_u32 s59, s59, s77\n\t"
  "s_mov_b32 s26, 5239296\n\t"
  "s_mov_b32 s27, 0x20000\n\t"
  "s_lshr_b32 s46, s76, 1\n\t"
  "s_and_b32 s47, s76, 1\n\t"
  "v_and_b32 v247, 3, v244\n\t"
  "v_bfe_u32 v248, v244, 4, 1\n\t"
  "v_bfe_u32 v249, v244, 2, 2\n\t"
  "v_lshl_add_u32 v248, v248, 3, v249\n\t"
  "s_lshl_b32 s48, s46, 4\n\t"
  "s_lshl_b32 s49, s47, 2\n\t"
  "s_add_u32 s48, s48, s49\n\t"
  "v_add_lshl_u32 v248, v248, s48, 13\n\t"
  "v_lshrrev_b32 v249, 5, v244\n\t"
  "v_lshl_add_u32 v248, v249, 6, v248\n\t"
  "v_lshl_add_u32 v248, v247, 4, v248\n\t"
  "v_add_u32 v28, 4096, v248\n\t"
  "v_add_u32 v29, 3200, v248\n\t"
  "v_add_u32 v30, 2304, v248\n\t"
  "v_add_u32 v31, 1408, v248\n\t"
  "s_lshl_b32 s44, s76, 12\n\t"
  "s_lshl_b32 s45, s76, 10\n\t"
  "s_lshl_b32 s52, s59, 9\n\t"
  "s_sub_u32 s52, s52, 0x1000\n\t"
  "s_ashr_i32 s53, s52, 31\n\t"
  "s_waitcnt lgkmcnt(0)\n\t"
  "s_add_u32 s24, s70, s52\n\t"
  "s_addc_u32 s25, s71, s53\n\t"
  "s_add_u32 m0, s44, 0\n\t"
  "s_mov_b32 s52, 0\n\t"
  "buffer_load_dwordx4 v28, s[24:27], s52 offen offset:0 lds\n\t"
  "buffer_load_dwordx4 v29, s[24:27], s52 offen offset:1024 lds\n\t"
  "buffer_load_dwordx4 v30, s[24:27], s52 offen offset:2048 lds\n\t"
  "buffer_load_dwordx4 v31, s[24:27], s52 offen offset:3072 lds\n\t"
  "s_mul_i32 s48, s57, 204800\n\t"
  "s_add_u32 s20, s64, s48\n\t"
  "s_addc_u32 s21, s65, 0\n\t"
  "s_mov_b32 s22, 204800\n\t"
  "s_mov_b32 s23, 0x20000\n\t"
  "v_lshrrev_b32 v245, 4, v244\n\t"
  "v_lshrrev_b32 v248, 3, v244\n\t"
  "s_lshl_b32 s48, s76, 5\n\t"
  "v_add_u32 v249, s48, v248\n\t"
  "v_mul_u32_u24 v249, 0x500, v249\n\t"
  "v_and_b32 v27, 7, v244\n\t"
  "v_xor_b32 v27, v27, v245\n\t"
  "v_lshlrev_b32 v27, 4, v27\n\t"
  "v_add_u32 v23, v249, v27\n\t"
  "v_xor_b32 v24, 64, v27\n\t"
  "v_add_u32 v24, v24, v249\n\t"
  "v_add_u32 v24, 9216, v24\n\t"
  "v_add_u32 v25, 18432, v23\n\t"
  "v_add_u32 v26, 18432, v24\n\t"
  "s_lshl_b32 s48, s47, 6\n\t"
  "v_xor_b32 v27, s48, v27\n\t"
  "s_lshl_b32 s48, s76, 3\n\t"
  "s_add_u32 s48, s48, 128\n\t"
  "v_add_u32 v249, s48, v248\n\t"
  "v_mul_u32_u24 v249, 0x500, v249\n\t"
  "v_add_u32 v27, v27, v249\n\t"
  "s_add_u32 m0, s44, 32768\n\t"
  "s_mov_b32 s52, 0\n\t"
  "buffer_load_dwordx4 v23, s[20:23], s52 offen offset:0 lds\n\t"
  "buffer_load_dwordx4 v24, s[20:23], s52 offen offset:1024 lds\n\t"
  "buffer_load_dwordx4 v25, s[20:23], s52 offen offset:2048 lds\n\t"
  "buffer_load_dwordx4 v26, s[20:23], s52 offen offset:3072 lds\n\t"
  "s_add_u32 m0, s45, 49152\n\t"
  "s_nop 0\n\t"
  "buffer_load_dwordx4 v27, s[20:23], s52 offen offset:0 lds\n\t"
  "s_add_u32 m0, s44, 16384\n\t"
  "s_mov_b32 s52, 262144\n\t"
  "buffer_load_dwordx4 v28, s[24:27], s52 offen offset:0 lds\n\t"
  "buffer_load_dwordx4 v29, s[24:27], s52 offen offset:1024 lds\n\t"
  "buffer_load_dwordx4 v30, s[24:27], s52 offen offset:2048 lds\n\t"
  "buffer_load_dwordx4 v31, s[24:27], s52 offen offset:3072 lds\n\t"
  "s_lshl_b32 s50, s58, 2\n\t"
  "s_add_u32 s50, s50, s76\n\t"
  "v_min_u32 v9, 17, v244\n\t"
  "v_cmp_lt_u32 vcc, 9, v9\n\t"
  "s_mul_i32 s48, s50, 10\n\t"
  "v_add_u32 v10, s48, v9\n\t"
  "v_mul_u32_u24 v10, 0x500, v10\n\t"
  "v_add_u32 v10, 128, v10\n\t"
  "s_lshl_b32 s48, s50, 3\n\t"
  "s_sub_u32 s48, s48, 10\n\t"
  "v_add_u32 v11, s48, v9\n\t"
  "v_and_b32 v12, 15, v11\n\t"
  "v_lshrrev_b32 v11, 4, v11\n\t"
  "v_lshlrev_b32 v11, 13, v11\n\t"
  "v_lshl_add_u32 v11, v12, 7, v11\n\t"
  "s_lshl_b32 s48, s79, 11\n\t"
  "s_add_u32 s48, s48, 524288\n\t"
  "v_add_u32 v11, s48, v11\n\t"
  "v_cndmask_b32 v10, v10, v11, vcc\n\t"
  "v_mov_b32 v11, 0\n\t"
  "v_mov_b32 v2, s64\n\t"
  "v_mov_b32 v12, s70\n\t"
  "v_cndmask_b32 v2, v2, v12, vcc\n\t"
  "v_mov_b32 v3, s65\n\t"
  "v_mov_b32 v12, s71\n\t"
  "v_cndmask_b32 v3, v3, v12, vcc\n\t"
  "v_lshl_add_u64 v[252:253], v[10:11], 0, v[2:3]\n\t"
  "v_mov_b32 v4, 128\n\t"
  "v_mov_b32 v12, 0x80000\n\t"
  "v_cndmask_b32 v4, v4, v12, vcc\n\t"
  "v_mov_b32 v5, 0\n\t"
  "global_load_dword v251, v[252:253], off\n\t"
  "v_lshl_add_u64 v[252:253], v[4:5], 0, v[252:253]\n\t"
  "global_load_dword v251, v[252:253], off\n\t"
  "v_lshl_add_u64 v[252:253], v[4:5], 0, v[252:253]\n\t"
  "v_bfe_u32 v246, v244, 2, 3\n\t"
  "v_bfe_u32 v248, v244, 1, 3\n\t"
  "v_xor_b32 v248, v248, v245\n\t"
  "v_lshlrev_b32 v248, 4, v248\n\t"
  "v_and_b32 v249, 15, v244\n\t"
  "v_lshlrev_b32 v249, 7, v249\n\t"
  "s_mul_i32 s48, s47, 10240\n\t"
  "s_add_u32 s48, s48, 32768\n\t"
  "v_add_u32 v249, s48, v249\n\t"
  "v_add_u32 v16, v249, v248\n\t"
  "v_xor_b32 v248, 64, v248\n\t"
  "v_add_u32 v17, v249, v248\n\t"
  "v_add_u32 v18, 106496, v16\n\t"
  "v_add_u32 v19, 106496, v17\n\t"
  "v_lshrrev_b32 v248, 5, v244\n\t"
  "v_lshlrev_b32 v248, 13, v248\n\t"
  "v_lshl_add_u32 v248, v246, 6, v248\n\t"
  "v_lshl_add_u32 v248, v247, 4, v248\n\t"
  "s_lshl_b32 s48, s46, 11\n\t"
  "v_add_u32 v20, s48, v248\n\t"
  "v_add_u32 v21, 53248, v20\n\t"
  "v_add_u32 v22, 106496, v20\n\t"
  "v_min_u32 v8, 3, v244\n\t"
  "s_mul_i32 s48, s50, 4\n\t"
  "v_add_u32 v8, s48, v8\n\t"
  "v_min_u32 v8, 496, v8\n\t"
  "v_lshlrev_b32 v8, 7, v8\n\t"
  "v_min_u32 v10, 39, v244\n\t"
  "s_mul_i32 s48, s50, 80\n\t"
  "v_add_lshl_u32 v10, v10, s48, 7\n\t"
  "s_mul_i32 s48, s79, 1310720\n\t"
  "v_add_u32 v10, s48, v10\n\t"
  "v_lshl_add_u64 v[6:7], v[10:11], 0, s[72:73]\n\t"
  "s_mul_i32 s49, s77, 320\n\t"
  "s_add_u32 s48, s48, s49\n\t"
  "s_sub_u32 s48, s48, 2048\n\t"
  "s_ashr_i32 s49, s48, 31\n\t"
  "s_add_u32 s72, s72, s48\n\t"
  "s_addc_u32 s73, s73, s49\n\t"
  "s_lshl_b32 s48, s57, 5\n\t"
  "s_lshl_b32 s49, s59, 1\n\t"
  "s_add_u32 s49, s49, s46\n\t"
  "s_add_u32 s48, s48, s49\n\t"
  "s_lshl_b32 s48, s48, 17\n\t"
  "s_add_u32 s66, s66, s48\n\t"
  "s_addc_u32 s67, s67, 0\n\t"
  "s_add_u32 s68, s68, s48\n\t"
  "s_addc_u32 s69, s69, 0\n\t"
  "s_add_u32 s64, s66, 0x2000\n\t"
  "s_addc_u32 s65, s67, 0\n\t"
  "s_cmp_eq_u32 s47, 0\n\t"
  "s_cselect_b64 s[68:69], s[64:65], s[68:69]\n\t"
  "s_sub_u32 s68, s68, 0x400\n\t"
  "s_subb_u32 s69, s69, 0\n\t"
  "s_bfe_u32 s48, %14, 0x10002\n\t"
  "s_lshl_b32 s48, s48, 3\n\t"
  "s_add_u32 s48, s48, s57\n\t"
  "s_lshl_b32 s48, s48, 1\n\t"
  "s_add_u32 s48, s48, s47\n\t"
  "s_lshl_b32 s48, s48, 5\n\t"
  "s_add_u32 s48, s48, s49\n\t"
  "s_mul_i32 s48, s48, 20480\n\t"
  "s_add_u32 s36, s74, s48\n\t"
  "s_addc_u32 s37, s75, 0\n\t"
  "s_add_u32 s70, s74, 67108864\n\t"
  "s_addc_u32 s71, s75, 0\n\t"
  "s_lshl_b32 s48, %14, 7\n\t"
  "s_add_u32 s74, s70, s48\n\t"
  "s_addc_u32 s75, s71, 0\n\t"
  "s_mov_b32 s28, s68\n\t"
  "s_mov_b32 s29, s69\n\t"
  "s_mov_b32 s31, 0x20000\n\t"
  "s_mov_b32 s48, 123904\n\t"
  "s_cmp_eq_u32 s47, 0\n\t"
  "s_cselect_b32 s30, s48, 132096\n\t"
  "s_mov_b32 s40, s66\n\t"
  "s_mov_b32 s41, s67\n\t"
  "s_mov_b32 s42, 8192\n\t"
  "s_mov_b32 s43, 0x20000\n\t"
  "v_and_b32 v248, 15, v244\n\t"
  "v_xor_b32 v249, v248, v245\n\t"
  "v_lshlrev_b32 v249, 4, v249\n\t"
  "v_lshlrev_b32 v248, 8, v248\n\t"
  "v_lshl_add_u32 v223, v245, 4, v248\n\t"
  "s_lshl_b32 s49, s46, 13\n\t"
  "v_add_u32 v248, s49, v248\n\t"
  "v_xor_b32 v216, 0, v249\n\t"
  "v_add_u32 v216, v216, v248\n\t"
  "v_xor_b32 v217, 64, v249\n\t"
  "v_add_u32 v217, v217, v248\n\t"
  "v_xor_b32 v218, 128, v249\n\t"
  "v_add_u32 v218, v218, v248\n\t"
  "v_xor_b32 v219, 192, v249\n\t"
  "v_add_u32 v219, v219, v248\n\t"
  "v_lshlrev_b32 v248, 8, v245\n\t"
  "v_add_u32 v248, 0x400, v248\n\t"
  "v_xor_b32 v212, 0, v249\n\t"
  "v_add_u32 v212, v212, v248\n\t"
  "v_xor_b32 v213, 64, v249\n\t"
  "v_add_u32 v213, v213, v248\n\t"
  "v_xor_b32 v214, 128, v249\n\t"
  "v_add_u32 v214, v214, v248\n\t"
  "v_xor_b32 v215, 192, v249\n\t"
  "v_add_u32 v215, v215, v248\n\t"
  "s_cmp_eq_u32 s47, 0\n\t"
  "s_cbranch_scc1 10f\n\t"
  "v_bfe_u32 v248, v244, 4, 1\n\t"
  "v_lshlrev_b32 v248, 1, v248\n\t"
  "v_xor_b32 v248, v248, v247\n\t"
  "v_lshlrev_b32 v248, 4, v248\n\t"
  "v_lshl_add_u32 v248, v246, 8, v248\n\t"
  "v_lshrrev_b32 v249, 5, v244\n\t"
  "v_lshl_add_u32 v248, v249, 6, v248\n\t"
  "v_add_u32 v212, 0x400, v248\n\t"
  "v_add_u32 v213, 128, v248\n\t"
  "v_mov_b32 v214, v212\n\t"
  "v_mov_b32 v215, v213\n\t"
  "10:\n\t"
  "s_lshl_b32 s48, s47, 14\n\t"
  "s_add_u32 s51, s48, s49\n\t"
  "v_lshrrev_b32 v248, 5, v244\n\t"
  "v_lshlrev_b32 v248, 11, v248\n\t"
  "v_lshl_add_u32 v248, v246, 6, v248\n\t"
  "v_lshl_add_u32 v248, v247, 3, v248\n\t"
  "s_add_u32 s49, s49, 0x4000\n\t"
  "v_add_u32 v248, s49, v248\n\t"
  "v_bfe_u32 v249, v244, 4, 1\n\t"
  "v_lshlrev_b32 v249, 5, v249\n\t"
  "v_add_u32 v220, v248, v249\n\t"
  "v_xor_b32 v249, 32, v249\n\t"
  "v_add_u32 v221, v248, v249\n\t"
  "s_mov_b32 s38, 20480\n\t"
  "s_mov_b32 s39, 0x20000\n\t"
  "v_lshlrev_b32 v222, 4, v244\n\t"
  "s_mov_b32 s50, 0x3e0293ee\n\t"
  "s_mov_b32 s54, 0x1000\n\t"
  "v_mov_b32 v248, 0x3f803f80\n\t"
  "v_accvgpr_write_b32 a244, v248\n\t"
  "v_accvgpr_write_b32 a248, 0\n\t"
  "v_accvgpr_write_b32 a245, v248\n\t"
  "v_accvgpr_write_b32 a249, 0\n\t"
  "v_accvgpr_write_b32 a246, v248\n\t"
  "v_accvgpr_write_b32 a250, 0\n\t"
  "v_accvgpr_write_b32 a247, v248\n\t"
  "v_accvgpr_write_b32 a251, 0\n\t"
  "s_add_u32 m0, s44, 53248\n\t"
  "s_mov_b32 s52, 524288\n\t"
  "buffer_load_dwordx4 v28, s[24:27], s52 offen offset:0 lds\n\t"
  "buffer_load_dwordx4 v29, s[24:27], s52 offen offset:1024 lds\n\t"
  "buffer_load_dwordx4 v30, s[24:27], s52 offen offset:2048 lds\n\t"
  "buffer_load_dwordx4 v31, s[24:27], s52 offen offset:3072 lds\n\t"
  "s_add_u32 m0, s44, 86016\n\t"
  "s_mov_b32 s52, 128\n\t"
  "buffer_load_dwordx4 v23, s[20:23], s52 offen offset:0 lds\n\t"
  "buffer_load_dwordx4 v24, s[20:23], s52 offen offset:1024 lds\n\t"
  "buffer_load_dwordx4 v25, s[20:23], s52 offen offset:2048 lds\n\t"
  "buffer_load_dwordx4 v26, s[20:23], s52 offen offset:3072 lds\n\t"
  "s_add_u32 m0, s45, 102400\n\t"
  "s_nop 0\n\t"
  "buffer_load_dwordx4 v27, s[20:23], s52 offen offset:0 lds\n\t"
  "s_add_u32 m0, s44, 69632\n\t"
  "s_mov_b32 s52, 786432\n\t"
  "buffer_load_dwordx4 v28, s[24:27], s52 offen offset:0 lds\n\t"
  "buffer_load_dwordx4 v29, s[24:27], s52 offen offset:1024 lds\n\t"
  "buffer_load_dwordx4 v30, s[24:27], s52 offen offset:2048 lds\n\t"
  "buffer_load_dwordx4 v31, s[24:27], s52 offen offset:3072 lds\n\t"
  "v_mov_b32 v255, 0\n\t"
  "global_load_dword v251, v8, s[62:63]\n\t"
  "global_load_dword v254, v255, s[74:75] sc1\n\t"
  "s_waitcnt vmcnt(17)\n\t"
  "s_barrier\n\t"
  "ds_read_b128 v[192:195], v16 offset:0\n\t"
  "ds_read_b128 v[196:199], v16 offset:2048\n\t"
  "ds_read_b128 v[200:203], v16 offset:4096\n\t"
  "ds_read_b128 v[204:207], v16 offset:6144\n\t"
  "ds_read_b128 v[208:211], v16 offset:8192\n\t"
  "ds_read_b64_tr_b16 a[180:181], v20 offset:0\n\t"
  "ds_read_b64_tr_b16 a[182:183], v20 offset:4096\n\t"
  "ds_read_b64_tr_b16 a[184:185], v20 offset:8\n\t"
  "ds_read_b64_tr_b16 a[186:187], v20 offset:4104\n\t"
  "ds_read_b64_tr_b16 a[188:189], v20 offset:512\n\t"
  "ds_read_b64_tr_b16 a[190:191], v20 offset:4608\n\t"
  "ds_read_b64_tr_b16 a[192:193], v20 offset:520\n\t"
  "ds_read_b64_tr_b16 a[194:195], v20 offset:4616\n\t"
  "ds_read_b64_tr_b16 a[196:197], v20 offset:1024\n\t"
  "ds_read_b64_tr_b16 a[198:199], v20 offset:5120\n\t"
  "ds_read_b64_tr_b16 a[200:201], v20 offset:1032\n\t"
  "ds_read_b64_tr_b16 a[202:203], v20 offset:5128\n\t"
  "ds_read_b64_tr_b16 a[204:205], v20 offset:1536\n\t"
  "ds_read_b64_tr_b16 a[206:207], v20 offset:5632\n\t"
  "ds_read_b64_tr_b16 a[208:209], v20 offset:1544\n\t"
  "ds_read_b64_tr_b16 a[210:211], v20 offset:5640\n\t"
  "s_waitcnt lgkmcnt(0)\n\t"
  "v_mfma_f32_16x16x32_bf16 v[32:35], a[180:183], v[192:195], 0\n\t"
  "s_add_u32 m0, s44, 106496\n\t"
  "s_mov_b32 s52, 1048576\n\t"
  "buffer_load_dwordx4 v28, s[24:27], s52 offen offset:0 lds\n\t"
  "v_mfma_f32_16x16x32_bf16 v[48:51], a[180:183], v[196:199], 0\n\t"
  "v_mfma_f32_16x16x32_bf16 v[64:67], a[180:183], v[200:203], 0\n\t"
  "ds_read_b128 v[224:227], v17 offset:0\n\t"
  "v_mfma_f32_16x16x32_bf16 v[80:83], a[180:183], v[204:207], 0\n\t"
  "ds_read_b128 v[228:231], v17 offset:2048\n\t"
  "buffer_load_dwordx4 v29, s[24:27], s52 offen offset:1024 lds\n\t"
  "v_mfma_f32_16x16x32_bf16 v[96:99], a[180:183], v[208:211], 0\n\t"
  "v_mfma_f32_16x16x32_bf16 v[112:115], a[184:187], v[192:195], 0\n\t"
  "ds_read_b128 v[232:235], v17 offset:4096\n\t"
  "ds_read_b128 v[236:239], v17 offset:6144\n\t"
  "v_mfma_f32_16x16x32_bf16 v[128:131], a[184:187], v[196:199], 0\n\t"
  "buffer_load_dwordx4 v30, s[24:27], s52 offen offset:2048 lds\n\t"
  "v_mfma_f32_16x16x32_bf16 v[144:147], a[184:187], v[200:203], 0\n\t"
  "ds_read_b128 v[240:243], v17 offset:8192\n\t"
  "v_mfma_f32_16x16x32_bf16 v[160:163], a[184:187], v[204:207], 0\n\t"
  "buffer_load_dwordx4 v31, s[24:27], s52 offen offset:3072 lds\n\t"
  "v_mfma_f32_16x16x32_bf16 v[176:179], a[184:187], v[208:211], 0\n\t"
  "v_mfma_f32_16x16x32_bf16 v[36:39], a[188:191], v[192:195], 0\n\t"
  "ds_read_b64_tr_b16 a[212:213], v20 offset:16384\n\t"
  "s_add_u32 m0, s44, 139264\n\t"
  "s_mov_b32 s52, 256\n\t"
  "buffer_load_dwordx4 v23, s[20:23], s52 offen offset:0 lds\n\t"
  "v_mfma_f32_16x16x32_bf16 v[52:55], a[188:191], v[196:199], 0\n\t"
  "v_mfma_f32_16x16x32_bf16 v[68:71], a[188:191], v[200:203], 0\n\t"
  "v_mfma_f32_16x16x32_bf16 v[84:87], a[188:191], v[204:207], 0\n\t"
  "ds_read_b64_tr_b16 a[214:215], v20 offset:20480\n\t"
  "ds_read_b64_tr_b16 a[216:217], v20 offset:16392\n\t"
  "buffer_load_dwordx4 v24, s[20:23], s52 offen offset:1024 lds\n\t"
  "v_mfma_f32_16x16x32_bf16 v[100:103], a[188:191], v[208:211], 0\n\t"
  "v_mfma_f32_16x16x32_bf16 v[116:119], a[192:195], v[192:195], 0\n\t"
  "ds_read_b64_tr_b16 a[218:219], v20 offset:20488\n\t"
  "v_mfma_f32_16x16x32_bf16 v[132:135], a[192:195], v[196:199], 0\n\t"
  "buffer_load_dwordx4 v25, s[20:23], s52 offen offset:2048 lds\n\t"
  "v_mfma_f32_16x16x32_bf16 v[148:151], a[192:195], v[200:203], 0\n\t"
  "ds_read_b64_tr_b16 a[220:221], v20 offset:16896\n\t"
  "v_mfma_f32_16x16x32_bf16 v[164:167], a[192:195], v[204:207], 0\n\t"
  "ds_read_b64_tr_b16 a[222:223], v20 offset:20992\n\t"
  "buffer_load_dwordx4 v26, s[20:23], s52 offen offset:3072 lds\n\t"
  "v_mfma_f32_16x16x32_bf16 v[180:183], a[192:195], v[208:211], 0\n\t"
  "v_mfma_f32_16x16x32_bf16 v[40:43], a[196:199], v[192:195], 0\n\t"
  "ds_read_b64_tr_b16 a[224:225], v20 offset:16904\n\t"
  "s_add_u32 m0, s45, 155648\n\t"
  "s_nop 0\n\t"
  "buffer_load_dwordx4 v27, s[20:23], s52 offen offset:0 lds\n\t"
  "v_mfma_f32_16x16x32_bf16 v[56:59], a[196:199], v[196:199], 0\n\t"
  "v_mfma_f32_16x16x32_bf16 v[72:75], a[196:199], v[200:203], 0\n\t"
  "v_mfma_f32_16x16x32_bf16 v[88:91], a[196:199], v[204:207], 0\n\t"
  "ds_read_b64_tr_b16 a[226:227], v20 offset:21000\n\t"
  "v_mfma_f32_16x16x32_bf16 v[104:107], a[196:199], v[208:211], 0\n\t"
  "ds_read_b64_tr_b16 a[228:229], v20 offset:17408\n\t"
  "s_add_u32 m0, s44, 122880\n\t"
  "s_mov_b32 s52, 1310720\n\t"
  "buffer_load_dwordx4 v28, s[24:27], s52 offen offset:0 lds\n\t"
  "v_mfma_f32_16x16x32_bf16 v[120:123], a[200:203], v[192:195], 0\n\t"
  "v_mfma_f32_16x16x32_bf16 v[136:139], a[200:203], v[196:199], 0\n\t"
  "v_mfma_f32_16x16x32_bf16 v[152:155], a[200:203], v[200:203], 0\n\t"
  "ds_read_b64_tr_b16 a[230:231], v20 offset:21504\n\t"
  "ds_read_b64_tr_b16 a[232:233], v20 offset:17416\n\t"
  "buffer_load_dwordx4 v29, s[24:27], s52 offen offset:1024 lds\n\t"
  "v_mfma_f32_16x16x32_bf16 v[168:171], a[200:203], v[204:207], 0\n\t"
  "v_mfma_f32_16x16x32_bf16 v[184:187], a[200:203], v[208:211], 0\n\t"
  "ds_read_b64_tr_b16 a[234:235], v20 offset:21512\n\t"
  "v_mfma_f32_16x16x32_bf16 v[44:47], a[204:207], v[192:195], 0\n\t"
  "buffer_load_dwordx4 v30, s[24:27], s52 offen offset:2048 lds\n\t"
  "v_mfma_f32_16x16x32_bf16 v[60:63], a[204:207], v[196:199], 0\n\t"
  "ds_read_b64_tr_b16 a[236:237], v20 offset:17920\n\t"
  "v_mfma_f32_16x16x32_bf16 v[76:79], a[204:207], v[200:203], 0\n\t"
  "ds_read_b64_tr_b16 a[238:239], v20 offset:22016\n\t"
  "buffer_load_dwordx4 v31, s[24:27], s52 offen offset:3072 lds\n\t"
  "v_mfma_f32_16x16x32_bf16 v[92:95], a[204:207], v[204:207], 0\n\t"
  "v_mfma_f32_16x16x32_bf16 v[108:111], a[204:207], v[208:211], 0\n\t"
  "ds_read_b64_tr_b16 a[240:241], v20 offset:17928\n\t"
  "global_load_dword v251, v[252:253], off\n\t"
  "v_lshl_add_u64 v[252:253], v[4:5], 0, v[252:253]\n\t"
  "v_mfma_f32_16x16x32_bf16 v[124:127], a[208:211], v[192:195], 0\n\t"
  "v_mfma_f32_16x16x32_bf16 v[140:143], a[208:211], v[196:199], 0\n\t"
  "v_mfma_f32_16x16x32_bf16 v[156:159], a[208:211], v[200:203], 0\n\t"
  "ds_read_b64_tr_b16 a[242:243], v20 offset:22024\n\t"
  "global_load_dword v251, v[252:253], off\n\t"
  "v_lshl_add_u64 v[252:253], v[4:5], 0, v[252:253]\n\t"
  "v_mfma_f32_16x16x32_bf16 v[172:175], a[208:211], v[204:207], 0\n\t"
  "v_mfma_f32_16x16x32_bf16 v[188:191], a[208:211], v[208:211], 0\n\t"
  "s_waitcnt vmcnt(17) lgkmcnt(0)\n\t"
  "s_barrier\n\t"
  "v_mfma_f32_16x16x32_bf16 v[32:35], a[212:215], v[224:227], v[32:35]\n\t"
  "s_add_u32 m0, s44, 0\n\t"
  "s_mov_b32 s52, 1572864\n\t"
  "buffer_load_dwordx4 v28, s[24:27], s52 offen offset:0 lds\n\t"
  "v_mfma_f32_16x16x32_bf16 v[48:51], a[212:215], v[228:231], v[48:51]\n\t"
  "v_mfma_f32_16x16x32_bf16 v[64:67], a[212:215], v[232:235], v[64:67]\n\t"
  "ds_read_b128 v[192:195], v16 offset:53248\n\t"
  "v_mfma_f32_16x16x32_bf16 v[80:83], a[212:215], v[236:239], v[80:83]\n\t"
  "ds_read_b128 v[196:199], v16 offset:55296\n\t"
  "ds_read_b128 v[200:203], v16 offset:57344\n\t"
  "v_mfma_f32_16x16x32_bf16 v[96:99], a[212:215], v[240:243], v[96:99]\n\t"
  "buffer_load_dwordx4 v29, s[24:27], s52 offen offset:1024 lds\n\t"
  "v_mfma_f32_16x16x32_bf16 v[112:115], a[216:219], v[224:227], v[112:115]\n\t"
  "v_mfma_f32_16x16x32_bf16 v[128:131], a[216:219], v[228:231], v[128:131]\n\t"
  "ds_read_b128 v[204:207], v16 offset:59392\n\t"
  "buffer_load_dwordx4 v30, s[24:27], s52 offen offset:2048 lds\n\t"
  "v_mfma_f32_16x16x32_bf16 v[144:147], a[216:219], v[232:235], v[144:147]\n\t"
  "v_mfma_f32_16x16x32_bf16 v[160:163], a[216:219], v[236:239], v[160:163]\n\t"
  "ds_read_b128 v[208:211], v16 offset:61440\n\t"
  "ds_read_b64_tr_b16 a[180:181], v21 offset:0\n\t"
  "v_mfma_f32_16x16x32_bf16 v[176:179], a[216:219], v[240:243], v[176:179]\n\t"
  "buffer_load_dwordx4 v31, s[24:27], s52 offen offset:3072 lds\n\t"
  "v_mfma_f32_16x16x32_bf16 v[36:39], a[220:223], v[224:227], v[36:39]\n\t"
  "v_mfma_f32_16x16x32_bf16 v[52:55], a[220:223], v[228:231], v[52:55]\n\t"
  "ds_read_b64_tr_b16 a[182:183], v21 offset:4096\n\t"
  "s_add_u32 m0, s44, 32768\n\t"
  "s_mov_b32 s52, 384\n\t"
  "buffer_load_dwordx4 v23, s[20:23], s52 offen offset:0 lds\n\t"
  "v_mfma_f32_16x16x32_bf16 v[68:71], a[220:223], v[232:235], v[68:71]\n\t"
  "v_mfma_f32_16x16x32_bf16 v[84:87], a[220:223], v[236:239], v[84:87]\n\t"
  "v_mfma_f32_16x16x32_bf16 v[100:103], a[220:223], v[240:243], v[100:103]\n\t"
  "ds_read_b64_tr_b16 a[184:185], v21 offset:8\n\t"
  "ds_read_b64_tr_b16 a[186:187], v21 offset:4104\n\t"
  "v_mfma_f32_16x16x32_bf16 v[116:119], a[224:227], v[224:227], v[116:119]\n\t"
  "ds_read_b64_tr_b16 a[188:189], v21 offset:512\n\t"
  "buffer_load_dwordx4 v24, s[20:23], s52 offen offset:1024 lds\n\t"
  "v_mfma_f32_16x16x32_bf16 v[132:135], a[224:227], v[228:231], v[132:135]\n\t"
  "v_mfma_f32_16x16x32_bf16 v[148:151], a[224:227], v[232:235], v[148:151]\n\t"
  "ds_read_b64_tr_b16 a[190:191], v21 offset:4608\n\t"
  "v_mfma_f32_16x16x32_bf16 v[164:167], a[224:227], v[236:239], v[164:167]\n\t"
  "buffer_load_dwordx4 v25, s[20:23], s52 offen offset:2048 lds\n\t"
  "v_mfma_f32_16x16x32_bf16 v[180:183], a[224:227], v[240:243], v[180:183]\n\t"
  "ds_read_b64_tr_b16 a[192:193], v21 offset:520\n\t"
  "v_mfma_f32_16x16x32_bf16 v[40:43], a[228:231], v[224:227], v[40:43]\n\t"
  "ds_read_b64_tr_b16 a[194:195], v21 offset:4616\n\t"
  "buffer_load_dwordx4 v26, s[20:23], s52 offen offset:3072 lds\n\t"
  "v_mfma_f32_16x16x32_bf16 v[56:59], a[228:231], v[228:231], v[56:59]\n\t"
  "v_mfma_f32_16x16x32_bf16 v[72:75], a[228:231], v[232:235], v[72:75]\n\t"
  "ds_read_b64_tr_b16 a[196:197], v21 offset:1024\n\t"
  "v_mfma_f32_16x16x32_bf16 v[88:91], a[228:231], v[236:239], v[88:91]\n\t"
  "s_add_u32 m0, s45, 49152\n\t"
  "s_nop 0\n\t"
  "buffer_load_dwordx4 v27, s[20:23], s52 offen offset:0 lds\n\t"
  "v_mfma_f32_16x16x32_bf16 v[104:107], a[228:231], v[240:243], v[104:107]\n\t"
  "v_mfma_f32_16x16x32_bf16 v[120:123], a[232:235], v[224:227], v[120:123]\n\t"
  "ds_read_b64_tr_b16 a[198:199], v21 offset:5120\n\t"
  "v_mfma_f32_16x16x32_bf16 v[136:139], a[232:235], v[228:231], v[136:139]\n\t"
  "ds_read_b64_tr_b16 a[200:201], v21 offset:1032\n\t"
  "s_add_u32 m0, s44, 16384\n\t"
  "s_mov_b32 s52, 1835008\n\t"
  "buffer_load_dwordx4 v28, s[24:27], s52 offen offset:0 lds\n\t"
  "v_mfma_f32_16x16x32_bf16 v[152:155], a[232:235], v[232:235], v[152:155]\n\t"
  "v_mfma_f32_16x16x32_bf16 v[168:171], a[232:235], v[236:239], v[168:171]\n\t"
  "v_mfma_f32_16x16x32_bf16 v[184:187], a[232:235], v[240:243], v[184:187]\n\t"
  "ds_read_b64_tr_b16 a[202:203], v21 offset:5128\n\t"
  "v_mfma_f32_16x16x32_bf16 v[44:47], a[236:239], v[224:227], v[44:47]\n\t"
  "ds_read_b64_tr_b16 a[204:205], v21 offset:1536\n\t"
  "ds_read_b64_tr_b16 a[206:207], v21 offset:5632\n\t"
  "v_mfma_f32_16x16x32_bf16 v[60:63], a[236:239], v[228:231], v[60:63]\n\t"
  "buffer_load_dwordx4 v29, s[24:27], s52 offen offset:1024 lds\n\t"
  "v_mfma_f32_16x16x32_bf16 v[76:79], a[236:239], v[232:235], v[76:79]\n\t"
  "ds_read_b64_tr_b16 a[208:209], v21 offset:1544\n\t"
  "v_mfma_f32_16x16x32_bf16 v[92:95], a[236:239], v[236:239], v[92:95]\n\t"
  "buffer_load_dwordx4 v30, s[24:27], s52 offen offset:2048 lds\n\t"
  "v_mfma_f32_16x16x32_bf16 v[108:111], a[236:239], v[240:243], v[108:111]\n\t"
  "v_mfma_f32_16x16x32_bf16 v[124:127], a[240:243], v[224:227], v[124:127]\n\t"
  "ds_read_b64_tr_b16 a[210:211], v21 offset:5640\n\t"
  "buffer_load_dwordx4 v31, s[24:27], s52 offen offset:3072 lds\n\t"
  "v_mfma_f32_16x16x32_bf16 v[140:143], a[240:243], v[228:231], v[140:143]\n\t"
  "v_mfma_f32_16x16x32_bf16 v[156:159], a[240:243], v[232:235], v[156:159]\n\t"
  "v_mfma_f32_16x16x32_bf16 v[172:175], a[240:243], v[236:239], v[172:175]\n\t"
  "v_mfma_f32_16x16x32_bf16 v[188:191], a[240:243], v[240:243], v[188:191]\n\t"
  "s_waitcnt lgkmcnt(0)\n\t"
  "v_mfma_f32_16x16x32_bf16 v[32:35], a[180:183], v[192:195], v[32:35]\n\t"
  "global_load_dword v251, v[252:253], off\n\t"
  "v_lshl_add_u64 v[252:253], v[4:5], 0, v[252:253]\n\t"
  "v_mfma_f32_16x16x32_bf16 v[48:51], a[180:183], v[196:199], v[48:51]\n\t"
  "v_mfma_f32_16x16x32_bf16 v[64:67], a[180:183], v[200:203], v[64:67]\n\t"
  "ds_read_b128 v[224:227], v17 offset:53248\n\t"
  "ds_read_b128 v[228:231], v17 offset:55296\n\t"
  "v_mfma_f32_16x16x32_bf16 v[80:83], a[180:183], v[204:207], v[80:83]\n\t"
  "ds_read_b128 v[232:235], v17 offset:57344\n\t"
  "v_mfma_f32_16x16x32_bf16 v[96:99], a[180:183], v[208:211], v[96:99]\n\t"
  "v_mfma_f32_16x16x32_bf16 v[112:115], a[184:187], v[192:195], v[112:115]\n\t"
  "ds_read_b128 v[236:239], v17 offset:59392\n\t"
  "v_mfma_f32_16x16x32_bf16 v[128:131], a[184:187], v[196:199], v[128:131]\n\t"
  "ds_read_b128 v[240:243], v17 offset:61440\n\t"
  "v_mfma_f32_16x16x32_bf16 v[144:147], a[184:187], v[200:203], v[144:147]\n\t"
  "v_mfma_f32_16x16x32_bf16 v[160:163], a[184:187], v[204:207], v[160:163]\n\t"
  "ds_read_b64_tr_b16 a[212:213], v21 offset:16384\n\t"
  "v_mfma_f32_16x16x32_bf16 v[176:179], a[184:187], v[208:211], v[176:179]\n\t"
  "v_mfma_f32_16x16x32_bf16 v[36:39], a[188:191], v[192:195], v[36:39]\n\t"
  "ds_read_b64_tr_b16 a[214:215], v21 offset:20480\n\t"
  "v_mfma_f32_16x16x32_bf16 v[52:55], a[188:191], v[196:199], v[52:55]\n\t"
  "ds_read_b64_tr_b16 a[216:217], v21 offset:16392\n\t"
  "v_mfma_f32_16x16x32_bf16 v[68:71], a[188:191], v[200:203], v[68:71]\n\t"
  "v_mfma_f32_16x16x32_bf16 v[84:87], a[188:191], v[204:207], v[84:87]\n\t"
  "ds_read_b64_tr_b16 a[218:219], v21 offset:20488\n\t"
  "v_mfma_f32_16x16x32_bf16 v[100:103], a[188:191], v[208:211], v[100:103]\n\t"
  "ds_read_b64_tr_b16 a[220:221], v21 offset:16896\n\t"
  "v_mfma_f32_16x16x32_bf16 v[116:119], a[192:195], v[192:195], v[116:119]\n\t"
  "v_mfma_f32_16x16x32_bf16 v[132:135], a[192:195], v[196:199], v[132:135]\n\t"
  "ds_read_b64_tr_b16 a[222:223], v21 offset:20992\n\t"
  "v_mfma_f32_16x16x32_bf16 v[148:151], a[192:195], v[200:203], v[148:151]\n\t"
  "ds_read_b64_tr_b16 a[224:225], v21 offset:16904\n\t"
  "v_mfma_f32_16x16x32_bf16 v[164:167], a[192:195], v[204:207], v[164:167]\n\t"
  "v_mfma_f32_16x16x32_bf16 v[180:183], a[192:195], v[208:211], v[180:183]\n\t"
  "ds_read_b64_tr_b16 a[226:227], v21 offset:21000\n\t"
  "v_mfma_f32_16x16x32_bf16 v[40:43], a[196:199], v[192:195], v[40:43]\n\t"
  "ds_read_b64_tr_b16 a[228:229], v21 offset:17408\n\t"
  "v_mfma_f32_16x16x32_bf16 v[56:59], a[196:199], v[196:199], v[56:59]\n\t"
  "v_mfma_f32_16x16x32_bf16 v[72:75], a[196:199], v[200:203], v[72:75]\n\t"
  "ds_read_b64_tr_b16 a[230:231], v21 offset:21504\n\t"
  "v_mfma_f32_16x16x32_bf16 v[88:91], a[196:199], v[204:207], v[88:91]\n\t"
  "ds_read_b64_tr_b16 a[232:233], v21 offset:17416\n\t"
  "v_mfma_f32_16x16x32_bf16 v[104:107], a[196:199], v[208:211], v[104:107]\n\t"
  "v_mfma_f32_16x16x32_bf16 v[120:123], a[200:203], v[192:195], v[120:123]\n\t"
  "ds_read_b64_tr_b16 a[234:235], v21 offset:21512\n\t"
  "v_mfma_f32_16x16x32_bf16 v[136:139], a[200:203], v[196:199], v[136:139]\n\t"
  "v_mfma_f32_16x16x32_bf16 v[152:155], a[200:203], v[200:203], v[152:155]\n\t"
  "ds_read_b64_tr_b16 a[236:237], v21 offset:17920\n\t"
  "v_mfma_f32_16x16x32_bf16 v[168:171], a[200:203], v[204:207], v[168:171]\n\t"
  "ds_read_b64_tr_b16 a[238:239], v21 offset:22016\n\t"
  "v_mfma_f32_16x16x32_bf16 v[184:187], a[200:203], v[208:211], v[184:187]\n\t"
  "v_mfma_f32_16x16x32_bf16 v[44:47], a[204:207], v[192:195], v[44:47]\n\t"
  "ds_read_b64_tr_b16 a[240:241], v21 offset:17928\n\t"
  "v_mfma_f32_16x16x32_bf16 v[60:63], a[204:207], v[196:199], v[60:63]\n\t"
  "ds_read_b64_tr_b16 a[242:243], v21 offset:22024\n\t"
  "v_mfma_f32_16x16x32_bf16 v[76:79], a[204:207], v[200:203], v[76:79]\n\t"
  "v_mfma_f32_16x16x32_bf16 v[92:95], a[204:207], v[204:207], v[92:95]\n\t"
  "v_mfma_f32_16x16x32_bf16 v[108:111], a[204:207], v[208:211], v[108:111]\n\t"
  "v_mfma_f32_16x16x32_bf16 v[124:127], a[208:211], v[192:195], v[124:127]\n\t"
  "v_mfma_f32_16x16x32_bf16 v[140:143], a[208:211], v[196:199], v[140:143]\n\t"
  "v_mfma_f32_16x16x32_bf16 v[156:159], a[208:211], v[200:203], v[156:159]\n\t"
  "v_mfma_f32_16x16x32_bf16 v[172:175], a[208:211], v[204:207], v[172:175]\n\t"
  "v_mfma_f32_16x16x32_bf16 v[188:191], a[208:211], v[208:211], v[188:191]\n\t"
  "s_waitcnt vmcnt(16) lgkmcnt(0)\n\t"
  "s_barrier\n\t"
  "v_mfma_f32_16x16x32_bf16 v[32:35], a[212:215], v[224:227], v[32:35]\n\t"
  "s_add_u32 m0, s44, 53248\n\t"
  "s_mov_b32 s52, 2097152\n\t"
  "buffer_load_dwordx4 v28, s[24:27], s52 offen offset:0 lds\n\t"
  "v_mfma_f32_16x16x32_bf16 v[48:51], a[212:215], v[228:231], v[48:51]\n\t"
  "v_mfma_f32_16x16x32_bf16 v[64:67], a[212:215], v[232:235], v[64:67]\n\t"
  "ds_read_b128 v[192:195], v18 offset:0\n\t"
  "v_mfma_f32_16x16x32_bf16 v[80:83], a[212:215], v[236:239], v[80:83]\n\t"
  "ds_read_b128 v[196:199], v18 offset:2048\n\t"
  "ds_read_b128 v[200:203], v18 offset:4096\n\t"
  "v_mfma_f32_16x16x32_bf16 v[96:99], a[212:215], v[240:243], v[96:99]\n\t"
  "buffer_load_dwordx4 v29, s[24:27], s52 offen offset:1024 lds\n\t"
  "v_mfma_f32_16x16x32_bf16 v[112:115], a[216:219], v[224:227], v[112:115]\n\t"
  "v_mfma_f32_16x16x32_bf16 v[128:131], a[216:219], v[228:231], v[128:131]\n\t"
  "ds_read_b128 v[204:207], v18 offset:6144\n\t"
  "buffer_load_dwordx4 v30, s[24:27], s52 offen offset:2048 lds\n\t"
  "v_mfma_f32_16x16x32_bf16 v[144:147], a[216:219], v[232:235], v[144:147]\n\t"
  "v_mfma_f32_16x16x32_bf16 v[160:163], a[216:219], v[236:239], v[160:163]\n\t"
  "ds_read_b128 v[208:211], v18 offset:8192\n\t"
  "ds_read_b64_tr_b16 a[180:181], v22 offset:0\n\t"
  "v_mfma_f32_16x16x32_bf16 v[176:179], a[216:219], v[240:243], v[176:179]\n\t"
  "buffer_load_dwordx4 v31, s[24:27], s52 offen offset:3072 lds\n\t"
  "v_mfma_f32_16x16x32_bf16 v[36:39], a[220:223], v[224:227], v[36:39]\n\t"
  "v_mfma_f32_16x16x32_bf16 v[52:55], a[220:223], v[228:231], v[52:55]\n\t"
  "ds_read_b64_tr_b16 a[182:183], v22 offset:4096\n\t"
  "s_add_u32 m0, s44, 86016\n\t"
  "s_mov_b32 s52, 512\n\t"
  "buffer_load_dwordx4 v23, s[20:23], s52 offen offset:0 lds\n\t"
  "v_mfma_f32_16x16x32_bf16 v[68:71], a[220:223], v[232:235], v[68:71]\n\t"
  "v_mfma_f32_16x16x32_bf16 v[84:87], a[220:223], v[236:239], v[84:87]\n\t"
  "v_mfma_f32_16x16x32_bf16 v[100:103], a[220:223], v[240:243], v[100:103]\n\t"
  "ds_read_b64_tr_b16 a[184:185], v22 offset:8\n\t"
  "ds_read_b64_tr_b16 a[186:187], v22 offset:4104\n\t"
  "v_mfma_f32_16x16x32_bf16 v[116:119], a[224:227], v[224:227], v[116:119]\n\t"
  "ds_read_b64_tr_b16 a[188:189], v22 offset:512\n\t"
  "buffer_load_dwordx4 v24, s[20:23], s52 offen offset:1024 lds\n\t"
  "v_mfma_f32_16x16x32_bf16 v[132:135], a[224:227], v[228:231], v[132:135]\n\t"
  "v_mfma_f32_16x16x32_bf16 v[148:151], a[224:227], v[232:235], v[148:151]\n\t"
  "ds_read_b64_tr_b16 a[190:191], v22 offset:4608\n\t"
  "v_mfma_f32_16x16x32_bf16 v[164:167], a[224:227], v[236:239], v[164:167]\n\t"
  "buffer_load_dwordx4 v25, s[20:23], s52 offen offset:2048 lds\n\t"
  "v_mfma_f32_16x16x32_bf16 v[180:183], a[224:227], v[240:243], v[180:183]\n\t"
  "ds_read_b64_tr_b16 a[192:193], v22 offset:520\n\t"
  "v_mfma_f32_16x16x32_bf16 v[40:43], a[228:231], v[224:227], v[40:43]\n\t"
  "ds_read_b64_tr_b16 a[194:195], v22 offset:4616\n\t"
  "buffer_load_dwordx4 v26, s[20:23], s52 offen offset:3072 lds\n\t"
  "v_mfma_f32_16x16x32_bf16 v[56:59], a[228:231], v[228:231], v[56:59]\n\t"
  "v_mfma_f32_16x16x32_bf16 v[72:75], a[228:231], v[232:235], v[72:75]\n\t"
  "ds_read_b64_tr_b16 a[196:197], v22 offset:1024\n\t"
  "v_mfma_f32_16x16x32_bf16 v[88:91], a[228:231], v[236:239], v[88:91]\n\t"
  "s_add_u32 m0, s45, 102400\n\t"
  "s_nop 0\n\t"
  "buffer_load_dwordx4 v27, s[20:23], s52 offen offset:0 lds\n\t"
  "v_mfma_f32_16x16x32_bf16 v[104:107], a[228:231], v[240:243], v[104:107]\n\t"
  "v_mfma_f32_16x16x32_bf16 v[120:123], a[232:235], v[224:227], v[120:123]\n\t"
  "ds_read_b64_tr_b16 a[198:199], v22 offset:5120\n\t"
  "v_mfma_f32_16x16x32_bf16 v[136:139], a[232:235], v[228:231], v[136:139]\n\t"
  "ds_read_b64_tr_b16 a[200:201], v22 offset:1032\n\t"
  "s_add_u32 m0, s44, 69632\n\t"
  "s_mov_b32 s52, 2359296\n\t"
  "buffer_load_dwordx4 v28, s[24:27], s52 offen offset:0 lds\n\t"
  "v_mfma_f32_16x16x32_bf16 v[152:155], a[232:235], v[232:235], v[152:155]\n\t"
  "v_mfma_f32_16x16x32_bf16 v[168:171], a[232:235], v[236:239], v[168:171]\n\t"
  "v_mfma_f32_16x16x32_bf16 v[184:187], a[232:235], v[240:243], v[184:187]\n\t"
  "ds_read_b64_tr_b16 a[202:203], v22 offset:5128\n\t"
  "v_mfma_f32_16x16x32_bf16 v[44:47], a[236:239], v[224:227], v[44:47]\n\t"
  "ds_read_b64_tr_b16 a[204:205], v22 offset:1536\n\t"
  "ds_read_b64_tr_b16 a[206:207], v22 offset:5632\n\t"
  "v_mfma_f32_16x16x32_bf16 v[60:63], a[236:239], v[228:231], v[60:63]\n\t"
  "buffer_load_dwordx4 v29, s[24:27], s52 offen offset:1024 lds\n\t"
  "v_mfma_f32_16x16x32_bf16 v[76:79], a[236:239], v[232:235], v[76:79]\n\t"
  "ds_read_b64_tr_b16 a[208:209], v22 offset:1544\n\t"
  "v_mfma_f32_16x16x32_bf16 v[92:95], a[236:239], v[236:239], v[92:95]\n\t"
  "buffer_load_dwordx4 v30, s[24:27], s52 offen offset:2048 lds\n\t"
  "v_mfma_f32_16x16x32_bf16 v[108:111], a[236:239], v[240:243], v[108:111]\n\t"
  "v_mfma_f32_16x16x32_bf16 v[124:127], a[240:243], v[224:227], v[124:127]\n\t"
  "ds_read_b64_tr_b16 a[210:211], v22 offset:5640\n\t"
  "buffer_load_dwordx4 v31, s[24:27], s52 offen offset:3072 lds\n\t"
  "v_mfma_f32_16x16x32_bf16 v[140:143], a[240:243], v[228:231], v[140:143]\n\t"
  "v_mfma_f32_16x16x32_bf16 v[156:159], a[240:243], v[232:235], v[156:159]\n\t"
  "v_mfma_f32_16x16x32_bf16 v[172:175], a[240:243], v[236:239], v[172:175]\n\t"
  "v_mfma_f32_16x16x32_bf16 v[188:191], a[240:243], v[240:243], v[188:191]\n\t"
  "s_waitcnt lgkmcnt(0)\n\t"
  "v_mfma_f32_16x16x32_bf16 v[32:35], a[180:183], v[192:195], v[32:35]\n\t"
  "global_load_dword v251, v[252:253], off\n\t"
  "v_lshl_add_u64 v[252:253], v[4:5], 0, v[252:253]\n\t"
  "v_mfma_f32_16x16x32_bf16 v[48:51], a[180:183], v[196:199], v[48:51]\n\t"
  "v_mfma_f32_16x16x32_bf16 v[64:67], a[180:183], v[200:203], v[64:67]\n\t"
  "ds_read_b128 v[224:227], v19 offset:0\n\t"
  "ds_read_b128 v[228:231], v19 offset:2048\n\t"
  "v_mfma_f32_16x16x32_bf16 v[80:83], a[180:183], v[204:207], v[80:83]\n\t"
  "ds_read_b128 v[232:235], v19 offset:4096\n\t"
  "v_mfma_f32_16x16x32_bf16 v[96:99], a[180:183], v[208:211], v[96:99]\n\t"
  "v_mfma_f32_16x16x32_bf16 v[112:115], a[184:187], v[192:195], v[112:115]\n\t"
  "ds_read_b128 v[236:239], v19 offset:6144\n\t"
  "v_mfma_f32_16x16x32_bf16 v[128:131], a[184:187], v[196:199], v[128:131]\n\t"
  "ds_read_b128 v[240:243], v19 offset:8192\n\t"
  "v_mfma_f32_16x16x32_bf16 v[144:147], a[184:187], v[200:203], v[144:147]\n\t"
  "v_mfma_f32_16x16x32_bf16 v[160:163], a[184:187], v[204:207], v[160:163]\n\t"
  "ds_read_b64_tr_b16 a[212:213], v22 offset:16384\n\t"
  "v_mfma_f32_16x16x32_bf16 v[176:179], a[184:187], v[208:211], v[176:179]\n\t"
  "v_mfma_f32_16x16x32_bf16 v[36:39], a[188:191], v[192:195], v[36:39]\n\t"
  "ds_read_b64_tr_b16 a[214:215], v22 offset:20480\n\t"
  "v_mfma_f32_16x16x32_bf16 v[52:55], a[188:191], v[196:199], v[52:55]\n\t"
  "ds_read_b64_tr_b16 a[216:217], v22 offset:16392\n\t"
  "v_mfma_f32_16x16x32_bf16 v[68:71], a[188:191], v[200:203], v[68:71]\n\t"
  "v_mfma_f32_16x16x32_bf16 v[84:87], a[188:191], v[204:207], v[84:87]\n\t"
  "ds_read_b64_tr_b16 a[218:219], v22 offset:20488\n\t"
  "v_mfma_f32_16x16x32_bf16 v[100:103], a[188:191], v[208:211], v[100:103]\n\t"
  "ds_read_b64_tr_b16 a[220:221], v22 offset:16896\n\t"
  "v_mfma_f32_16x16x32_bf16 v[116:119], a[192:195], v[192:195], v[116:119]\n\t"
  "v_mfma_f32_16x16x32_bf16 v[132:135], a[192:195], v[196:199], v[132:135]\n\t"
  "ds_read_b64_tr_b16 a[222:223], v22 offset:20992\n\t"
  "v_mfma_f32_16x16x32_bf16 v[148:151], a[192:195], v[200:203], v[148:151]\n\t"
  "ds_read_b64_tr_b16 a[224:225], v22 offset:16904\n\t"
  "v_mfma_f32_16x16x32_bf16 v[164:167], a[192:195], v[204:207], v[164:167]\n\t"
  "v_mfma_f32_16x16x32_bf16 v[180:183], a[192:195], v[208:211], v[180:183]\n\t"
  "ds_read_b64_tr_b16 a[226:227], v22 offset:21000\n\t"
  "v_mfma_f32_16x16x32_bf16 v[40:43], a[196:199], v[192:195], v[40:43]\n\t"
  "ds_read_b64_tr_b16 a[228:229], v22 offset:17408\n\t"
  "v_mfma_f32_16x16x32_bf16 v[56:59], a[196:199], v[196:199], v[56:59]\n\t"
  "v_mfma_f32_16x16x32_bf16 v[72:75], a[196:199], v[200:203], v[72:75]\n\t"
  "ds_read_b64_tr_b16 a[230:231], v22 offset:21504\n\t"
  "v_mfma_f32_16x16x32_bf16 v[88:91], a[196:199], v[204:207], v[88:91]\n\t"
  "ds_read_b64_tr_b16 a[232:233], v22 offset:17416\n\t"
  "v_mfma_f32_16x16x32_bf16 v[104:107], a[196:199], v[208:211], v[104:107]\n\t"
  "v_mfma_f32_16x16x32_bf16 v[120:123], a[200:203], v[192:195], v[120:123]\n\t"
  "ds_read_b64_tr_b16 a[234:235], v22 offset:21512\n\t"
  "v_mfma_f32_16x16x32_bf16 v[136:139], a[200:203], v[196:199], v[136:139]\n\t"
  "v_mfma_f32_16x16x32_bf16 v[152:155], a[200:203], v[200:203], v[152:155]\n\t"
  "ds_read_b64_tr_b16 a[236:237], v22 offset:17920\n\t"
  "v_mfma_f32_16x16x32_bf16 v[168:171], a[200:203], v[204:207], v[168:171]\n\t"
  "ds_read_b64_tr_b16 a[238:239], v22 offset:22016\n\t"
  "v_mfma_f32_16x16x32_bf16 v[184:187], a[200:203], v[208:211], v[184:187]\n\t"
  "v_mfma_f32_16x16x32_bf16 v[44:47], a[204:207], v[192:195], v[44:47]\n\t"
  "ds_read_b64_tr_b16 a[240:241], v22 offset:17928\n\t"
  "v_mfma_f32_16x16x32_bf16 v[60:63], a[204:207], v[196:199], v[60:63]\n\t"
  "ds_read_b64_tr_b16 a[242:243], v22 offset:22024\n\t"
  "v_mfma_f32_16x16x32_bf16 v[76:79], a[204:207], v[200:203], v[76:79]\n\t"
  "v_mfma_f32_16x16x32_bf16 v[92:95], a[204:207], v[204:207], v[92:95]\n\t"
  "v_mfma_f32_16x16x32_bf16 v[108:111], a[204:207], v[208:211], v[108:111]\n\t"
  "v_mfma_f32_16x16x32_bf16 v[124:127], a[208:211], v[192:195], v[124:127]\n\t"
  "v_mfma_f32_16x16x32_bf16 v[140:143], a[208:211], v[196:199], v[140:143]\n\t"
  "v_mfma_f32_16x16x32_bf16 v[156:159], a[208:211], v[200:203], v[156:159]\n\t"
  "v_mfma_f32_16x16x32_bf16 v[172:175], a[208:211], v[204:207], v[172:175]\n\t"
  "v_mfma_f32_16x16x32_bf16 v[188:191], a[208:211], v[208:211], v[188:191]\n\t"
  "s_waitcnt vmcnt(15) lgkmcnt(0)\n\t"
  "s_barrier\n\t"
  "v_mfma_f32_16x16x32_bf16 v[32:35], a[212:215], v[224:227], v[32:35]\n\t"
  "s_add_u32 m0, s44, 106496\n\t"
  "s_mov_b32 s52, 2621440\n\t"
  "buffer_load_dwordx4 v28, s[24:27], s52 offen offset:0 lds\n\t"
  "v_mfma_f32_16x16x32_bf16 v[48:51], a[212:215], v[228:231], v[48:51]\n\t"
  "v_mfma_f32_16x16x32_bf16 v[64:67], a[212:215], v[232:235], v[64:67]\n\t"
  "ds_read_b128 v[192:195], v16 offset:0\n\t"
  "v_mfma_f32_16x16x32_bf16 v[80:83], a[212:215], v[236:239], v[80:83]\n\t"
  "s_waitcnt vmcnt(44)\n\t"
  "v_add_u32 v254, 1, v254\n\t"
  "s_nop 0\n\t"
  "v_readfirstlane_b32 %1, v254\n\t"
  "global_store_dword v255, v254, s[74:75] sc1\n\t"
  "v_mfma_f32_16x16x32_bf16 v[96:99], a[212:215], v[240:243], v[96:99]\n\t"
  "v_mfma_f32_16x16x32_bf16 v[112:115], a[216:219], v[224:227], v[112:115]\n\t"
  "v_mfma_f32_16x16x32_bf16 v[128:131], a[216:219], v[228:231], v[128:131]\n\t"
  "ds_read_b128 v[196:199], v16 offset:2048\n\t"
  "v_mfma_f32_16x16x32_bf16 v[144:147], a[216:219], v[232:235], v[144:147]\n\t"
  "ds_read_b128 v[200:203], v16 offset:4096\n\t"
  "buffer_load_dwordx4 v29, s[24:27], s52 offen offset:1024 lds\n\t"
  "v_mfma_f32_16x16x32_bf16 v[160:163], a[216:219], v[236:239], v[160:163]\n\t"
  "v_mfma_f32_16x16x32_bf16 v[176:179], a[216:219], v[240:243], v[176:179]\n\t"
  "ds_read_b128 v[204:207], v16 offset:6144\n\t"
  "buffer_load_dwordx4 v30, s[24:27], s52 offen offset:2048 lds\n\t"
  "v_mfma_f32_16x16x32_bf16 v[36:39], a[220:223], v[224:227], v[36:39]\n\t"
  "v_mfma_f32_16x16x32_bf16 v[52:55], a[220:223], v[228:231], v[52:55]\n\t"
  "ds_read_b128 v[208:211], v16 offset:8192\n\t"
  "v_mfma_f32_16x16x32_bf16 v[68:71], a[220:223], v[232:235], v[68:71]\n\t"
  "ds_read_b64_tr_b16 a[180:181], v20 offset:0\n\t"
  "buffer_load_dwordx4 v31, s[24:27], s52 offen offset:3072 lds\n\t"
  "v_mfma_f32_16x16x32_bf16 v[84:87], a[220:223], v[236:239], v[84:87]\n\t"
  "v_mfma_f32_16x16x32_bf16 v[100:103], a[220:223], v[240:243], v[100:103]\n\t"
  "ds_read_b64_tr_b16 a[182:183], v20 offset:4096\n\t"
  "s_add_u32 m0, s44, 139264\n\t"
  "s_mov_b32 s52, 640\n\t"
  "buffer_load_dwordx4 v23, s[20:23], s52 offen offset:0 lds\n\t"
  "v_mfma_f32_16x16x32_bf16 v[116:119], a[224:227], v[224:227], v[116:119]\n\t"
  "v_mfma_f32_16x16x32_bf16 v[132:135], a[224:227], v[228:231], v[132:135]\n\t"
  "v_mfma_f32_16x16x32_bf16 v[148:151], a[224:227], v[232:235], v[148:151]\n\t"
  "ds_read_b64_tr_b16 a[184:185], v20 offset:8\n\t"
  "ds_read_b64_tr_b16 a[186:187], v20 offset:4104\n\t"
  "v_mfma_f32_16x16x32_bf16 v[164:167], a[224:227], v[236:239], v[164:167]\n\t"
  "ds_read_b64_tr_b16 a[188:189], v20 offset:512\n\t"
  "buffer_load_dwordx4 v24, s[20:23], s52 offen offset:1024 lds\n\t"
  "v_mfma_f32_16x16x32_bf16 v[180:183], a[224:227], v[240:243], v[180:183]\n\t"
  "v_mfma_f32_16x16x32_bf16 v[40:43], a[228:231], v[224:227], v[40:43]\n\t"
  "ds_read_b64_tr_b16 a[190:191], v20 offset:4608\n\t"
  "buffer_load_dwordx4 v25, s[20:23], s52 offen offset:2048 lds\n\t"
  "v_mfma_f32_16x16x32_bf16 v[56:59], a[228:231], v[228:231], v[56:59]\n\t"
  "v_mfma_f32_16x16x32_bf16 v[72:75], a[228:231], v[232:235], v[72:75]\n\t"
  "ds_read_b64_tr_b16 a[192:193], v20 offset:520\n\t"
  "v_mfma_f32_16x16x32_bf16 v[88:91], a[228:231], v[236:239], v[88:91]\n\t"
  "ds_read_b64_tr_b16 a[194:195], v20 offset:4616\n\t"
  "buffer_load_dwordx4 v26, s[20:23], s52 offen offset:3072 lds\n\t"
  "v_mfma_f32_16x16x32_bf16 v[104:107], a[228:231], v[240:243], v[104:107]\n\t"
  "v_mfma_f32_16x16x32_bf16 v[120:123], a[232:235], v[224:227], v[120:123]\n\t"
  "ds_read_b64_tr_b16 a[196:197], v20 offset:1024\n\t"
  "s_add_u32 m0, s45, 155648\n\t"
  "s_nop 0\n\t"
  "buffer_load_dwordx4 v27, s[20:23], s52 offen offset:0 lds\n\t"
  "v_mfma_f32_16x16x32_bf16 v[136:139], a[232:235], v[228:231], v[136:139]\n\t"
  "v_mfma_f32_16x16x32_bf16 v[152:155], a[232:235], v[232:235], v[152:155]\n\t"
  "v_mfma_f32_16x16x32_bf16 v[168:171], a[232:235], v[236:239], v[168:171]\n\t"
  "ds_read_b64_tr_b16 a[198:199], v20 offset:5120\n\t"
  "ds_read_b64_tr_b16 a[200:201], v20 offset:1032\n\t"
  "v_mfma_f32_16x16x32_bf16 v[184:187], a[232:235], v[240:243], v[184:187]\n\t"
  "s_add_u32 m0, s44, 122880\n\t"
  "s_mov_b32 s52, 2883584\n\t"
  "buffer_load_dwordx4 v28, s[24:27], s52 offen offset:0 lds\n\t"
  "v_mfma_f32_16x16x32_bf16 v[44:47], a[236:239], v[224:227], v[44:47]\n\t"
  "v_mfma_f32_16x16x32_bf16 v[60:63], a[236:239], v[228:231], v[60:63]\n\t"
  "v_mfma_f32_16x16x32_bf16 v[76:79], a[236:239], v[232:235], v[76:79]\n\t"
  "ds_read_b64_tr_b16 a[202:203], v20 offset:5128\n\t"
  "ds_read_b64_tr_b16 a[204:205], v20 offset:1536\n\t"
  "v_mfma_f32_16x16x32_bf16 v[92:95], a[236:239], v[236:239], v[92:95]\n\t"
  "ds_read_b64_tr_b16 a[206:207], v20 offset:5632\n\t"
  "buffer_load_dwordx4 v29, s[24:27], s52 offen offset:1024 lds\n\t"
  "v_mfma_f32_16x16x32_bf16 v[108:111], a[236:239], v[240:243], v[108:111]\n\t"
  "v_mfma_f32_16x16x32_bf16 v[124:127], a[240:243], v[224:227], v[124:127]\n\t"
  "ds_read_b64_tr_b16 a[208:209], v20 offset:1544\n\t"
  "buffer_load_dwordx4 v30, s[24:27], s52 offen offset:2048 lds\n\t"
  "v_mfma_f32_16x16x32_bf16 v[140:143], a[240:243], v[228:231], v[140:143]\n\t"
  "v_mfma_f32_16x16x32_bf16 v[156:159], a[240:243], v[232:235], v[156:159]\n\t"
  "ds_read_b64_tr_b16 a[210:211], v20 offset:5640\n\t"
  "buffer_load_dwordx4 v31, s[24:27], s52 offen offset:3072 lds\n\t"
  "v_mfma_f32_16x16x32_bf16 v[172:175], a[240:243], v[236:239], v[172:175]\n\t"
  "v_mfma_f32_16x16x32_bf16 v[188:191], a[240:243], v[240:243], v[188:191]\n\t"
  "s_waitcnt lgkmcnt(0)\n\t"
  "v_mfma_f32_16x16x32_bf16 v[32:35], a[180:183], v[192:195], v[32:35]\n\t"
  "global_load_dword v251, v[252:253], off\n\t"
  "v_lshl_add_u64 v[252:253], v[4:5], 0, v[252:253]\n\t"
  "v_mfma_f32_16x16x32_bf16 v[48:51], a[180:183], v[196:199], v[48:51]\n\t"
  "v_mfma_f32_16x16x32_bf16 v[64:67], a[180:183], v[200:203], v[64:67]\n\t"
  "ds_read_b128 v[224:227], v17 offset:0\n\t"
  "ds_read_b128 v[228:231], v17 offset:2048\n\t"
  "v_mfma_f32_16x16x32_bf16 v[80:83], a[180:183], v[204:207], v[80:83]\n\t"
  "ds_read_b128 v[232:235], v17 offset:4096\n\t"
  "v_mfma_f32_16x16x32_bf16 v[96:99], a[180:183], v[208:211], v[96:99]\n\t"
  "v_mfma_f32_16x16x32_bf16 v[112:115], a[184:187], v[192:195], v[112:115]\n\t"
  "ds_read_b128 v[236:239], v17 offset:6144\n\t"
  "v_mfma_f32_16x16x32_bf16 v[128:131], a[184:187], v[196:199], v[128:131]\n\t"
  "ds_read_b128 v[240:243], v17 offset:8192\n\t"
  "v_mfma_f32_16x16x32_bf16 v[144:147], a[184:187], v[200:203], v[144:147]\n\t"
  "v_mfma_f32_16x16x32_bf16 v[160:163], a[184:187], v[204:207], v[160:163]\n\t"
  "ds_read_b64_tr_b16 a[212:213], v20 offset:16384\n\t"
  "v_mfma_f32_16x16x32_bf16 v[176:179], a[184:187], v[208:211], v[176:179]\n\t"
  "v_mfma_f32_16x16x32_bf16 v[36:39], a[188:191], v[192:195], v[36:39]\n\t"
  "ds_read_b64_tr_b16 a[214:215], v20 offset:20480\n\t"
  "v_mfma_f32_16x16x32_bf16 v[52:55], a[188:191], v[196:199], v[52:55]\n\t"
  "ds_read_b64_tr_b16 a[216:217], v20 offset:16392\n\t"
  "v_mfma_f32_16x16x32_bf16 v[68:71], a[188:191], v[200:203], v[68:71]\n\t"
  "v_mfma_f32_16x16x32_bf16 v[84:87], a[188:191], v[204:207], v[84:87]\n\t"
  "ds_read_b64_tr_b16 a[218:219], v20 offset:20488\n\t"
  "v_mfma_f32_16x16x32_bf16 v[100:103], a[188:191], v[208:211], v[100:103]\n\t"
  "ds_read_b64_tr_b16 a[220:221], v20 offset:16896\n\t"
  "v_mfma_f32_16x16x32_bf16 v[116:119], a[192:195], v[192:195], v[116:119]\n\t"
  "v_mfma_f32_16x16x32_bf16 v[132:135], a[192:195], v[196:199], v[132:135]\n\t"
  "ds_read_b64_tr_b16 a[222:223], v20 offset:20992\n\t"
  "v_mfma_f32_16x16x32_bf16 v[148:151], a[192:195], v[200:203], v[148:151]\n\t"
  "ds_read_b64_tr_b16 a[224:225], v20 offset:16904\n\t"
  "v_mfma_f32_16x16x32_bf16 v[164:167], a[192:195], v[204:207], v[164:167]\n\t"
  "v_mfma_f32_16x16x32_bf16 v[180:183], a[192:195], v[208:211], v[180:183]\n\t"
  "ds_read_b64_tr_b16 a[226:227], v20 offset:21000\n\t"
  "v_mfma_f32_16x16x32_bf16 v[40:43], a[196:199], v[192:195], v[40:43]\n\t"
  "ds_read_b64_tr_b16 a[228:229], v20 offset:17408\n\t"
  "v_mfma_f32_16x16x32_bf16 v[56:59], a[196:199], v[196:199], v[56:59]\n\t"
  "v_mfma_f32_16x16x32_bf16 v[72:75], a[196:199], v[200:203], v[72:75]\n\t"
  "ds_read_b64_tr_b16 a[230:231], v20 offset:21504\n\t"
  "v_mfma_f32_16x16x32_bf16 v[88:91], a[196:199], v[204:207], v[88:91]\n\t"
  "ds_read_b64_tr_b16 a[232:233], v20 offset:17416\n\t"
  "v_mfma_f32_16x16x32_bf16 v[104:107], a[196:199], v[208:211], v[104:107]\n\t"
  "v_mfma_f32_16x16x32_bf16 v[120:123], a[200:203], v[192:195], v[120:123]\n\t"
  "ds_read_b64_tr_b16 a[234:235], v20 offset:21512\n\t"
  "v_mfma_f32_16x16x32_bf16 v[136:139], a[200:203], v[196:199], v[136:139]\n\t"
  "v_mfma_f32_16x16x32_bf16 v[152:155], a[200:203], v[200:203], v[152:155]\n\t"
  "ds_read_b64_tr_b16 a[236:237], v20 offset:17920\n\t"
  "v_mfma_f32_16x16x32_bf16 v[168:171], a[200:203], v[204:207], v[168:171]\n\t"
  "ds_read_b64_tr_b16 a[238:239], v20 offset:22016\n\t"
  "v_mfma_f32_16x16x32_bf16 v[184:187], a[200:203], v[208:211], v[184:187]\n\t"
  "v_mfma_f32_16x16x32_bf16 v[44:47], a[204:207], v[192:195], v[44:47]\n\t"
  "ds_read_b64_tr_b16 a[240:241], v20 offset:17928\n\t"
  "v_mfma_f32_16x16x32_bf16 v[60:63], a[204:207], v[196:199], v[60:63]\n\t"
  "ds_read_b64_tr_b16 a[242:243], v20 offset:22024\n\t"
  "v_mfma_f32_16x16x32_bf16 v[76:79], a[204:207], v[200:203], v[76:79]\n\t"
  "v_mfma_f32_16x16x32_bf16 v[92:95], a[204:207], v[204:207], v[92:95]\n\t"
  "v_mfma_f32_16x16x32_bf16 v[108:111], a[204:207], v[208:211], v[108:111]\n\t"
  "v_mfma_f32_16x16x32_bf16 v[124:127], a[208:211], v[192:195], v[124:127]\n\t"
  "v_mfma_f32_16x16x32_bf16 v[140:143], a[208:211], v[196:199], v[140:143]\n\t"
  "v_mfma_f32_16x16x32_bf16 v[156:159], a[208:211], v[200:203], v[156:159]\n\t"
  "v_mfma_f32_16x16x32_bf16 v[172:175], a[208:211], v[204:207], v[172:175]\n\t"
  "v_mfma_f32_16x16x32_bf16 v[188:191], a[208:211], v[208:211], v[188:191]\n\t"
  "s_waitcnt vmcnt(16) lgkmcnt(0)\n\t"
  "s_barrier\n\t"
  "v_mfma_f32_16x16x32_bf16 v[32:35], a[212:215], v[224:227], v[32:35]\n\t"
  "s_add_u32 m0, s44, 0\n\t"
  "s_mov_b32 s52, 3145728\n\t"
  "buffer_load_dwordx4 v28, s[24:27], s52 offen offset:0 lds\n\t"
  "v_mfma_f32_16x16x32_bf16 v[48:51], a[212:215], v[228:231], v[48:51]\n\t"
  "v_mfma_f32_16x16x32_bf16 v[64:67], a[212:215], v[232:235], v[64:67]\n\t"
  "ds_read_b128 v[192:195], v16 offset:53248\n\t"
  "v_mfma_f32_16x16x32_bf16 v[80:83], a[212:215], v[236:239], v[80:83]\n\t"
  "ds_read_b128 v[196:199], v16 offset:55296\n\t"
  "ds_read_b128 v[200:203], v16 offset:57344\n\t"
  "v_mfma_f32_16x16x32_bf16 v[96:99], a[212:215], v[240:243], v[96:99]\n\t"
  "buffer_load_dwordx4 v29, s[24:27], s52 offen offset:1024 lds\n\t"
  "v_mfma_f32_16x16x32_bf16 v[112:115], a[216:219], v[224:227], v[112:115]\n\t"
  "v_mfma_f32_16x16x32_bf16 v[128:131], a[216:219], v[228:231], v[128:131]\n\t"
  "ds_read_b128 v[204:207], v16 offset:59392\n\t"
  "buffer_load_dwordx4 v30, s[24:27], s52 offen offset:2048 lds\n\t"
  "v_mfma_f32_16x16x32_bf16 v[144:147], a[216:219], v[232:235], v[144:147]\n\t"
  "v_mfma_f32_16x16x32_bf16 v[160:163], a[216:219], v[236:239], v[160:163]\n\t"
  "ds_read_b128 v[208:211], v16 offset:61440\n\t"
  "ds_read_b64_tr_b16 a[180:181], v21 offset:0\n\t"
  "v_mfma_f32_16x16x32_bf16 v[176:179], a[216:219], v[240:243], v[176:179]\n\t"
  "buffer_load_dwordx4 v31, s[24:27], s52 offen offset:3072 lds\n\t"
  "v_mfma_f32_16x16x32_bf16 v[36:39], a[220:223], v[224:227], v[36:39]\n\t"
  "v_mfma_f32_16x16x32_bf16 v[52:55], a[220:223], v[228:231], v[52:55]\n\t"
  "ds_read_b64_tr_b16 a[182:183], v21 offset:4096\n\t"
  "s_add_u32 m0, s44, 32768\n\t"
  "s_mov_b32 s52, 768\n\t"
  "buffer_load_dwordx4 v23, s[20:23], s52 offen offset:0 lds\n\t"
  "v_mfma_f32_16x16x32_bf16 v[68:71], a[220:223], v[232:235], v[68:71]\n\t"
  "v_mfma_f32_16x16x32_bf16 v[84:87], a[220:223], v[236:239], v[84:87]\n\t"
  "v_mfma_f32_16x16x32_bf16 v[100:103], a[220:223], v[240:243], v[100:103]\n\t"
  "ds_read_b64_tr_b16 a[184:185], v21 offset:8\n\t"
  "ds_read_b64_tr_b16 a[186:187], v21 offset:4104\n\t"
  "v_mfma_f32_16x16x32_bf16 v[116:119], a[224:227], v[224:227], v[116:119]\n\t"
  "ds_read_b64_tr_b16 a[188:189], v21 offset:512\n\t"
  "buffer_load_dwordx4 v24, s[20:23], s52 offen offset:1024 lds\n\t"
  "v_mfma_f32_16x16x32_bf16 v[132:135], a[224:227], v[228:231], v[132:135]\n\t"
  "v_mfma_f32_16x16x32_bf16 v[148:151], a[224:227], v[232:235], v[148:151]\n\t"
  "ds_read_b64_tr_b16 a[190:191], v21 offset:4608\n\t"
  "v_mfma_f32_16x16x32_bf16 v[164:167], a[224:227], v[236:239], v[164:167]\n\t"
  "buffer_load_dwordx4 v25, s[20:23], s52 offen offset:2048 lds\n\t"
  "v_mfma_f32_16x16x32_bf16 v[180:183], a[224:227], v[240:243], v[180:183]\n\t"
  "ds_read_b64_tr_b16 a[192:193], v21 offset:520\n\t"
  "v_mfma_f32_16x16x32_bf16 v[40:43], a[228:231], v[224:227], v[40:43]\n\t"
  "ds_read_b64_tr_b16 a[194:195], v21 offset:4616\n\t"
  "buffer_load_dwordx4 v26, s[20:23], s52 offen offset:3072 lds\n\t"
  "v_mfma_f32_16x16x32_bf16 v[56:59], a[228:231], v[228:231], v[56:59]\n\t"
  "v_mfma_f32_16x16x32_bf16 v[72:75], a[228:231], v[232:235], v[72:75]\n\t"
  "ds_read_b64_tr_b16 a[196:197], v21 offset:1024\n\t"
  "v_mfma_f32_16x16x32_bf16 v[88:91], a[228:231], v[236:239], v[88:91]\n\t"
  "s_add_u32 m0, s45, 49152\n\t"
  "s_nop 0\n\t"
  "buffer_load_dwordx4 v27, s[20:23], s52 offen offset:0 lds\n\t"
  "v_mfma_f32_16x16x32_bf16 v[104:107], a[228:231], v[240:243], v[104:107]\n\t"
  "v_mfma_f32_16x16x32_bf16 v[120:123], a[232:235], v[224:227], v[120:123]\n\t"
  "ds_read_b64_tr_b16 a[198:199], v21 offset:5120\n\t"
  "v_mfma_f32_16x16x32_bf16 v[136:139], a[232:235], v[228:231], v[136:139]\n\t"
  "ds_read_b64_tr_b16 a[200:201], v21 offset:1032\n\t"
  "s_add_u32 m0, s44, 16384\n\t"
  "s_mov_b32 s52, 3407872\n\t"
  "buffer_load_dwordx4 v28, s[24:27], s52 offen offset:0 lds\n\t"
  "v_mfma_f32_16x16x32_bf16 v[152:155], a[232:235], v[232:235], v[152:155]\n\t"
  "v_mfma_f32_16x16x32_bf16 v[168:171], a[232:235], v[236:239], v[168:171]\n\t"
  "v_mfma_f32_16x16x32_bf16 v[184:187], a[232:235], v[240:243], v[184:187]\n\t"
  "ds_read_b64_tr_b16 a[202:203], v21 offset:5128\n\t"
  "v_mfma_f32_16x16x32_bf16 v[44:47], a[236:239], v[224:227], v[44:47]\n\t"
  "ds_read_b64_tr_b16 a[204:205], v21 offset:1536\n\t"
  "ds_read_b64_tr_b16 a[206:207], v21 offset:5632\n\t"
  "v_mfma_f32_16x16x32_bf16 v[60:63], a[236:239], v[228:231], v[60:63]\n\t"
  "buffer_load_dwordx4 v29, s[24:27], s52 offen offset:1024 lds\n\t"
  "v_mfma_f32_16x16x32_bf16 v[76:79], a[236:239], v[232:235], v[76:79]\n\t"
  "ds_read_b64_tr_b16 a[208:209], v21 offset:1544\n\t"
  "v_mfma_f32_16x16x32_bf16 v[92:95], a[236:239], v[236:239], v[92:95]\n\t"
  "buffer_load_dwordx4 v30, s[24:27], s52 offen offset:2048 lds\n\t"
  "v_mfma_f32_16x16x32_bf16 v[108:111], a[236:239], v[240:243], v[108:111]\n\t"
  "v_mfma_f32_16x16x32_bf16 v[124:127], a[240:243], v[224:227], v[124:127]\n\t"
  "ds_read_b64_tr_b16 a[210:211], v21 offset:5640\n\t"
  "buffer_load_dwordx4 v31, s[24:27], s52 offen offset:3072 lds\n\t"
  "v_mfma_f32_16x16x32_bf16 v[140:143], a[240:243], v[228:231], v[140:143]\n\t"
  "v_mfma_f32_16x16x32_bf16 v[156:159], a[240:243], v[232:235], v[156:159]\n\t"
  "v_mfma_f32_16x16x32_bf16 v[172:175], a[240:243], v[236:239], v[172:175]\n\t"
  "v_mfma_f32_16x16x32_bf16 v[188:191], a[240:243], v[240:243], v[188:191]\n\t"
  "s_waitcnt lgkmcnt(0)\n\t"
  "v_mfma_f32_16x16x32_bf16 v[32:35], a[180:183], v[192:195], v[32:35]\n\t"
  "global_load_dword v251, v[252:253], off\n\t"
  "v_lshl_add_u64 v[252:253], v[4:5], 0, v[252:253]\n\t"
  "v_mfma_f32_16x16x32_bf16 v[48:51], a[180:183], v[196:199], v[48:51]\n\t"
  "v_mfma_f32_16x16x32_bf16 v[64:67], a[180:183], v[200:203], v[64:67]\n\t"
  "ds_read_b128 v[224:227], v17 offset:53248\n\t"
  "s_mov_b32 s53, 0\n\t"
  "buffer_load_dwordx4 a[0:3], v212, s[28:31], s53 offen offset:0 nt\n\t"
  "v_mfma_f32_16x16x32_bf16 v[80:83], a[180:183], v[204:207], v[80:83]\n\t"
  "v_mfma_f32_16x16x32_bf16 v[96:99], a[180:183], v[208:211], v[96:99]\n\t"
  "v_mfma_f32_16x16x32_bf16 v[112:115], a[184:187], v[192:195], v[112:115]\n\t"
  "ds_read_b128 v[228:231], v17 offset:55296\n\t"
  "ds_read_b128 v[232:235], v17 offset:57344\n\t"
  "v_mfma_f32_16x16x32_bf16 v[128:131], a[184:187], v[196:199], v[128:131]\n\t"
  "buffer_load_dwordx4 a[4:7], v213, s[28:31], s53 offen offset:1024 nt\n\t"
  "v_mfma_f32_16x16x32_bf16 v[144:147], a[184:187], v[200:203], v[144:147]\n\t"
  "buffer_load_dwordx4 a[8:11], v214, s[28:31], s53 offen offset:2048 nt\n\t"
  "v_mfma_f32_16x16x32_bf16 v[160:163], a[184:187], v[204:207], v[160:163]\n\t"
  "v_mfma_f32_16x16x32_bf16 v[176:179], a[184:187], v[208:211], v[176:179]\n\t"
  "ds_read_b128 v[236:239], v17 offset:59392\n\t"
  "v_mfma_f32_16x16x32_bf16 v[36:39], a[188:191], v[192:195], v[36:39]\n\t"
  "buffer_load_dwordx4 a[12:15], v215, s[28:31], s53 offen offset:3072 nt\n\t"
  "v_mfma_f32_16x16x32_bf16 v[52:55], a[188:191], v[196:199], v[52:55]\n\t"
  "ds_read_b128 v[240:243], v17 offset:61440\n\t"
  "v_mfma_f32_16x16x32_bf16 v[68:71], a[188:191], v[200:203], v[68:71]\n\t"
  "ds_read_b64_tr_b16 a[212:213], v21 offset:16384\n\t"
  "ds_read_b64_tr_b16 a[214:215], v21 offset:20480\n\t"
  "v_mfma_f32_16x16x32_bf16 v[84:87], a[188:191], v[204:207], v[84:87]\n\t"
  "ds_read_b64_tr_b16 a[216:217], v21 offset:16392\n\t"
  "ds_read_b64_tr_b16 a[218:219], v21 offset:20488\n\t"
  "v_mfma_f32_16x16x32_bf16 v[100:103], a[188:191], v[208:211], v[100:103]\n\t"
  "ds_read_b64_tr_b16 a[220:221], v21 offset:16896\n\t"
  "v_mfma_f32_16x16x32_bf16 v[116:119], a[192:195], v[192:195], v[116:119]\n\t"
  "v_mfma_f32_16x16x32_bf16 v[132:135], a[192:195], v[196:199], v[132:135]\n\t"
  "ds_read_b64_tr_b16 a[222:223], v21 offset:20992\n\t"
  "v_mfma_f32_16x16x32_bf16 v[148:151], a[192:195], v[200:203], v[148:151]\n\t"
  "ds_read_b64_tr_b16 a[224:225], v21 offset:16904\n\t"
  "v_mfma_f32_16x16x32_bf16 v[164:167], a[192:195], v[204:207], v[164:167]\n\t"
  "v_mfma_f32_16x16x32_bf16 v[180:183], a[192:195], v[208:211], v[180:183]\n\t"
  "ds_read_b64_tr_b16 a[226:227], v21 offset:21000\n\t"
  "v_mfma_f32_16x16x32_bf16 v[40:43], a[196:199], v[192:195], v[40:43]\n\t"
  "ds_read_b64_tr_b16 a[228:229], v21 offset:17408\n\t"
  "v_mfma_f32_16x16x32_bf16 v[56:59], a[196:199], v[196:199], v[56:59]\n\t"
  "v_mfma_f32_16x16x32_bf16 v[72:75], a[196:199], v[200:203], v[72:75]\n\t"
  "ds_read_b64_tr_b16 a[230:231], v21 offset:21504\n\t"
  "v_mfma_f32_16x16x32_bf16 v[88:91], a[196:199], v[204:207], v[88:91]\n\t"
  "ds_read_b64_tr_b16 a[232:233], v21 offset:17416\n\t"
  "v_mfma_f32_16x16x32_bf16 v[104:107], a[196:199], v[208:211], v[104:107]\n\t"
  "v_mfma_f32_16x16x32_bf16 v[120:123], a[200:203], v[192:195], v[120:123]\n\t"
  "ds_read_b64_tr_b16 a[234:235], v21 offset:21512\n\t"
  "v_mfma_f32_16x16x32_bf16 v[136:139], a[200:203], v[196:199], v[136:139]\n\t"
  "v_mfma_f32_16x16x32_bf16 v[152:155], a[200:203], v[200:203], v[152:155]\n\t"
  "ds_read_b64_tr_b16 a[236:237], v21 offset:17920\n\t"
  "v_mfma_f32_16x16x32_bf16 v[168:171], a[200:203], v[204:207], v[168:171]\n\t"
  "ds_read_b64_tr_b16 a[238:239], v21 offset:22016\n\t"
  "v_mfma_f32_16x16x32_bf16 v[184:187], a[200:203], v[208:211], v[184:187]\n\t"
  "v_mfma_f32_16x16x32_bf16 v[44:47], a[204:207], v[192:195], v[44:47]\n\t"
  "ds_read_b64_tr_b16 a[240:241], v21 offset:17928\n\t"
  "v_mfma_f32_16x16x32_bf16 v[60:63], a[204:207], v[196:199], v[60:63]\n\t"
  "ds_read_b64_tr_b16 a[242:243], v21 offset:22024\n\t"
  "v_mfma_f32_16x16x32_bf16 v[76:79], a[204:207], v[200:203], v[76:79]\n\t"
  "v_mfma_f32_16x16x32_bf16 v[92:95], a[204:207], v[204:207], v[92:95]\n\t"
  "v_mfma_f32_16x16x32_bf16 v[108:111], a[204:207], v[208:211], v[108:111]\n\t"
  "v_mfma_f32_16x16x32_bf16 v[124:127], a[208:211], v[192:195], v[124:127]\n\t"
  "v_mfma_f32_16x16x32_bf16 v[140:143], a[208:211], v[196:199], v[140:143]\n\t"
  "v_mfma_f32_16x16x32_bf16 v[156:159], a[208:211], v[200:203], v[156:159]\n\t"
  "v_mfma_f32_16x16x32_bf16 v[172:175], a[208:211], v[204:207], v[172:175]\n\t"
  "v_mfma_f32_16x16x32_bf16 v[188:191], a[208:211], v[208:211], v[188:191]\n\t"
  "s_waitcnt vmcnt(19) lgkmcnt(0)\n\t"
  "s_barrier\n\t"
  "v_mfma_f32_16x16x32_bf16 v[32:35], a[212:215], v[224:227], v[32:35]\n\t"
  "s_add_u32 m0, s44, 53248\n\t"
  "s_mov_b32 s52, 3670016\n\t"
  "buffer_load_dwordx4 v28, s[24:27], s52 offen offset:0 lds\n\t"
  "v_mfma_f32_16x16x32_bf16 v[48:51], a[212:215], v[228:231], v[48:51]\n\t"
  "v_mfma_f32_16x16x32_bf16 v[64:67], a[212:215], v[232:235], v[64:67]\n\t"
  "ds_read_b128 v[192:195], v18 offset:0\n\t"
  "v_mfma_f32_16x16x32_bf16 v[80:83], a[212:215], v[236:239], v[80:83]\n\t"
  "ds_read_b128 v[196:199], v18 offset:2048\n\t"
  "ds_read_b128 v[200:203], v18 offset:4096\n\t"
  "v_mfma_f32_16x16x32_bf16 v[96:99], a[212:215], v[240:243], v[96:99]\n\t"
  "buffer_load_dwordx4 v29, s[24:27], s52 offen offset:1024 lds\n\t"
  "v_mfma_f32_16x16x32_bf16 v[112:115], a[216:219], v[224:227], v[112:115]\n\t"
  "v_mfma_f32_16x16x32_bf16 v[128:131], a[216:219], v[228:231], v[128:131]\n\t"
  "ds_read_b128 v[204:207], v18 offset:6144\n\t"
  "buffer_load_dwordx4 v30, s[24:27], s52 offen offset:2048 lds\n\t"
  "v_mfma_f32_16x16x32_bf16 v[144:147], a[216:219], v[232:235], v[144:147]\n\t"
  "v_mfma_f32_16x16x32_bf16 v[160:163], a[216:219], v[236:239], v[160:163]\n\t"
  "ds_read_b128 v[208:211], v18 offset:8192\n\t"
  "ds_read_b64_tr_b16 a[180:181], v22 offset:0\n\t"
  "v_mfma_f32_16x16x32_bf16 v[176:179], a[216:219], v[240:243], v[176:179]\n\t"
  "buffer_load_dwordx4 v31, s[24:27], s52 offen offset:3072 lds\n\t"
  "v_mfma_f32_16x16x32_bf16 v[36:39], a[220:223], v[224:227], v[36:39]\n\t"
  "v_mfma_f32_16x16x32_bf16 v[52:55], a[220:223], v[228:231], v[52:55]\n\t"
  "ds_read_b64_tr_b16 a[182:183], v22 offset:4096\n\t"
  "s_add_u32 m0, s44, 86016\n\t"
  "s_mov_b32 s52, 896\n\t"
  "buffer_load_dwordx4 v23, s[20:23], s52 offen offset:0 lds\n\t"
  "v_mfma_f32_16x16x32_bf16 v[68:71], a[220:223], v[232:235], v[68:71]\n\t"
  "v_mfma_f32_16x16x32_bf16 v[84:87], a[220:223], v[236:239], v[84:87]\n\t"
  "v_mfma_f32_16x16x32_bf16 v[100:103], a[220:223], v[240:243], v[100:103]\n\t"
  "ds_read_b64_tr_b16 a[184:185], v22 offset:8\n\t"
  "ds_read_b64_tr_b16 a[186:187], v22 offset:4104\n\t"
  "v_mfma_f32_16x16x32_bf16 v[116:119], a[224:227], v[224:227], v[116:119]\n\t"
  "ds_read_b64_tr_b16 a[188:189], v22 offset:512\n\t"
  "buffer_load_dwordx4 v24, s[20:23], s52 offen offset:1024 lds\n\t"
  "v_mfma_f32_16x16x32_bf16 v[132:135], a[224:227], v[228:231], v[132:135]\n\t"
  "v_mfma_f32_16x16x32_bf16 v[148:151], a[224:227], v[232:235], v[148:151]\n\t"
  "ds_read_b64_tr_b16 a[190:191], v22 offset:4608\n\t"
  "v_mfma_f32_16x16x32_bf16 v[164:167], a[224:227], v[236:239], v[164:167]\n\t"
  "buffer_load_dwordx4 v25, s[20:23], s52 offen offset:2048 lds\n\t"
  "v_mfma_f32_16x16x32_bf16 v[180:183], a[224:227], v[240:243], v[180:183]\n\t"
  "ds_read_b64_tr_b16 a[192:193], v22 offset:520\n\t"
  "v_mfma_f32_16x16x32_bf16 v[40:43], a[228:231], v[224:227], v[40:43]\n\t"
  "ds_read_b64_tr_b16 a[194:195], v22 offset:4616\n\t"
  "buffer_load_dwordx4 v26, s[20:23], s52 offen offset:3072 lds\n\t"
  "v_mfma_f32_16x16x32_bf16 v[56:59], a[228:231], v[228:231], v[56:59]\n\t"
  "v_mfma_f32_16x16x32_bf16 v[72:75], a[228:231], v[232:235], v[72:75]\n\t"
  "ds_read_b64_tr_b16 a[196:197], v22 offset:1024\n\t"
  "v_mfma_f32_16x16x32_bf16 v[88:91], a[228:231], v[236:239], v[88:91]\n\t"
  "s_add_u32 m0, s45, 102400\n\t"
  "s_nop 0\n\t"
  "buffer_load_dwordx4 v27, s[20:23], s52 offen offset:0 lds\n\t"
  "v_mfma_f32_16x16x32_bf16 v[104:107], a[228:231], v[240:243], v[104:107]\n\t"
  "v_mfma_f32_16x16x32_bf16 v[120:123], a[232:235], v[224:227], v[120:123]\n\t"
  "ds_read_b64_tr_b16 a[198:199], v22 offset:5120\n\t"
  "v_mfma_f32_16x16x32_bf16 v[136:139], a[232:235], v[228:231], v[136:139]\n\t"
  "ds_read_b64_tr_b16 a[200:201], v22 offset:1032\n\t"
  "s_add_u32 m0, s44, 69632\n\t"
  "s_mov_b32 s52, 3932160\n\t"
  "buffer_load_dwordx4 v28, s[24:27], s52 offen offset:0 lds\n\t"
  "v_mfma_f32_16x16x32_bf16 v[152:155], a[232:235], v[232:235], v[152:155]\n\t"
  "v_mfma_f32_16x16x32_bf16 v[168:171], a[232:235], v[236:239], v[168:171]\n\t"
  "v_mfma_f32_16x16x32_bf16 v[184:187], a[232:235], v[240:243], v[184:187]\n\t"
  "ds_read_b64_tr_b16 a[202:203], v22 offset:5128\n\t"
  "v_mfma_f32_16x16x32_bf16 v[44:47], a[236:239], v[224:227], v[44:47]\n\t"
  "ds_read_b64_tr_b16 a[204:205], v22 offset:1536\n\t"
  "ds_read_b64_tr_b16 a[206:207], v22 offset:5632\n\t"
  "v_mfma_f32_16x16x32_bf16 v[60:63], a[236:239], v[228:231], v[60:63]\n\t"
  "buffer_load_dwordx4 v29, s[24:27], s52 offen offset:1024 lds\n\t"
  "v_mfma_f32_16x16x32_bf16 v[76:79], a[236:239], v[232:235], v[76:79]\n\t"
  "ds_read_b64_tr_b16 a[208:209], v22 offset:1544\n\t"
  "v_mfma_f32_16x16x32_bf16 v[92:95], a[236:239], v[236:239], v[92:95]\n\t"
  "buffer_load_dwordx4 v30, s[24:27], s52 offen offset:2048 lds\n\t"
  "v_mfma_f32_16x16x32_bf16 v[108:111], a[236:239], v[240:243], v[108:111]\n\t"
  "v_mfma_f32_16x16x32_bf16 v[124:127], a[240:243], v[224:227], v[124:127]\n\t"
  "ds_read_b64_tr_b16 a[210:211], v22 offset:5640\n\t"
  "buffer_load_dwordx4 v31, s[24:27], s52 offen offset:3072 lds\n\t"
  "v_mfma_f32_16x16x32_bf16 v[140:143], a[240:243], v[228:231], v[140:143]\n\t"
  "v_mfma_f32_16x16x32_bf16 v[156:159], a[240:243], v[232:235], v[156:159]\n\t"
  "v_mfma_f32_16x16x32_bf16 v[172:175], a[240:243], v[236:239], v[172:175]\n\t"
  "v_mfma_f32_16x16x32_bf16 v[188:191], a[240:243], v[240:243], v[188:191]\n\t"
  "s_waitcnt lgkmcnt(0)\n\t"
  "v_mfma_f32_16x16x32_bf16 v[32:35], a[180:183], v[192:195], v[32:35]\n\t"
  "global_load_dword v251, v[252:253], off\n\t"
  "v_lshl_add_u64 v[252:253], v[4:5], 0, v[252:253]\n\t"
  "v_mfma_f32_16x16x32_bf16 v[48:51], a[180:183], v[196:199], v[48:51]\n\t"
  "v_mfma_f32_16x16x32_bf16 v[64:67], a[180:183], v[200:203], v[64:67]\n\t"
  "ds_read_b128 v[224:227], v19 offset:0\n\t"
  "s_mov_b32 s53, 4096\n\t"
  "buffer_load_dwordx4 a[16:19], v212, s[28:31], s53 offen offset:0 nt\n\t"
  "v_mfma_f32_16x16x32_bf16 v[80:83], a[180:183], v[204:207], v[80:83]\n\t"
  "v_mfma_f32_16x16x32_bf16 v[96:99], a[180:183], v[208:211], v[96:99]\n\t"
  "v_mfma_f32_16x16x32_bf16 v[112:115], a[184:187], v[192:195], v[112:115]\n\t"
  "ds_read_b128 v[228:231], v19 offset:2048\n\t"
  "ds_read_b128 v[232:235], v19 offset:4096\n\t"
  "v_mfma_f32_16x16x32_bf16 v[128:131], a[184:187], v[196:199], v[128:131]\n\t"
  "buffer_load_dwordx4 a[20:23], v213, s[28:31], s53 offen offset:1024 nt\n\t"
  "v_mfma_f32_16x16x32_bf16 v[144:147], a[184:187], v[200:203], v[144:147]\n\t"
  "buffer_load_dwordx4 a[24:27], v214, s[28:31], s53 offen offset:2048 nt\n\t"
  "v_mfma_f32_16x16x32_bf16 v[160:163], a[184:187], v[204:207], v[160:163]\n\t"
  "v_mfma_f32_16x16x32_bf16 v[176:179], a[184:187], v[208:211], v[176:179]\n\t"
  "ds_read_b128 v[236:239], v19 offset:6144\n\t"
  "v_mfma_f32_16x16x32_bf16 v[36:39], a[188:191], v[192:195], v[36:39]\n\t"
  "buffer_load_dwordx4 a[28:31], v215, s[28:31], s53 offen offset:3072 nt\n\t"
  "v_mfma_f32_16x16x32_bf16 v[52:55], a[188:191], v[196:199], v[52:55]\n\t"
  "ds_read_b128 v[240:243], v19 offset:8192\n\t"
  "v_mfma_f32_16x16x32_bf16 v[68:71], a[188:191], v[200:203], v[68:71]\n\t"
  "ds_read_b64_tr_b16 a[212:213], v22 offset:16384\n\t"
  "ds_read_b64_tr_b16 a[214:215], v22 offset:20480\n\t"
  "v_mfma_f32_16x16x32_bf16 v[84:87], a[188:191], v[204:207], v[84:87]\n\t"
  "ds_read_b64_tr_b16 a[216:217], v22 offset:16392\n\t"
  "ds_read_b64_tr_b16 a[218:219], v22 offset:20488\n\t"
  "v_mfma_f32_16x16x32_bf16 v[100:103], a[188:191], v[208:211], v[100:103]\n\t"
  "ds_read_b64_tr_b16 a[220:221], v22 offset:16896\n\t"
  "v_mfma_f32_16x16x32_bf16 v[116:119], a[192:195], v[192:195], v[116:119]\n\t"
  "v_mfma_f32_16x16x32_bf16 v[132:135], a[192:195], v[196:199], v[132:135]\n\t"
  "ds_read_b64_tr_b16 a[222:223], v22 offset:20992\n\t"
  "v_mfma_f32_16x16x32_bf16 v[148:151], a[192:195], v[200:203], v[148:151]\n\t"
  "ds_read_b64_tr_b16 a[224:225], v22 offset:16904\n\t"
  "v_mfma_f32_16x16x32_bf16 v[164:167], a[192:195], v[204:207], v[164:167]\n\t"
  "v_mfma_f32_16x16x32_bf16 v[180:183], a[192:195], v[208:211], v[180:183]\n\t"
  "ds_read_b64_tr_b16 a[226:227], v22 offset:21000\n\t"
  "v_mfma_f32_16x16x32_bf16 v[40:43], a[196:199], v[192:195], v[40:43]\n\t"
  "ds_read_b64_tr_b16 a[228:229], v22 offset:17408\n\t"
  "v_mfma_f32_16x16x32_bf16 v[56:59], a[196:199], v[196:199], v[56:59]\n\t"
  "v_mfma_f32_16x16x32_bf16 v[72:75], a[196:199], v[200:203], v[72:75]\n\t"
  "ds_read_b64_tr_b16 a[230:231], v22 offset:21504\n\t"
  "v_mfma_f32_16x16x32_bf16 v[88:91], a[196:199], v[204:207], v[88:91]\n\t"
  "ds_read_b64_tr_b16 a[232:233], v22 offset:17416\n\t"
  "v_mfma_f32_16x16x32_bf16 v[104:107], a[196:199], v[208:211], v[104:107]\n\t"
  "v_mfma_f32_16x16x32_bf16 v[120:123], a[200:203], v[192:195], v[120:123]\n\t"
  "ds_read_b64_tr_b16 a[234:235], v22 offset:21512\n\t"
  "v_mfma_f32_16x16x32_bf16 v[136:139], a[200:203], v[196:199], v[136:139]\n\t"
  "v_mfma_f32_16x16x32_bf16 v[152:155], a[200:203], v[200:203], v[152:155]\n\t"
  "ds_read_b64_tr_b16 a[236:237], v22 offset:17920\n\t"
  "v_mfma_f32_16x16x32_bf16 v[168:171], a[200:203], v[204:207], v[168:171]\n\t"
  "ds_read_b64_tr_b16 a[238:239], v22 offset:22016\n\t"
  "v_mfma_f32_16x16x32_bf16 v[184:187], a[200:203], v[208:211], v[184:187]\n\t"
  "v_mfma_f32_16x16x32_bf16 v[44:47], a[204:207], v[192:195], v[44:47]\n\t"
  "ds_read_b64_tr_b16 a[240:241], v22 offset:17928\n\t"
  "v_mfma_f32_16x16x32_bf16 v[60:63], a[204:207], v[196:199], v[60:63]\n\t"
  "ds_read_b64_tr_b16 a[242:243], v22 offset:22024\n\t"
  "v_mfma_f32_16x16x32_bf16 v[76:79], a[204:207], v[200:203], v[76:79]\n\t"
  "v_mfma_f32_16x16x32_bf16 v[92:95], a[204:207], v[204:207], v[92:95]\n\t"
  "v_mfma_f32_16x16x32_bf16 v[108:111], a[204:207], v[208:211], v[108:111]\n\t"
  "v_mfma_f32_16x16x32_bf16 v[124:127], a[208:211], v[192:195], v[124:127]\n\t"
  "v_mfma_f32_16x16x32_bf16 v[140:143], a[208:211], v[196:199], v[140:143]\n\t"
  "v_mfma_f32_16x16x32_bf16 v[156:159], a[208:211], v[200:203], v[156:159]\n\t"
  "v_mfma_f32_16x16x32_bf16 v[172:175], a[208:211], v[204:207], v[172:175]\n\t"
  "v_mfma_f32_16x16x32_bf16 v[188:191], a[208:211], v[208:211], v[188:191]\n\t"
  "s_waitcnt vmcnt(23) lgkmcnt(0)\n\t"
  "s_barrier\n\t"
  "v_mfma_f32_16x16x32_bf16 v[32:35], a[212:215], v[224:227], v[32:35]\n\t"
  "s_add_u32 m0, s44, 106496\n\t"
  "s_mov_b32 s52, 4194304\n\t"
  "buffer_load_dwordx4 v28, s[24:27], s52 offen offset:0 lds\n\t"
  "v_mfma_f32_16x16x32_bf16 v[48:51], a[212:215], v[228:231], v[48:51]\n\t"
  "v_mfma_f32_16x16x32_bf16 v[64:67], a[212:215], v[232:235], v[64:67]\n\t"
  "ds_read_b128 v[192:195], v16 offset:0\n\t"
  "v_mfma_f32_16x16x32_bf16 v[80:83], a[212:215], v[236:239], v[80:83]\n\t"
  "ds_read_b128 v[196:199], v16 offset:2048\n\t"
  "ds_read_b128 v[200:203], v16 offset:4096\n\t"
  "v_mfma_f32_16x16x32_bf16 v[96:99], a[212:215], v[240:243], v[96:99]\n\t"
  "buffer_load_dwordx4 v29, s[24:27], s52 offen offset:1024 lds\n\t"
  "v_mfma_f32_16x16x32_bf16 v[112:115], a[216:219], v[224:227], v[112:115]\n\t"
  "v_mfma_f32_16x16x32_bf16 v[128:131], a[216:219], v[228:231], v[128:131]\n\t"
  "ds_read_b128 v[204:207], v16 offset:6144\n\t"
  "buffer_load_dwordx4 v30, s[24:27], s52 offen offset:2048 lds\n\t"
  "v_mfma_f32_16x16x32_bf16 v[144:147], a[216:219], v[232:235], v[144:147]\n\t"
  "v_mfma_f32_16x16x32_bf16 v[160:163], a[216:219], v[236:239], v[160:163]\n\t"
  "ds_read_b128 v[208:211], v16 offset:8192\n\t"
  "ds_read_b64_tr_b16 a[180:181], v20 offset:0\n\t"
  "v_mfma_f32_16x16x32_bf16 v[176:179], a[216:219], v[240:243], v[176:179]\n\t"
  "buffer_load_dwordx4 v31, s[24:27], s52 offen offset:3072 lds\n\t"
  "v_mfma_f32_16x16x32_bf16 v[36:39], a[220:223], v[224:227], v[36:39]\n\t"
  "v_mfma_f32_16x16x32_bf16 v[52:55], a[220:223], v[228:231], v[52:55]\n\t"
  "ds_read_b64_tr_b16 a[182:183], v20 offset:4096\n\t"
  "s_add_u32 m0, s44, 139264\n\t"
  "s_mov_b32 s52, 1024\n\t"
  "buffer_load_dwordx4 v23, s[20:23], s52 offen offset:0 lds\n\t"
  "v_mfma_f32_16x16x32_bf16 v[68:71], a[220:223], v[232:235], v[68:71]\n\t"
  "v_mfma_f32_16x16x32_bf16 v[84:87], a[220:223], v[236:239], v[84:87]\n\t"
  "v_mfma_f32_16x16x32_bf16 v[100:103], a[220:223], v[240:243], v[100:103]\n\t"
  "ds_read_b64_tr_b16 a[184:185], v20 offset:8\n\t"
  "ds_read_b64_tr_b16 a[186:187], v20 offset:4104\n\t"
  "v_mfma_f32_16x16x32_bf16 v[116:119], a[224:227], v[224:227], v[116:119]\n\t"
  "ds_read_b64_tr_b16 a[188:189], v20 offset:512\n\t"
  "buffer_load_dwordx4 v24, s[20:23], s52 offen offset:1024 lds\n\t"
  "v_mfma_f32_16x16x32_bf16 v[132:135], a[224:227], v[228:231], v[132:135]\n\t"
  "v_mfma_f32_16x16x32_bf16 v[148:151], a[224:227], v[232:235], v[148:151]\n\t"
  "ds_read_b64_tr_b16 a[190:191], v20 offset:4608\n\t"
  "v_mfma_f32_16x16x32_bf16 v[164:167], a[224:227], v[236:239], v[164:167]\n\t"
  "buffer_load_dwordx4 v25, s[20:23], s52 offen offset:2048 lds\n\t"
  "v_mfma_f32_16x16x32_bf16 v[180:183], a[224:227], v[240:243], v[180:183]\n\t"
  "ds_read_b64_tr_b16 a[192:193], v20 offset:520\n\t"
  "v_mfma_f32_16x16x32_bf16 v[40:43], a[228:231], v[224:227], v[40:43]\n\t"
  "ds_read_b64_tr_b16 a[194:195], v20 offset:4616\n\t"
  "buffer_load_dwordx4 v26, s[20:23], s52 offen offset:3072 lds\n\t"
  "v_mfma_f32_16x16x32_bf16 v[56:59], a[228:231], v[228:231], v[56:59]\n\t"
  "v_mfma_f32_16x16x32_bf16 v[72:75], a[228:231], v[232:235], v[72:75]\n\t"
  "ds_read_b64_tr_b16 a[196:197], v20 offset:1024\n\t"
  "v_mfma_f32_16x16x32_bf16 v[88:91], a[228:231], v[236:239], v[88:91]\n\t"
  "s_add_u32 m0, s45, 155648\n\t"
  "s_nop 0\n\t"
  "buffer_load_dwordx4 v27, s[20:23], s52 offen offset:0 lds\n\t"
  "v_mfma_f32_16x16x32_bf16 v[104:107], a[228:231], v[240:243], v[104:107]\n\t"
  "v_mfma_f32_16x16x32_bf16 v[120:123], a[232:235], v[224:227], v[120:123]\n\t"
  "ds_read_b64_tr_b16 a[198:199], v20 offset:5120\n\t"
  "v_mfma_f32_16x16x32_bf16 v[136:139], a[232:235], v[228:231], v[136:139]\n\t"
  "ds_read_b64_tr_b16 a[200:201], v20 offset:1032\n\t"
  "s_add_u32 m0, s44, 122880\n\t"
  "s_mov_b32 s52, 4456448\n\t"
  "buffer_load_dwordx4 v28, s[24:27], s52 offen offset:0 lds\n\t"
  "v_mfma_f32_16x16x32_bf16 v[152:155], a[232:235], v[232:235], v[152:155]\n\t"
  "v_mfma_f32_16x16x32_bf16 v[168:171], a[232:235], v[236:239], v[168:171]\n\t"
  "v_mfma_f32_16x16x32_bf16 v[184:187], a[232:235], v[240:243], v[184:187]\n\t"
  "ds_read_b64_tr_b16 a[202:203], v20 offset:5128\n\t"
  "v_mfma_f32_16x16x32_bf16 v[44:47], a[236:239], v[224:227], v[44:47]\n\t"
  "ds_read_b64_tr_b16 a[204:205], v20 offset:1536\n\t"
  "ds_read_b64_tr_b16 a[206:207], v20 offset:5632\n\t"
  "v_mfma_f32_16x16x32_bf16 v[60:63], a[236:239], v[228:231], v[60:63]\n\t"
  "buffer_load_dwordx4 v29, s[24:27], s52 offen offset:1024 lds\n\t"
  "v_mfma_f32_16x16x32_bf16 v[76:79], a[236:239], v[232:235], v[76:79]\n\t"
  "ds_read_b64_tr_b16 a[208:209], v20 offset:1544\n\t"
  "v_mfma_f32_16x16x32_bf16 v[92:95], a[236:239], v[236:239], v[92:95]\n\t"
  "buffer_load_dwordx4 v30, s[24:27], s52 offen offset:2048 lds\n\t"
  "v_mfma_f32_16x16x32_bf16 v[108:111], a[236:239], v[240:243], v[108:111]\n\t"
  "v_mfma_f32_16x16x32_bf16 v[124:127], a[240:243], v[224:227], v[124:127]\n\t"
  "ds_read_b64_tr_b16 a[210:211], v20 offset:5640\n\t"
  "buffer_load_dwordx4 v31, s[24:27], s52 offen offset:3072 lds\n\t"
  "v_mfma_f32_16x16x32_bf16 v[140:143], a[240:243], v[228:231], v[140:143]\n\t"
  "v_mfma_f32_16x16x32_bf16 v[156:159], a[240:243], v[232:235], v[156:159]\n\t"
  "v_mfma_f32_16x16x32_bf16 v[172:175], a[240:243], v[236:239], v[172:175]\n\t"
  "v_mfma_f32_16x16x32_bf16 v[188:191], a[240:243], v[240:243], v[188:191]\n\t"
  "s_waitcnt lgkmcnt(0)\n\t"
  "v_mfma_f32_16x16x32_bf16 v[32:35], a[180:183], v[192:195], v[32:35]\n\t"
  "buffer_load_dwordx4 a[128:131], v223, s[40:43], 0 offen offset:0 nt\n\t"
  "v_mfma_f32_16x16x32_bf16 v[48:51], a[180:183], v[196:199], v[48:51]\n\t"
  "ds_read_b128 v[224:227], v17 offset:0\n\t"
  "v_mfma_f32_16x16x32_bf16 v[64:67], a[180:183], v[200:203], v[64:67]\n\t"
  "buffer_load_dwordx4 a[132:135], v223, s[40:43], 0 offen offset:64 nt\n\t"
  "v_mfma_f32_16x16x32_bf16 v[80:83], a[180:183], v[204:207], v[80:83]\n\t"
  "v_mfma_f32_16x16x32_bf16 v[96:99], a[180:183], v[208:211], v[96:99]\n\t"
  "ds_read_b128 v[228:231], v17 offset:2048\n\t"
  "ds_read_b128 v[232:235], v17 offset:4096\n\t"
  "v_mfma_f32_16x16x32_bf16 v[112:115], a[184:187], v[192:195], v[112:115]\n\t"
  "buffer_load_dwordx4 a[136:139], v223, s[40:43], 0 offen offset:128 nt\n\t"
  "v_mfma_f32_16x16x32_bf16 v[128:131], a[184:187], v[196:199], v[128:131]\n\t"
  "ds_read_b128 v[236:239], v17 offset:6144\n\t"
  "v_mfma_f32_16x16x32_bf16 v[144:147], a[184:187], v[200:203], v[144:147]\n\t"
  "buffer_load_dwordx4 a[140:143], v223, s[40:43], 0 offen offset:192 nt\n\t"
  "v_mfma_f32_16x16x32_bf16 v[160:163], a[184:187], v[204:207], v[160:163]\n\t"
  "v_mfma_f32_16x16x32_bf16 v[176:179], a[184:187], v[208:211], v[176:179]\n\t"
  "ds_read_b128 v[240:243], v17 offset:8192\n\t"
  "ds_read_b64_tr_b16 a[212:213], v20 offset:16384\n\t"
  "v_mfma_f32_16x16x32_bf16 v[36:39], a[188:191], v[192:195], v[36:39]\n\t"
  "ds_read_b64_tr_b16 a[214:215], v20 offset:20480\n\t"
  "v_mfma_f32_16x16x32_bf16 v[52:55], a[188:191], v[196:199], v[52:55]\n\t"
  "ds_read_b64_tr_b16 a[216:217], v20 offset:16392\n\t"
  "v_mfma_f32_16x16x32_bf16 v[68:71], a[188:191], v[200:203], v[68:71]\n\t"
  "v_mfma_f32_16x16x32_bf16 v[84:87], a[188:191], v[204:207], v[84:87]\n\t"
  "ds_read_b64_tr_b16 a[218:219], v20 offset:20488\n\t"
  "v_mfma_f32_16x16x32_bf16 v[100:103], a[188:191], v[208:211], v[100:103]\n\t"
  "ds_read_b64_tr_b16 a[220:221], v20 offset:16896\n\t"
  "v_mfma_f32_16x16x32_bf16 v[116:119], a[192:195], v[192:195], v[116:119]\n\t"
  "v_mfma_f32_16x16x32_bf16 v[132:135], a[192:195], v[196:199], v[132:135]\n\t"
  "ds_read_b64_tr_b16 a[222:223], v20 offset:20992\n\t"
  "v_mfma_f32_16x16x32_bf16 v[148:151], a[192:195], v[200:203], v[148:151]\n\t"
  "ds_read_b64_tr_b16 a[224:225], v20 offset:16904\n\t"
  "v_mfma_f32_16x16x32_bf16 v[164:167], a[192:195], v[204:207], v[164:167]\n\t"
  "v_mfma_f32_16x16x32_bf16 v[180:183], a[192:195], v[208:211], v[180:183]\n\t"
  "ds_read_b64_tr_b16 a[226:227], v20 offset:21000\n\t"
  "v_mfma_f32_16x16x32_bf16 v[40:43], a[196:199], v[192:195], v[40:43]\n\t"
  "ds_read_b64_tr_b16 a[228:229], v20 offset:17408\n\t"
  "v_mfma_f32_16x16x32_bf16 v[56:59], a[196:199], v[196:199], v[56:59]\n\t"
  "v_mfma_f32_16x16x32_bf16 v[72:75], a[196:199], v[200:203], v[72:75]\n\t"
  "ds_read_b64_tr_b16 a[230:231], v20 offset:21504\n\t"
  "v_mfma_f32_16x16x32_bf16 v[88:91], a[196:199], v[204:207], v[88:91]\n\t"
  "ds_read_b64_tr_b16 a[232:233], v20 offset:17416\n\t"
  "v_mfma_f32_16x16x32_bf16 v[104:107], a[196:199], v[208:211], v[104:107]\n\t"
  "v_mfma_f32_16x16x32_bf16 v[120:123], a[200:203], v[192:195], v[120:123]\n\t"
  "ds_read_b64_tr_b16 a[234:235], v20 offset:21512\n\t"
  "v_mfma_f32_16x16x32_bf16 v[136:139], a[200:203], v[196:199], v[136:139]\n\t"
  "v_mfma_f32_16x16x32_bf16 v[152:155], a[200:203], v[200:203], v[152:155]\n\t"
  "ds_read_b64_tr_b16 a[236:237], v20 offset:17920\n\t"
  "v_mfma_f32_16x16x32_bf16 v[168:171], a[200:203], v[204:207], v[168:171]\n\t"
  "ds_read_b64_tr_b16 a[238:239], v20 offset:22016\n\t"
  "v_mfma_f32_16x16x32_bf16 v[184:187], a[200:203], v[208:211], v[184:187]\n\t"
  "v_mfma_f32_16x16x32_bf16 v[44:47], a[204:207], v[192:195], v[44:47]\n\t"
  "ds_read_b64_tr_b16 a[240:241], v20 offset:17928\n\t"
  "v_mfma_f32_16x16x32_bf16 v[60:63], a[204:207], v[196:199], v[60:63]\n\t"
  "ds_read_b64_tr_b16 a[242:243], v20 offset:22024\n\t"
  "v_mfma_f32_16x16x32_bf16 v[76:79], a[204:207], v[200:203], v[76:79]\n\t"
  "v_mfma_f32_16x16x32_bf16 v[92:95], a[204:207], v[204:207], v[92:95]\n\t"
  "v_mfma_f32_16x16x32_bf16 v[108:111], a[204:207], v[208:211], v[108:111]\n\t"
  "v_mfma_f32_16x16x32_bf16 v[124:127], a[208:211], v[192:195], v[124:127]\n\t"
  "v_mfma_f32_16x16x32_bf16 v[140:143], a[208:211], v[196:199], v[140:143]\n\t"
  "v_mfma_f32_16x16x32_bf16 v[156:159], a[208:211], v[200:203], v[156:159]\n\t"
  "v_mfma_f32_16x16x32_bf16 v[172:175], a[208:211], v[204:207], v[172:175]\n\t"
  "v_mfma_f32_16x16x32_bf16 v[188:191], a[208:211], v[208:211], v[188:191]\n\t"
  "s_waitcnt vmcnt(22) lgkmcnt(0)\n\t"
  "s_barrier\n\t"
  "v_mfma_f32_16x16x32_bf16 v[32:35], a[212:215], v[224:227], v[32:35]\n\t"
  "s_add_u32 m0, s44, 0\n\t"
  "s_mov_b32 s52, 4718592\n\t"
  "buffer_load_dwordx4 v28, s[24:27], s52 offen offset:0 lds\n\t"
  "v_mfma_f32_16x16x32_bf16 v[48:51], a[212:215], v[228:231], v[48:51]\n\t"
  "v_mfma_f32_16x16x32_bf16 v[64:67], a[212:215], v[232:235], v[64:67]\n\t"
  "ds_read_b128 v[192:195], v16 offset:53248\n\t"
  "v_mfma_f32_16x16x32_bf16 v[80:83], a[212:215], v[236:239], v[80:83]\n\t"
  "ds_read_b128 v[196:199], v16 offset:55296\n\t"
  "buffer_load_dwordx4 v29, s[24:27], s52 offen offset:1024 lds\n\t"
  "v_mfma_f32_16x16x32_bf16 v[96:99], a[212:215], v[240:243], v[96:99]\n\t"
  "v_mfma_f32_16x16x32_bf16 v[112:115], a[216:219], v[224:227], v[112:115]\n\t"
  "ds_read_b128 v[200:203], v16 offset:57344\n\t"
  "ds_read_b128 v[204:207], v16 offset:59392\n\t"
  "v_mfma_f32_16x16x32_bf16 v[128:131], a[216:219], v[228:231], v[128:131]\n\t"
  "buffer_load_dwordx4 v30, s[24:27], s52 offen offset:2048 lds\n\t"
  "v_mfma_f32_16x16x32_bf16 v[144:147], a[216:219], v[232:235], v[144:147]\n\t"
  "ds_read_b128 v[208:211], v16 offset:61440\n\t"
  "v_mfma_f32_16x16x32_bf16 v[160:163], a[216:219], v[236:239], v[160:163]\n\t"
  "buffer_load_dwordx4 v31, s[24:27], s52 offen offset:3072 lds\n\t"
  "v_mfma_f32_16x16x32_bf16 v[176:179], a[216:219], v[240:243], v[176:179]\n\t"
  "v_mfma_f32_16x16x32_bf16 v[36:39], a[220:223], v[224:227], v[36:39]\n\t"
  "ds_read_b64_tr_b16 a[180:181], v21 offset:0\n\t"
  "s_add_u32 m0, s44, 32768\n\t"
  "s_mov_b32 s52, 1152\n\t"
  "buffer_load_dwordx4 v23, s[20:23], s52 offen offset:0 lds\n\t"
  "v_mfma_f32_16x16x32_bf16 v[52:55], a[220:223], v[228:231], v[52:55]\n\t"
  "v_mfma_f32_16x16x32_bf16 v[68:71], a[220:223], v[232:235], v[68:71]\n\t"
  "v_mfma_f32_16x16x32_bf16 v[84:87], a[220:223], v[236:239], v[84:87]\n\t"
  "ds_read_b64_tr_b16 a[182:183], v21 offset:4096\n\t"
  "ds_read_b64_tr_b16 a[184:185], v21 offset:8\n\t"
  "buffer_load_dwordx4 v24, s[20:23], s52 offen offset:1024 lds\n\t"
  "v_mfma_f32_16x16x32_bf16 v[100:103], a[220:223], v[240:243], v[100:103]\n\t"
  "v_mfma_f32_16x16x32_bf16 v[116:119], a[224:227], v[224:227], v[116:119]\n\t"
  "ds_read_b64_tr_b16 a[186:187], v21 offset:4104\n\t"
  "v_mfma_f32_16x16x32_bf16 v[132:135], a[224:227], v[228:231], v[132:135]\n\t"
  "buffer_load_dwordx4 v25, s[20:23], s52 offen offset:2048 lds\n\t"
  "v_mfma_f32_16x16x32_bf16 v[148:151], a[224:227], v[232:235], v[148:151]\n\t"
  "ds_read_b64_tr_b16 a[188:189], v21 offset:512\n\t"
  "v_mfma_f32_16x16x32_bf16 v[164:167], a[224:227], v[236:239], v[164:167]\n\t"
  "ds_read_b64_tr_b16 a[190:191], v21 offset:4608\n\t"
  "buffer_load_dwordx4 v26, s[20:23], s52 offen offset:3072 lds\n\t"
  "v_mfma_f32_16x16x32_bf16 v[180:183], a[224:227], v[240:243], v[180:183]\n\t"
  "v_mfma_f32_16x16x32_bf16 v[40:43], a[228:231], v[224:227], v[40:43]\n\t"
  "ds_read_b64_tr_b16 a[192:193], v21 offset:520\n\t"
  "s_add_u32 m0, s45, 49152\n\t"
  "s_nop 0\n\t"
  "buffer_load_dwordx4 v27, s[20:23], s52 offen offset:0 lds\n\t"
  "v_mfma_f32_16x16x32_bf16 v[56:59], a[228:231], v[228:231], v[56:59]\n\t"
  "v_mfma_f32_16x16x32_bf16 v[72:75], a[228:231], v[232:235], v[72:75]\n\t"
  "v_mfma_f32_16x16x32_bf16 v[88:91], a[228:231], v[236:239], v[88:91]\n\t"
  "ds_read_b64_tr_b16 a[194:195], v21 offset:4616\n\t"
  "v_mfma_f32_16x16x32_bf16 v[104:107], a[228:231], v[240:243], v[104:107]\n\t"
  "ds_read_b64_tr_b16 a[196:197], v21 offset:1024\n\t"
  "s_add_u32 m0, s44, 16384\n\t"
  "s_mov_b32 s52, 4980736\n\t"
  "buffer_load_dwordx4 v28, s[24:27], s52 offen offset:0 lds\n\t"
  "v_mfma_f32_16x16x32_bf16 v[120:123], a[232:235], v[224:227], v[120:123]\n\t"
  "v_mfma_f32_16x16x32_bf16 v[136:139], a[232:235], v[228:231], v[136:139]\n\t"
  "v_mfma_f32_16x16x32_bf16 v[152:155], a[232:235], v[232:235], v[152:155]\n\t"
  "ds_read_b64_tr_b16 a[198:199], v21 offset:5120\n\t"
  "ds_read_b64_tr_b16 a[200:201], v21 offset:1032\n\t"
  "buffer_load_dwordx4 v29, s[24:27], s52 offen offset:1024 lds\n\t"
  "v_mfma_f32_16x16x32_bf16 v[168:171], a[232:235], v[236:239], v[168:171]\n\t"
  "v_mfma_f32_16x16x32_bf16 v[184:187], a[232:235], v[240:243], v[184:187]\n\t"
  "ds_read_b64_tr_b16 a[202:203], v21 offset:5128\n\t"
  "v_mfma_f32_16x16x32_bf16 v[44:47], a[236:239], v[224:227], v[44:47]\n\t"
  "buffer_load_dwordx4 v30, s[24:27], s52 offen offset:2048 lds\n\t"
  "v_mfma_f32_16x16x32_bf16 v[60:63], a[236:239], v[228:231], v[60:63]\n\t"
  "ds_read_b64_tr_b16 a[204:205], v21 offset:1536\n\t"
  "v_mfma_f32_16x16x32_bf16 v[76:79], a[236:239], v[232:235], v[76:79]\n\t"
  "ds_read_b64_tr_b16 a[206:207], v21 offset:5632\n\t"
  "buffer_load_dwordx4 v31, s[24:27], s52 offen offset:3072 lds\n\t"
  "v_mfma_f32_16x16x32_bf16 v[92:95], a[236:239], v[236:239], v[92:95]\n\t"
  "v_mfma_f32_16x16x32_bf16 v[108:111], a[236:239], v[240:243], v[108:111]\n\t"
  "ds_read_b64_tr_b16 a[208:209], v21 offset:1544\n\t"
  "global_load_dword v251, v[6:7], off\n\t"
  "v_mfma_f32_16x16x32_bf16 v[124:127], a[240:243], v[224:227], v[124:127]\n\t"
  "v_mfma_f32_16x16x32_bf16 v[140:143], a[240:243], v[228:231], v[140:143]\n\t"
  "ds_read_b64_tr_b16 a[210:211], v21 offset:5640\n\t"
  "s_mov_b64 s[60:61], 0x1400\n\t"
  "v_lshl_add_u64 v[248:249], v[6:7], 0, s[60:61]\n\t"
  "global_load_dword v251, v[248:249], off\n\t"
  "v_mfma_f32_16x16x32_bf16 v[156:159], a[240:243], v[232:235], v[156:159]\n\t"
  "v_mfma_f32_16x16x32_bf16 v[172:175], a[240:243], v[236:239], v[172:175]\n\t"
  "v_mfma_f32_16x16x32_bf16 v[188:191], a[240:243], v[240:243], v[188:191]\n\t"
  "s_waitcnt lgkmcnt(0)\n\t"
  "v_mfma_f32_16x16x32_bf16 v[32:35], a[180:183], v[192:195], v[32:35]\n\t"
  "buffer_load_dwordx4 a[144:147], v223, s[40:43], s54 offen offset:0 nt\n\t"
  "v_mfma_f32_16x16x32_bf16 v[48:51], a[180:183], v[196:199], v[48:51]\n\t"
  "ds_read_b128 v[224:227], v17 offset:53248\n\t"
  "v_mfma_f32_16x16x32_bf16 v[64:67], a[180:183], v[200:203], v[64:67]\n\t"
  "buffer_load_dwordx4 a[148:151], v223, s[40:43], s54 offen offset:64 nt\n\t"
  "v_mfma_f32_16x16x32_bf16 v[80:83], a[180:183], v[204:207], v[80:83]\n\t"
  "v_mfma_f32_16x16x32_bf16 v[96:99], a[180:183], v[208:211], v[96:99]\n\t"
  "ds_read_b128 v[228:231], v17 offset:55296\n\t"
  "ds_read_b128 v[232:235], v17 offset:57344\n\t"
  "v_mfma_f32_16x16x32_bf16 v[112:115], a[184:187], v[192:195], v[112:115]\n\t"
  "buffer_load_dwordx4 a[152:155], v223, s[40:43], s54 offen offset:128 nt\n\t"
  "v_mfma_f32_16x16x32_bf16 v[128:131], a[184:187], v[196:199], v[128:131]\n\t"
  "ds_read_b128 v[236:239], v17 offset:59392\n\t"
  "v_mfma_f32_16x16x32_bf16 v[144:147], a[184:187], v[200:203], v[144:147]\n\t"
  "buffer_load_dwordx4 a[156:159], v223, s[40:43], s54 offen offset:192 nt\n\t"
  "v_mfma_f32_16x16x32_bf16 v[160:163], a[184:187], v[204:207], v[160:163]\n\t"
  "v_mfma_f32_16x16x32_bf16 v[176:179], a[184:187], v[208:211], v[176:179]\n\t"
  "ds_read_b128 v[240:243], v17 offset:61440\n\t"
  "ds_read_b64_tr_b16 a[212:213], v21 offset:16384\n\t"
  "v_mfma_f32_16x16x32_bf16 v[36:39], a[188:191], v[192:195], v[36:39]\n\t"
  "ds_read_b64_tr_b16 a[214:215], v21 offset:20480\n\t"
  "v_mfma_f32_16x16x32_bf16 v[52:55], a[188:191], v[196:199], v[52:55]\n\t"
  "ds_read_b64_tr_b16 a[216:217], v21 offset:16392\n\t"
  "v_mfma_f32_16x16x32_bf16 v[68:71], a[188:191], v[200:203], v[68:71]\n\t"
  "v_mfma_f32_16x16x32_bf16 v[84:87], a[188:191], v[204:207], v[84:87]\n\t"
  "ds_read_b64_tr_b16 a[218:219], v21 offset:20488\n\t"
  "v_mfma_f32_16x16x32_bf16 v[100:103], a[188:191], v[208:211], v[100:103]\n\t"
  "ds_read_b64_tr_b16 a[220:221], v21 offset:16896\n\t"
  "v_mfma_f32_16x16x32_bf16 v[116:119], a[192:195], v[192:195], v[116:119]\n\t"
  "v_mfma_f32_16x16x32_bf16 v[132:135], a[192:195], v[196:199], v[132:135]\n\t"
  "ds_read_b64_tr_b16 a[222:223], v21 offset:20992\n\t"
  "v_mfma_f32_16x16x32_bf16 v[148:151], a[192:195], v[200:203], v[148:151]\n\t"
  "ds_read_b64_tr_b16 a[224:225], v21 offset:16904\n\t"
  "v_mfma_f32_16x16x32_bf16 v[164:167], a[192:195], v[204:207], v[164:167]\n\t"
  "v_mfma_f32_16x16x32_bf16 v[180:183], a[192:195], v[208:211], v[180:183]\n\t"
  "ds_read_b64_tr_b16 a[226:227], v21 offset:21000\n\t"
  "v_mfma_f32_16x16x32_bf16 v[40:43], a[196:199], v[192:195], v[40:43]\n\t"
  "ds_read_b64_tr_b16 a[228:229], v21 offset:17408\n\t"
  "v_mfma_f32_16x16x32_bf16 v[56:59], a[196:199], v[196:199], v[56:59]\n\t"
  "v_mfma_f32_16x16x32_bf16 v[72:75], a[196:199], v[200:203], v[72:75]\n\t"
  "ds_read_b64_tr_b16 a[230:231], v21 offset:21504\n\t"
  "v_mfma_f32_16x16x32_bf16 v[88:91], a[196:199], v[204:207], v[88:91]\n\t"
  "ds_read_b64_tr_b16 a[232:233], v21 offset:17416\n\t"
  "v_mfma_f32_16x16x32_bf16 v[104:107], a[196:199], v[208:211], v[104:107]\n\t"
  "v_mfma_f32_16x16x32_bf16 v[120:123], a[200:203], v[192:195], v[120:123]\n\t"
  "ds_read_b64_tr_b16 a[234:235], v21 offset:21512\n\t"
  "v_mfma_f32_16x16x32_bf16 v[136:139], a[200:203], v[196:199], v[136:139]\n\t"
  "v_mfma_f32_16x16x32_bf16 v[152:155], a[200:203], v[200:203], v[152:155]\n\t"
  "ds_read_b64_tr_b16 a[236:237], v21 offset:17920\n\t"
  "v_mfma_f32_16x16x32_bf16 v[168:171], a[200:203], v[204:207], v[168:171]\n\t"
  "ds_read_b64_tr_b16 a[238:239], v21 offset:22016\n\t"
  "v_mfma_f32_16x16x32_bf16 v[184:187], a[200:203], v[208:211], v[184:187]\n\t"
  "v_mfma_f32_16x16x32_bf16 v[44:47], a[204:207], v[192:195], v[44:47]\n\t"
  "ds_read_b64_tr_b16 a[240:241], v21 offset:17928\n\t"
  "v_mfma_f32_16x16x32_bf16 v[60:63], a[204:207], v[196:199], v[60:63]\n\t"
  "ds_read_b64_tr_b16 a[242:243], v21 offset:22024\n\t"
  "v_mfma_f32_16x16x32_bf16 v[76:79], a[204:207], v[200:203], v[76:79]\n\t"
  "v_mfma_f32_16x16x32_bf16 v[92:95], a[204:207], v[204:207], v[92:95]\n\t"
  "v_mfma_f32_16x16x32_bf16 v[108:111], a[204:207], v[208:211], v[108:111]\n\t"
  "v_mfma_f32_16x16x32_bf16 v[124:127], a[208:211], v[192:195], v[124:127]\n\t"
  "v_mfma_f32_16x16x32_bf16 v[140:143], a[208:211], v[196:199], v[140:143]\n\t"
  "v_mfma_f32_16x16x32_bf16 v[156:159], a[208:211], v[200:203], v[156:159]\n\t"
  "v_mfma_f32_16x16x32_bf16 v[172:175], a[208:211], v[204:207], v[172:175]\n\t"
  "v_mfma_f32_16x16x32_bf16 v[188:191], a[208:211], v[208:211], v[188:191]\n\t"
  "s_waitcnt vmcnt(23) lgkmcnt(0)\n\t"
  "s_barrier\n\t"
  "v_mfma_f32_16x16x32_bf16 v[32:35], a[212:215], v[224:227], v[32:35]\n\t"
  "s_waitcnt vmcnt(4)\n\t"
  "s_add_u32 m0, s51, 65536\n\t"
  "s_mov_b32 s52, 8192\n\t"
  "buffer_load_dwordx4 v212, s[28:31], s52 offen offset:0 nt lds\n\t"
  "v_mfma_f32_16x16x32_bf16 v[48:51], a[212:215], v[228:231], v[48:51]\n\t"
  "v_mfma_f32_16x16x32_bf16 v[64:67], a[212:215], v[232:235], v[64:67]\n\t"
  "v_mfma_f32_16x16x32_bf16 v[80:83], a[212:215], v[236:239], v[80:83]\n\t"
  "ds_read_b128 v[192:195], v18 offset:0\n\t"
  "ds_read_b128 v[196:199], v18 offset:2048\n\t"
  "v_mfma_f32_16x16x32_bf16 v[96:99], a[212:215], v[240:243], v[96:99]\n\t"
  "ds_read_b128 v[200:203], v18 offset:4096\n\t"
  "v_mfma_f32_16x16x32_bf16 v[112:115], a[216:219], v[224:227], v[112:115]\n\t"
  "ds_read_b128 v[204:207], v18 offset:6144\n\t"
  "v_mfma_f32_16x16x32_bf16 v[128:131], a[216:219], v[228:231], v[128:131]\n\t"
  "v_mfma_f32_16x16x32_bf16 v[144:147], a[216:219], v[232:235], v[144:147]\n\t"
  "ds_read_b128 v[208:211], v18 offset:8192\n\t"
  "v_mfma_f32_16x16x32_bf16 v[160:163], a[216:219], v[236:239], v[160:163]\n\t"
  "ds_read_b64_tr_b16 a[180:181], v22 offset:0\n\t"
  "v_mfma_f32_16x16x32_bf16 v[176:179], a[216:219], v[240:243], v[176:179]\n\t"
  "v_mfma_f32_16x16x32_bf16 v[36:39], a[220:223], v[224:227], v[36:39]\n\t"
  "ds_read_b64_tr_b16 a[182:183], v22 offset:4096\n\t"
  "v_mfma_f32_16x16x32_bf16 v[52:55], a[220:223], v[228:231], v[52:55]\n\t"
  "ds_read_b64_tr_b16 a[184:185], v22 offset:8\n\t"
  "v_mfma_f32_16x16x32_bf16 v[68:71], a[220:223], v[232:235], v[68:71]\n\t"
  "v_mfma_f32_16x16x32_bf16 v[84:87], a[220:223], v[236:239], v[84:87]\n\t"
  "ds_read_b64_tr_b16 a[186:187], v22 offset:4104\n\t"
  "v_mfma_f32_16x16x32_bf16 v[100:103], a[220:223], v[240:243], v[100:103]\n\t"
  "v_mfma_f32_16x16x32_bf16 v[116:119], a[224:227], v[224:227], v[116:119]\n\t"
  "ds_read_b64_tr_b16 a[188:189], v22 offset:512\n\t"
  "v_mfma_f32_16x16x32_bf16 v[132:135], a[224:227], v[228:231], v[132:135]\n\t"
  "ds_read_b64_tr_b16 a[190:191], v22 offset:4608\n\t"
  "v_mfma_f32_16x16x32_bf16 v[148:151], a[224:227], v[232:235], v[148:151]\n\t"
  "v_mfma_f32_16x16x32_bf16 v[164:167], a[224:227], v[236:239], v[164:167]\n\t"
  "ds_read_b64_tr_b16 a[192:193], v22 offset:520\n\t"
  "v_mfma_f32_16x16x32_bf16 v[180:183], a[224:227], v[240:243], v[180:183]\n\t"
  "ds_read_b64_tr_b16 a[194:195], v22 offset:4616\n\t"
  "v_mfma_f32_16x16x32_bf16 v[40:43], a[228:231], v[224:227], v[40:43]\n\t"
  "v_mfma_f32_16x16x32_bf16 v[56:59], a[228:231], v[228:231], v[56:59]\n\t"
  "ds_read_b64_tr_b16 a[196:197], v22 offset:1024\n\t"
  "s_waitcnt vmcnt(4)\n\t"
  "buffer_load_dwordx4 v213, s[28:31], s52 offen offset:1024 nt lds\n\t"
  "v_mfma_f32_16x16x32_bf16 v[72:75], a[228:231], v[232:235], v[72:75]\n\t"
  "v_mfma_f32_16x16x32_bf16 v[88:91], a[228:231], v[236:239], v[88:91]\n\t"
  "v_mfma_f32_16x16x32_bf16 v[104:107], a[228:231], v[240:243], v[104:107]\n\t"
  "ds_read_b64_tr_b16 a[198:199], v22 offset:5120\n\t"
  "ds_read_b64_tr_b16 a[200:201], v22 offset:1032\n\t"
  "v_mfma_f32_16x16x32_bf16 v[120:123], a[232:235], v[224:227], v[120:123]\n\t"
  "v_mfma_f32_16x16x32_bf16 v[136:139], a[232:235], v[228:231], v[136:139]\n\t"
  "ds_read_b64_tr_b16 a[202:203], v22 offset:5128\n\t"
  "v_mfma_f32_16x16x32_bf16 v[152:155], a[232:235], v[232:235], v[152:155]\n\t"
  "ds_read_b64_tr_b16 a[204:205], v22 offset:1536\n\t"
  "v_mfma_f32_16x16x32_bf16 v[168:171], a[232:235], v[236:239], v[168:171]\n\t"
  "v_mfma_f32_16x16x32_bf16 v[184:187], a[232:235], v[240:243], v[184:187]\n\t"
  "ds_read_b64_tr_b16 a[206:207], v22 offset:5632\n\t"
  "v_mfma_f32_16x16x32_bf16 v[44:47], a[236:239], v[224:227], v[44:47]\n\t"
  "v_mfma_f32_16x16x32_bf16 v[60:63], a[236:239], v[228:231], v[60:63]\n\t"
  "ds_read_b64_tr_b16 a[208:209], v22 offset:1544\n\t"
  "v_mfma_f32_16x16x32_bf16 v[76:79], a[236:239], v[232:235], v[76:79]\n\t"
  "ds_read_b64_tr_b16 a[210:211], v22 offset:5640\n\t"
  "v_mfma_f32_16x16x32_bf16 v[92:95], a[236:239], v[236:239], v[92:95]\n\t"
  "v_mfma_f32_16x16x32_bf16 v[108:111], a[236:239], v[240:243], v[108:111]\n\t"
  "v_mfma_f32_16x16x32_bf16 v[124:127], a[240:243], v[224:227], v[124:127]\n\t"
  "v_mfma_f32_16x16x32_bf16 v[140:143], a[240:243], v[228:231], v[140:143]\n\t"
  "v_mfma_f32_16x16x32_bf16 v[156:159], a[240:243], v[232:235], v[156:159]\n\t"
  "v_mfma_f32_16x16x32_bf16 v[172:175], a[240:243], v[236:239], v[172:175]\n\t"
  "v_mfma_f32_16x16x32_bf16 v[188:191], a[240:243], v[240:243], v[188:191]\n\t"
  "s_waitcnt lgkmcnt(0)\n\t"
  "v_mfma_f32_16x16x32_bf16 v[32:35], a[180:183], v[192:195], v[32:35]\n\t"
  "s_waitcnt vmcnt(4)\n\t"
  "buffer_load_dwordx4 v214, s[28:31], s52 offen offset:2048 nt lds\n\t"
  "v_mfma_f32_16x16x32_bf16 v[48:51], a[180:183], v[196:199], v[48:51]\n\t"
  "v_mfma_f32_16x16x32_bf16 v[64:67], a[180:183], v[200:203], v[64:67]\n\t"
  "ds_read_b128 v[224:227], v19 offset:0\n\t"
  "ds_read_b128 v[228:231], v19 offset:2048\n\t"
  "v_mfma_f32_16x16x32_bf16 v[80:83], a[180:183], v[204:207], v[80:83]\n\t"
  "ds_read_b128 v[232:235], v19 offset:4096\n\t"
  "v_mfma_f32_16x16x32_bf16 v[96:99], a[180:183], v[208:211], v[96:99]\n\t"
  "v_mfma_f32_16x16x32_bf16 v[112:115], a[184:187], v[192:195], v[112:115]\n\t"
  "ds_read_b128 v[236:239], v19 offset:6144\n\t"
  "v_mfma_f32_16x16x32_bf16 v[128:131], a[184:187], v[196:199], v[128:131]\n\t"
  "ds_read_b128 v[240:243], v19 offset:8192\n\t"
  "v_mfma_f32_16x16x32_bf16 v[144:147], a[184:187], v[200:203], v[144:147]\n\t"
  "v_mfma_f32_16x16x32_bf16 v[160:163], a[184:187], v[204:207], v[160:163]\n\t"
  "ds_read_b64_tr_b16 a[212:213], v22 offset:16384\n\t"
  "v_mfma_f32_16x16x32_bf16 v[176:179], a[184:187], v[208:211], v[176:179]\n\t"
  "v_mfma_f32_16x16x32_bf16 v[36:39], a[188:191], v[192:195], v[36:39]\n\t"
  "ds_read_b64_tr_b16 a[214:215], v22 offset:20480\n\t"
  "v_mfma_f32_16x16x32_bf16 v[52:55], a[188:191], v[196:199], v[52:55]\n\t"
  "ds_read_b64_tr_b16 a[216:217], v22 offset:16392\n\t"
  "v_mfma_f32_16x16x32_bf16 v[68:71], a[188:191], v[200:203], v[68:71]\n\t"
  "v_mfma_f32_16x16x32_bf16 v[84:87], a[188:191], v[204:207], v[84:87]\n\t"
  "ds_read_b64_tr_b16 a[218:219], v22 offset:20488\n\t"
  "v_mfma_f32_16x16x32_bf16 v[100:103], a[188:191], v[208:211], v[100:103]\n\t"
  "ds_read_b64_tr_b16 a[220:221], v22 offset:16896\n\t"
  "v_mfma_f32_16x16x32_bf16 v[116:119], a[192:195], v[192:195], v[116:119]\n\t"
  "v_mfma_f32_16x16x32_bf16 v[132:135], a[192:195], v[196:199], v[132:135]\n\t"
  "ds_read_b64_tr_b16 a[222:223], v22 offset:20992\n\t"
  "v_mfma_f32_16x16x32_bf16 v[148:151], a[192:195], v[200:203], v[148:151]\n\t"
  "ds_read_b64_tr_b16 a[224:225], v22 offset:16904\n\t"
  "v_mfma_f32_16x16x32_bf16 v[164:167], a[192:195], v[204:207], v[164:167]\n\t"
  "s_waitcnt vmcnt(4)\n\t"
  "buffer_load_dwordx4 v215, s[28:31], s52 offen offset:3072 nt lds\n\t"
  "v_mfma_f32_16x16x32_bf16 v[180:183], a[192:195], v[208:211], v[180:183]\n\t"
  "v_mfma_f32_16x16x32_bf16 v[40:43], a[196:199], v[192:195], v[40:43]\n\t"
  "ds_read_b64_tr_b16 a[226:227], v22 offset:21000\n\t"
  "v_mfma_f32_16x16x32_bf16 v[56:59], a[196:199], v[196:199], v[56:59]\n\t"
  "ds_read_b64_tr_b16 a[228:229], v22 offset:17408\n\t"
  "v_mfma_f32_16x16x32_bf16 v[72:75], a[196:199], v[200:203], v[72:75]\n\t"
  "ds_read_b64_tr_b16 a[230:231], v22 offset:21504\n\t"
  "v_mfma_f32_16x16x32_bf16 v[88:91], a[196:199], v[204:207], v[88:91]\n\t"
  "ds_read_b64_tr_b16 a[232:233], v22 offset:17416\n\t"
  "v_mfma_f32_16x16x32_bf16 v[104:107], a[196:199], v[208:211], v[104:107]\n\t"
  "v_mfma_f32_16x16x32_bf16 v[120:123], a[200:203], v[192:195], v[120:123]\n\t"
  "ds_read_b64_tr_b16 a[234:235], v22 offset:21512\n\t"
  "v_mfma_f32_16x16x32_bf16 v[136:139], a[200:203], v[196:199], v[136:139]\n\t"
  "v_mfma_f32_16x16x32_bf16 v[152:155], a[200:203], v[200:203], v[152:155]\n\t"
  "ds_read_b64_tr_b16 a[236:237], v22 offset:17920\n\t"
  "v_mfma_f32_16x16x32_bf16 v[168:171], a[200:203], v[204:207], v[168:171]\n\t"
  "ds_read_b64_tr_b16 a[238:239], v22 offset:22016\n\t"
  "v_mfma_f32_16x16x32_bf16 v[184:187], a[200:203], v[208:211], v[184:187]\n\t"
  "v_mfma_f32_16x16x32_bf16 v[44:47], a[204:207], v[192:195], v[44:47]\n\t"
  "ds_read_b64_tr_b16 a[240:241], v22 offset:17928\n\t"
  "v_mfma_f32_16x16x32_bf16 v[60:63], a[204:207], v[196:199], v[60:63]\n\t"
  "ds_read_b64_tr_b16 a[242:243], v22 offset:22024\n\t"
  "v_mfma_f32_16x16x32_bf16 v[76:79], a[204:207], v[200:203], v[76:79]\n\t"
  "v_mfma_f32_16x16x32_bf16 v[92:95], a[204:207], v[204:207], v[92:95]\n\t"
  "v_mfma_f32_16x16x32_bf16 v[108:111], a[204:207], v[208:211], v[108:111]\n\t"
  "v_mfma_f32_16x16x32_bf16 v[124:127], a[208:211], v[192:195], v[124:127]\n\t"
  "v_mfma_f32_16x16x32_bf16 v[140:143], a[208:211], v[196:199], v[140:143]\n\t"
  "v_mfma_f32_16x16x32_bf16 v[156:159], a[208:211], v[200:203], v[156:159]\n\t"
  "v_mfma_f32_16x16x32_bf16 v[172:175], a[208:211], v[204:207], v[172:175]\n\t"
  "v_mfma_f32_16x16x32_bf16 v[188:191], a[208:211], v[208:211], v[188:191]\n\t"
  "s_waitcnt vmcnt(10) lgkmcnt(0)\n\t"
  "s_barrier\n\t"
  "v_mfma_f32_16x16x32_bf16 v[32:35], a[212:215], v[224:227], v[32:35]\n\t"
  "s_waitcnt vmcnt(4)\n\t"
  "s_add_u32 m0, s51, 69632\n\t"
  "s_mov_b32 s52, 12288\n\t"
  "buffer_load_dwordx4 v212, s[28:31], s52 offen offset:0 nt lds\n\t"
  "v_mfma_f32_16x16x32_bf16 v[48:51], a[212:215], v[228:231], v[48:51]\n\t"
  "v_mfma_f32_16x16x32_bf16 v[64:67], a[212:215], v[232:235], v[64:67]\n\t"
  "v_mfma_f32_16x16x32_bf16 v[80:83], a[212:215], v[236:239], v[80:83]\n\t"
  "ds_read_b128 v[192:195], v16 offset:0\n\t"
  "ds_read_b128 v[196:199], v16 offset:2048\n\t"
  "v_mfma_f32_16x16x32_bf16 v[96:99], a[212:215], v[240:243], v[96:99]\n\t"
  "ds_read_b128 v[200:203], v16 offset:4096\n\t"
  "v_mfma_f32_16x16x32_bf16 v[112:115], a[216:219], v[224:227], v[112:115]\n\t"
  "ds_read_b128 v[204:207], v16 offset:6144\n\t"
  "v_mfma_f32_16x16x32_bf16 v[128:131], a[216:219], v[228:231], v[128:131]\n\t"
  "v_mfma_f32_16x16x32_bf16 v[144:147], a[216:219], v[232:235], v[144:147]\n\t"
  "ds_read_b128 v[208:211], v16 offset:8192\n\t"
  "v_mfma_f32_16x16x32_bf16 v[160:163], a[216:219], v[236:239], v[160:163]\n\t"
  "ds_read_b64_tr_b16 a[180:181], v20 offset:0\n\t"
  "v_mfma_f32_16x16x32_bf16 v[176:179], a[216:219], v[240:243], v[176:179]\n\t"
  "v_mfma_f32_16x16x32_bf16 v[36:39], a[220:223], v[224:227], v[36:39]\n\t"
  "ds_read_b64_tr_b16 a[182:183], v20 offset:4096\n\t"
  "v_mfma_f32_16x16x32_bf16 v[52:55], a[220:223], v[228:231], v[52:55]\n\t"
  "ds_read_b64_tr_b16 a[184:185], v20 offset:8\n\t"
  "v_mfma_f32_16x16x32_bf16 v[68:71], a[220:223], v[232:235], v[68:71]\n\t"
  "v_mfma_f32_16x16x32_bf16 v[84:87], a[220:223], v[236:239], v[84:87]\n\t"
  "ds_read_b64_tr_b16 a[186:187], v20 offset:4104\n\t"
  "v_mfma_f32_16x16x32_bf16 v[100:103], a[220:223], v[240:243], v[100:103]\n\t"
  "v_mfma_f32_16x16x32_bf16 v[116:119], a[224:227], v[224:227], v[116:119]\n\t"
  "ds_read_b64_tr_b16 a[188:189], v20 offset:512\n\t"
  "s_waitcnt vmcnt(4)\n\t"
  "buffer_load_dwordx4 v213, s[28:31], s52 offen offset:1024 nt lds\n\t"
  "v_mfma_f32_16x16x32_bf16 v[132:135], a[224:227], v[228:231], v[132:135]\n\t"
  "v_mfma_f32_16x16x32_bf16 v[148:151], a[224:227], v[232:235], v[148:151]\n\t"
  "v_mfma_f32_16x16x32_bf16 v[164:167], a[224:227], v[236:239], v[164:167]\n\t"
  "ds_read_b64_tr_b16 a[190:191], v20 offset:4608\n\t"
  "ds_read_b64_tr_b16 a[192:193], v20 offset:520\n\t"
  "v_mfma_f32_16x16x32_bf16 v[180:183], a[224:227], v[240:243], v[180:183]\n\t"
  "ds_read_b64_tr_b16 a[194:195], v20 offset:4616\n\t"
  "v_mfma_f32_16x16x32_bf16 v[40:43], a[228:231], v[224:227], v[40:43]\n\t"
  "v_mfma_f32_16x16x32_bf16 v[56:59], a[228:231], v[228:231], v[56:59]\n\t"
  "ds_read_b64_tr_b16 a[196:197], v20 offset:1024\n\t"
  "v_mfma_f32_16x16x32_bf16 v[72:75], a[228:231], v[232:235], v[72:75]\n\t"
  "v_mfma_f32_16x16x32_bf16 v[88:91], a[228:231], v[236:239], v[88:91]\n\t"
  "ds_read_b64_tr_b16 a[198:199], v20 offset:5120\n\t"
  "v_mfma_f32_16x16x32_bf16 v[104:107], a[228:231], v[240:243], v[104:107]\n\t"
  "ds_read_b64_tr_b16 a[200:201], v20 offset:1032\n\t"
  "v_mfma_f32_16x16x32_bf16 v[120:123], a[232:235], v[224:227], v[120:123]\n\t"
  "v_mfma_f32_16x16x32_bf16 v[136:139], a[232:235], v[228:231], v[136:139]\n\t"
  "ds_read_b64_tr_b16 a[202:203], v20 offset:5128\n\t"
  "s_waitcnt vmcnt(4)\n\t"
  "buffer_load_dwordx4 v214, s[28:31], s52 offen offset:2048 nt lds\n\t"
  "v_mfma_f32_16x16x32_bf16 v[152:155], a[232:235], v[232:235], v[152:155]\n\t"
  "v_mfma_f32_16x16x32_bf16 v[168:171], a[232:235], v[236:239], v[168:171]\n\t"
  "v_mfma_f32_16x16x32_bf16 v[184:187], a[232:235], v[240:243], v[184:187]\n\t"
  "ds_read_b64_tr_b16 a[204:205], v20 offset:1536\n\t"
  "ds_read_b64_tr_b16 a[206:207], v20 offset:5632\n\t"
  "v_mfma_f32_16x16x32_bf16 v[44:47], a[236:239], v[224:227], v[44:47]\n\t"
  "v_mfma_f32_16x16x32_bf16 v[60:63], a[236:239], v[228:231], v[60:63]\n\t"
  "ds_read_b64_tr_b16 a[208:209], v20 offset:1544\n\t"
  "v_mfma_f32_16x16x32_bf16 v[76:79], a[236:239], v[232:235], v[76:79]\n\t"
  "ds_read_b64_tr_b16 a[210:211], v20 offset:5640\n\t"
  "v_mfma_f32_16x16x32_bf16 v[92:95], a[236:239], v[236:239], v[92:95]\n\t"
  "v_mfma_f32_16x16x32_bf16 v[108:111], a[236:239], v[240:243], v[108:111]\n\t"
  "v_mfma_f32_16x16x32_bf16 v[124:127], a[240:243], v[224:227], v[124:127]\n\t"
  "v_mfma_f32_16x16x32_bf16 v[140:143], a[240:243], v[228:231], v[140:143]\n\t"
  "v_mfma_f32_16x16x32_bf16 v[156:159], a[240:243], v[232:235], v[156:159]\n\t"
  "v_mfma_f32_16x16x32_bf16 v[172:175], a[240:243], v[236:239], v[172:175]\n\t"
  "v_mfma_f32_16x16x32_bf16 v[188:191], a[240:243], v[240:243], v[188:191]\n\t"
  "s_waitcnt lgkmcnt(0)\n\t"
  "v_mfma_f32_16x16x32_bf16 v[32:35], a[180:183], v[192:195], v[32:35]\n\t"
  "s_waitcnt vmcnt(4)\n\t"
  "buffer_load_dwordx4 v215, s[28:31], s52 offen offset:3072 nt lds\n\t"
  "v_mfma_f32_16x16x32_bf16 v[48:51], a[180:183], v[196:199], v[48:51]\n\t"
  "v_mfma_f32_16x16x32_bf16 v[64:67], a[180:183], v[200:203], v[64:67]\n\t"
  "ds_read_b128 v[224:227], v17 offset:0\n\t"
  "ds_read_b128 v[228:231], v17 offset:2048\n\t"
  "v_mfma_f32_16x16x32_bf16 v[80:83], a[180:183], v[204:207], v[80:83]\n\t"
  "ds_read_b128 v[232:235], v17 offset:4096\n\t"
  "v_mfma_f32_16x16x32_bf16 v[96:99], a[180:183], v[208:211], v[96:99]\n\t"
  "v_mfma_f32_16x16x32_bf16 v[112:115], a[184:187], v[192:195], v[112:115]\n\t"
  "ds_read_b128 v[236:239], v17 offset:6144\n\t"
  "v_mfma_f32_16x16x32_bf16 v[128:131], a[184:187], v[196:199], v[128:131]\n\t"
  "ds_read_b128 v[240:243], v17 offset:8192\n\t"
  "v_mfma_f32_16x16x32_bf16 v[144:147], a[184:187], v[200:203], v[144:147]\n\t"
  "v_mfma_f32_16x16x32_bf16 v[160:163], a[184:187], v[204:207], v[160:163]\n\t"
  "ds_read_b64_tr_b16 a[212:213], v20 offset:16384\n\t"
  "v_mfma_f32_16x16x32_bf16 v[176:179], a[184:187], v[208:211], v[176:179]\n\t"
  "v_mfma_f32_16x16x32_bf16 v[36:39], a[188:191], v[192:195], v[36:39]\n\t"
  "ds_read_b64_tr_b16 a[214:215], v20 offset:20480\n\t"
  "s_waitcnt vmcnt(4)\n\t"
  "s_add_u32 m0, s51, 98304\n\t"
  "s_mov_b32 s52, 16384\n\t"
  "buffer_load_dwordx4 v212, s[28:31], s52 offen offset:0 nt lds\n\t"
  "v_mfma_f32_16x16x32_bf16 v[52:55], a[188:191], v[196:199], v[52:55]\n\t"
  "v_mfma_f32_16x16x32_bf16 v[68:71], a[188:191], v[200:203], v[68:71]\n\t"
  "v_mfma_f32_16x16x32_bf16 v[84:87], a[188:191], v[204:207], v[84:87]\n\t"
  "ds_read_b64_tr_b16 a[216:217], v20 offset:16392\n\t"
  "v_mfma_f32_16x16x32_bf16 v[100:103], a[188:191], v[208:211], v[100:103]\n\t"
  "ds_read_b64_tr_b16 a[218:219], v20 offset:20488\n\t"
  "ds_read_b64_tr_b16 a[220:221], v20 offset:16896\n\t"
  "v_mfma_f32_16x16x32_bf16 v[116:119], a[192:195], v[192:195], v[116:119]\n\t"
  "v_mfma_f32_16x16x32_bf16 v[132:135], a[192:195], v[196:199], v[132:135]\n\t"
  "ds_read_b64_tr_b16 a[222:223], v20 offset:20992\n\t"
  "v_mfma_f32_16x16x32_bf16 v[148:151], a[192:195], v[200:203], v[148:151]\n\t"
  "ds_read_b64_tr_b16 a[224:225], v20 offset:16904\n\t"
  "v_mfma_f32_16x16x32_bf16 v[164:167], a[192:195], v[204:207], v[164:167]\n\t"
  "v_mfma_f32_16x16x32_bf16 v[180:183], a[192:195], v[208:211], v[180:183]\n\t"
  "ds_read_b64_tr_b16 a[226:227], v20 offset:21000\n\t"
  "v_mfma_f32_16x16x32_bf16 v[40:43], a[196:199], v[192:195], v[40:43]\n\t"
  "ds_read_b64_tr_b16 a[228:229], v20 offset:17408\n\t"
  "v_mfma_f32_16x16x32_bf16 v[56:59], a[196:199], v[196:199], v[56:59]\n\t"
  "v_mfma_f32_16x16x32_bf16 v[72:75], a[196:199], v[200:203], v[72:75]\n\t"
  "ds_read_b64_tr_b16 a[230:231], v20 offset:21504\n\t"
  "v_mfma_f32_16x16x32_bf16 v[88:91], a[196:199], v[204:207], v[88:91]\n\t"
  "ds_read_b64_tr_b16 a[232:233], v20 offset:17416\n\t"
  "v_mfma_f32_16x16x32_bf16 v[104:107], a[196:199], v[208:211], v[104:107]\n\t"
  "v_mfma_f32_16x16x32_bf16 v[120:123], a[200:203], v[192:195], v[120:123]\n\t"
  "ds_read_b64_tr_b16 a[234:235], v20 offset:21512\n\t"
  "s_waitcnt vmcnt(4)\n\t"
  "buffer_load_dwordx4 v213, s[28:31], s52 offen offset:1024 nt lds\n\t"
  "v_mfma_f32_16x16x32_bf16 v[136:139], a[200:203], v[196:199], v[136:139]\n\t"
  "v_mfma_f32_16x16x32_bf16 v[152:155], a[200:203], v[200:203], v[152:155]\n\t"
  "v_mfma_f32_16x16x32_bf16 v[168:171], a[200:203], v[204:207], v[168:171]\n\t"
  "ds_read_b64_tr_b16 a[236:237], v20 offset:17920\n\t"
  "ds_read_b64_tr_b16 a[238:239], v20 offset:22016\n\t"
  "v_mfma_f32_16x16x32_bf16 v[184:187], a[200:203], v[208:211], v[184:187]\n\t"
  "v_mfma_f32_16x16x32_bf16 v[44:47], a[204:207], v[192:195], v[44:47]\n\t"
  "ds_read_b64_tr_b16 a[240:241], v20 offset:17928\n\t"
  "v_mfma_f32_16x16x32_bf16 v[60:63], a[204:207], v[196:199], v[60:63]\n\t"
  "ds_read_b64_tr_b16 a[242:243], v20 offset:22024\n\t"
  "v_mfma_f32_16x16x32_bf16 v[76:79], a[204:207], v[200:203], v[76:79]\n\t"
  "v_mfma_f32_16x16x32_bf16 v[92:95], a[204:207], v[204:207], v[92:95]\n\t"
  "v_mfma_f32_16x16x32_bf16 v[108:111], a[204:207], v[208:211], v[108:111]\n\t"
  "v_mfma_f32_16x16x32_bf16 v[124:127], a[208:211], v[192:195], v[124:127]\n\t"
  "v_mfma_f32_16x16x32_bf16 v[140:143], a[208:211], v[196:199], v[140:143]\n\t"
  "v_mfma_f32_16x16x32_bf16 v[156:159], a[208:211], v[200:203], v[156:159]\n\t"
  "v_mfma_f32_16x16x32_bf16 v[172:175], a[208:211], v[204:207], v[172:175]\n\t"
  "v_mfma_f32_16x16x32_bf16 v[188:191], a[208:211], v[208:211], v[188:191]\n\t"
  "s_waitcnt lgkmcnt(0)\n\t"
  "s_barrier\n\t"
  "v_mfma_f32_16x16x32_bf16 v[32:35], a[212:215], v[224:227], v[32:35]\n\t"
  "s_waitcnt vmcnt(4)\n\t"
  "buffer_load_dwordx4 v214, s[28:31], s52 offen offset:2048 nt lds\n\t"
  "v_mfma_f32_16x16x32_bf16 v[48:51], a[212:215], v[228:231], v[48:51]\n\t"
  "v_mfma_f32_16x16x32_bf16 v[64:67], a[212:215], v[232:235], v[64:67]\n\t"
  "s_waitcnt vmcnt(47)\n\t"
  "s_add_u32 s48, s51, 32768\n\t"
  "v_add_u32 v250, s48, v222\n\t"
  "v_mfma_f32_16x16x32_bf16 v[80:83], a[212:215], v[236:239], v[80:83]\n\t"
  "v_mfma_f32_16x16x32_bf16 v[96:99], a[212:215], v[240:243], v[96:99]\n\t"
  "v_mfma_f32_16x16x32_bf16 v[112:115], a[216:219], v[224:227], v[112:115]\n\t"
  "v_mfma_f32_16x16x32_bf16 v[128:131], a[216:219], v[228:231], v[128:131]\n\t"
  "v_mfma_f32_16x16x32_bf16 v[144:147], a[216:219], v[232:235], v[144:147]\n\t"
  "v_mfma_f32_16x16x32_bf16 v[160:163], a[216:219], v[236:239], v[160:163]\n\t"
  "ds_write_b128 v250, a[0:3] offset:0\n\t"
  "v_mfma_f32_16x16x32_bf16 v[176:179], a[216:219], v[240:243], v[176:179]\n\t"
  "v_mfma_f32_16x16x32_bf16 v[36:39], a[220:223], v[224:227], v[36:39]\n\t"
  "s_waitcnt vmcnt(4)\n\t"
  "buffer_load_dwordx4 v215, s[28:31], s52 offen offset:3072 nt lds\n\t"
  "v_mfma_f32_16x16x32_bf16 v[52:55], a[220:223], v[228:231], v[52:55]\n\t"
  "v_mfma_f32_16x16x32_bf16 v[68:71], a[220:223], v[232:235], v[68:71]\n\t"
  "v_mfma_f32_16x16x32_bf16 v[84:87], a[220:223], v[236:239], v[84:87]\n\t"
  "ds_write_b128 v250, a[4:7] offset:1024\n\t"
  "v_mfma_f32_16x16x32_bf16 v[100:103], a[220:223], v[240:243], v[100:103]\n\t"
  "v_mfma_f32_16x16x32_bf16 v[116:119], a[224:227], v[224:227], v[116:119]\n\t"
  "ds_write_b128 v250, a[8:11] offset:2048\n\t"
  "v_mfma_f32_16x16x32_bf16 v[132:135], a[224:227], v[228:231], v[132:135]\n\t"
  "v_mfma_f32_16x16x32_bf16 v[148:151], a[224:227], v[232:235], v[148:151]\n\t"
  "v_mfma_f32_16x16x32_bf16 v[164:167], a[224:227], v[236:239], v[164:167]\n\t"
  "ds_write_b128 v250, a[12:15] offset:3072\n\t"
  "v_mfma_f32_16x16x32_bf16 v[180:183], a[224:227], v[240:243], v[180:183]\n\t"
  "v_mfma_f32_16x16x32_bf16 v[40:43], a[228:231], v[224:227], v[40:43]\n\t"
  "v_mfma_f32_16x16x32_bf16 v[56:59], a[228:231], v[228:231], v[56:59]\n\t"
  "s_waitcnt vmcnt(4)\n\t"
  "s_add_u32 m0, s51, 102400\n\t"
  "s_mov_b32 s52, 20480\n\t"
  "buffer_load_dwordx4 v212, s[28:31], s52 offen offset:0 nt lds\n\t"
  "v_mfma_f32_16x16x32_bf16 v[72:75], a[228:231], v[232:235], v[72:75]\n\t"
  "v_mfma_f32_16x16x32_bf16 v[88:91], a[228:231], v[236:239], v[88:91]\n\t"
  "v_mfma_f32_16x16x32_bf16 v[104:107], a[228:231], v[240:243], v[104:107]\n\t"
  "v_mfma_f32_16x16x32_bf16 v[120:123], a[232:235], v[224:227], v[120:123]\n\t"
  "ds_write_b128 v250, a[16:19] offset:4096\n\t"
  "v_mfma_f32_16x16x32_bf16 v[136:139], a[232:235], v[228:231], v[136:139]\n\t"
  "ds_write_b128 v250, a[20:23] offset:5120\n\t"
  "v_mfma_f32_16x16x32_bf16 v[152:155], a[232:235], v[232:235], v[152:155]\n\t"
  "v_mfma_f32_16x16x32_bf16 v[168:171], a[232:235], v[236:239], v[168:171]\n\t"
  "v_mfma_f32_16x16x32_bf16 v[184:187], a[232:235], v[240:243], v[184:187]\n\t"
  "ds_write_b128 v250, a[24:27] offset:6144\n\t"
  "v_mfma_f32_16x16x32_bf16 v[44:47], a[236:239], v[224:227], v[44:47]\n\t"
  "v_mfma_f32_16x16x32_bf16 v[60:63], a[236:239], v[228:231], v[60:63]\n\t"
  "v_mfma_f32_16x16x32_bf16 v[76:79], a[236:239], v[232:235], v[76:79]\n\t"
  "v_mfma_f32_16x16x32_bf16 v[92:95], a[236:239], v[236:239], v[92:95]\n\t"
  "ds_write_b128 v250, a[28:31] offset:7168\n\t"
  "v_mfma_f32_16x16x32_bf16 v[108:111], a[236:239], v[240:243], v[108:111]\n\t"
  "v_mfma_f32_16x16x32_bf16 v[124:127], a[240:243], v[224:227], v[124:127]\n\t"
  "v_mfma_f32_16x16x32_bf16 v[140:143], a[240:243], v[228:231], v[140:143]\n\t"
  "v_mfma_f32_16x16x32_bf16 v[156:159], a[240:243], v[232:235], v[156:159]\n\t"
  "v_mfma_f32_16x16x32_bf16 v[172:175], a[240:243], v[236:239], v[172:175]\n\t"
  "v_mfma_f32_16x16x32_bf16 v[188:191], a[240:243], v[240:243], v[188:191]\n\t"
  "v_mul_f32 v32, s50, v32\n\t"
  "v_mul_f32 v33, s50, v33\n\t"
  "v_mul_f32 v34, s50, v34\n\t"
  "v_mul_f32 v35, s50, v35\n\t"
  "v_mul_f32 v112, s50, v112\n\t"
  "v_mul_f32 v113, s50, v113\n\t"
  "v_mul_f32 v114, s50, v114\n\t"
  "v_mul_f32 v115, s50, v115\n\t"
  "v_cvt_pk_bf16_f32 v32, v32, v33\n\t"
  "v_cvt_pk_bf16_f32 v33, v34, v35\n\t"
  "v_cvt_pk_bf16_f32 v34, v112, v113\n\t"
  "v_cvt_pk_bf16_f32 v35, v114, v115\n\t"
  "v_mul_f32 v36, s50, v36\n\t"
  "v_mul_f32 v37, s50, v37\n\t"
  "v_mul_f32 v38, s50, v38\n\t"
  "v_mul_f32 v39, s50, v39\n\t"
  "v_mul_f32 v116, s50, v116\n\t"
  "v_mul_f32 v117, s50, v117\n\t"
  "v_mul_f32 v118, s50, v118\n\t"
  "v_mul_f32 v119, s50, v119\n\t"
  "v_cvt_pk_bf16_f32 v36, v36, v37\n\t"
  "v_cvt_pk_bf16_f32 v37, v38, v39\n\t"
  "v_cvt_pk_bf16_f32 v38, v116, v117\n\t"
  "v_cvt_pk_bf16_f32 v39, v118, v119\n\t"
  "v_mul_f32 v40, s50, v40\n\t"
  "v_mul_f32 v41, s50, v41\n\t"
  "v_mul_f32 v42, s50, v42\n\t"
  "v_mul_f32 v43, s50, v43\n\t"
  "v_mul_f32 v120, s50, v120\n\t"
  "v_mul_f32 v121, s50, v121\n\t"
  "v_mul_f32 v122, s50, v122\n\t"
  "v_mul_f32 v123, s50, v123\n\t"
  "v_cvt_pk_bf16_f32 v40, v40, v41\n\t"
  "v_cvt_pk_bf16_f32 v41, v42, v43\n\t"
  "v_cvt_pk_bf16_f32 v42, v120, v121\n\t"
  "v_cvt_pk_bf16_f32 v43, v122, v123\n\t"
  "v_mul_f32 v44, s50, v44\n\t"
  "v_mul_f32 v45, s50, v45\n\t"
  "v_mul_f32 v46, s50, v46\n\t"
  "v_mul_f32 v47, s50, v47\n\t"
  "v_mul_f32 v124, s50, v124\n\t"
  "v_mul_f32 v125, s50, v125\n\t"
  "v_mul_f32 v126, s50, v126\n\t"
  "v_mul_f32 v127, s50, v127\n\t"
  "v_cvt_pk_bf16_f32 v44, v44, v45\n\t"
  "v_cvt_pk_bf16_f32 v45, v46, v47\n\t"
  "v_cvt_pk_bf16_f32 v46, v124, v125\n\t"
  "v_cvt_pk_bf16_f32 v47, v126, v127\n\t"
  "s_waitcnt vmcnt(13) lgkmcnt(0)\n\t"
  "s_barrier\n\t"
  "v_mfma_f32_16x16x32_bf16 v[112:115], a[128:131], v[32:35], 0\n\t"
  "s_waitcnt vmcnt(4)\n\t"
  "buffer_load_dwordx4 v213, s[28:31], s52 offen offset:1024 nt lds\n\t"
  "s_waitcnt vmcnt(4)\n\t"
  "buffer_load_dwordx4 v214, s[28:31], s52 offen offset:2048 nt lds\n\t"
  "v_mul_f32 v48, s50, v48\n\t"
  "v_mul_f32 v49, s50, v49\n\t"
  "v_mul_f32 v50, s50, v50\n\t"
  "v_mul_f32 v51, s50, v51\n\t"
  "v_mul_f32 v128, s50, v128\n\t"
  "v_mfma_f32_16x16x32_bf16 v[116:119], a[144:147], v[32:35], 0\n\t"
  "v_mul_f32 v129, s50, v129\n\t"
  "v_mul_f32 v130, s50, v130\n\t"
  "v_mul_f32 v131, s50, v131\n\t"
  "v_cvt_pk_bf16_f32 v48, v48, v49\n\t"
  "v_cvt_pk_bf16_f32 v49, v50, v51\n\t"
  "v_cvt_pk_bf16_f32 v50, v128, v129\n\t"
  "v_cvt_pk_bf16_f32 v51, v130, v131\n\t"
  "v_mfma_f32_16x16x32_bf16 v[112:115], a[132:135], v[36:39], v[112:115]\n\t"
  "v_mul_f32 v52, s50, v52\n\t"
  "v_mul_f32 v53, s50, v53\n\t"
  "v_mul_f32 v54, s50, v54\n\t"
  "v_mul_f32 v55, s50, v55\n\t"
  "v_mul_f32 v132, s50, v132\n\t"
  "v_mul_f32 v133, s50, v133\n\t"
  "v_mul_f32 v134, s50, v134\n\t"
  "v_mfma_f32_16x16x32_bf16 v[116:119], a[148:151], v[36:39], v[116:119]\n\t"
  "v_mul_f32 v135, s50, v135\n\t"
  "v_cvt_pk_bf16_f32 v52, v52, v53\n\t"
  "v_cvt_pk_bf16_f32 v53, v54, v55\n\t"
  "v_cvt_pk_bf16_f32 v54, v132, v133\n\t"
  "v_cvt_pk_bf16_f32 v55, v134, v135\n\t"
  "v_mul_f32 v56, s50, v56\n\t"
  "v_mul_f32 v57, s50, v57\n\t"
  "v_mul_f32 v58, s50, v58\n\t"
  "v_mfma_f32_16x16x32_bf16 v[112:115], a[136:139], v[40:43], v[112:115]\n\t"
  "v_mul_f32 v59, s50, v59\n\t"
  "v_mul_f32 v136, s50, v136\n\t"
  "v_mul_f32 v137, s50, v137\n\t"
  "v_mul_f32 v138, s50, v138\n\t"
  "v_mul_f32 v139, s50, v139\n\t"
  "v_cvt_pk_bf16_f32 v56, v56, v57\n\t"
  "v_cvt_pk_bf16_f32 v57, v58, v59\n\t"
  "v_mfma_f32_16x16x32_bf16 v[116:119], a[152:155], v[40:43], v[116:119]\n\t"
  "v_cvt_pk_bf16_f32 v58, v136, v137\n\t"
  "v_cvt_pk_bf16_f32 v59, v138, v139\n\t"
  "v_mul_f32 v60, s50, v60\n\t"
  "v_mul_f32 v61, s50, v61\n\t"
  "v_mul_f32 v62, s50, v62\n\t"
  "v_mul_f32 v63, s50, v63\n\t"
  "v_mul_f32 v140, s50, v140\n\t"
  "v_mfma_f32_16x16x32_bf16 v[112:115], a[140:143], v[44:47], v[112:115]\n\t"
  "v_mul_f32 v141, s50, v141\n\t"
  "v_mul_f32 v142, s50, v142\n\t"
  "v_mul_f32 v143, s50, v143\n\t"
  "v_cvt_pk_bf16_f32 v60, v60, v61\n\t"
  "v_cvt_pk_bf16_f32 v61, v62, v63\n\t"
  "v_cvt_pk_bf16_f32 v62, v140, v141\n\t"
  "v_cvt_pk_bf16_f32 v63, v142, v143\n\t"
  "v_mfma_f32_16x16x32_bf16 v[116:119], a[156:159], v[44:47], v[116:119]\n\t"
  "ds_read_b128 a[180:183], v216 offset:32768\n\t"
  "ds_read_b128 a[184:187], v217 offset:32768\n\t"
  "ds_read_b128 a[188:191], v218 offset:32768\n\t"
  "ds_read_b128 a[192:195], v219 offset:32768\n\t"
  "ds_read_b128 a[196:199], v216 offset:36864\n\t"
  "ds_read_b128 a[200:203], v217 offset:36864\n\t"
  "ds_read_b128 a[204:207], v218 offset:36864\n\t"
  "ds_read_b128 a[208:211], v219 offset:36864\n\t"
  "v_mfma_f32_16x16x32_bf16 v[120:123], a[128:131], v[48:51], 0\n\t"
  "s_waitcnt vmcnt(4)\n\t"
  "buffer_load_dwordx4 v215, s[28:31], s52 offen offset:3072 nt lds\n\t"
  "s_waitcnt vmcnt(4)\n\t"
  "s_add_u32 m0, s51, 131072\n\t"
  "s_mov_b32 s52, 24576\n\t"
  "buffer_load_dwordx4 v212, s[28:31], s52 offen offset:0 nt lds\n\t"
  "v_mul_f32 v64, s50, v64\n\t"
  "v_mul_f32 v65, s50, v65\n\t"
  "v_mul_f32 v66, s50, v66\n\t"
  "v_mul_f32 v67, s50, v67\n\t"
  "v_mul_f32 v144, s50, v144\n\t"
  "v_mul_f32 v145, s50, v145\n\t"
  "v_mul_f32 v146, s50, v146\n\t"
  "v_mul_f32 v147, s50, v147\n\t"
  "v_mfma_f32_16x16x32_bf16 v[124:127], a[144:147], v[48:51], 0\n\t"
  "v_cvt_pk_bf16_f32 v64, v64, v65\n\t"
  "v_cvt_pk_bf16_f32 v65, v66, v67\n\t"
  "v_cvt_pk_bf16_f32 v66, v144, v145\n\t"
  "v_cvt_pk_bf16_f32 v67, v146, v147\n\t"
  "v_mul_f32 v68, s50, v68\n\t"
  "v_mul_f32 v69, s50, v69\n\t"
  "v_mul_f32 v70, s50, v70\n\t"
  "v_mul_f32 v71, s50, v71\n\t"
  "v_mul_f32 v148, s50, v148\n\t"
  "v_mul_f32 v149, s50, v149\n\t"
  "v_mfma_f32_16x16x32_bf16 v[120:123], a[132:135], v[52:55], v[120:123]\n\t"
  "v_mul_f32 v150, s50, v150\n\t"
  "v_mul_f32 v151, s50, v151\n\t"
  "v_cvt_pk_bf16_f32 v68, v68, v69\n\t"
  "v_cvt_pk_bf16_f32 v69, v70, v71\n\t"
  "v_cvt_pk_bf16_f32 v70, v148, v149\n\t"
  "v_cvt_pk_bf16_f32 v71, v150, v151\n\t"
  "v_mul_f32 v72, s50, v72\n\t"
  "v_mul_f32 v73, s50, v73\n\t"
  "v_mul_f32 v74, s50, v74\n\t"
  "v_mul_f32 v75, s50, v75\n\t"
  "v_mfma_f32_16x16x32_bf16 v[124:127], a[148:151], v[52:55], v[124:127]\n\t"
  "v_mul_f32 v152, s50, v152\n\t"
  "v_mul_f32 v153, s50, v153\n\t"
  "v_mul_f32 v154, s50, v154\n\t"
  "v_mul_f32 v155, s50, v155\n\t"
  "v_cvt_pk_bf16_f32 v72, v72, v73\n\t"
  "v_cvt_pk_bf16_f32 v73, v74, v75\n\t"
  "v_cvt_pk_bf16_f32 v74, v152, v153\n\t"
  "v_cvt_pk_bf16_f32 v75, v154, v155\n\t"
  "v_mul_f32 v76, s50, v76\n\t"
  "v_mul_f32 v77, s50, v77\n\t"
  "v_mfma_f32_16x16x32_bf16 v[120:123], a[136:139], v[56:59], v[120:123]\n\t"
  "v_mul_f32 v78, s50, v78\n\t"
  "v_mul_f32 v79, s50, v79\n\t"
  "v_mul_f32 v156, s50, v156\n\t"
  "v_mul_f32 v157, s50, v157\n\t"
  "v_mul_f32 v158, s50, v158\n\t"
  "v_mul_f32 v159, s50, v159\n\t"
  "v_cvt_pk_bf16_f32 v76, v76, v77\n\t"
  "v_cvt_pk_bf16_f32 v77, v78, v79\n\t"
  "v_cvt_pk_bf16_f32 v78, v156, v157\n\t"
  "v_cvt_pk_bf16_f32 v79, v158, v159\n\t"
  "v_mfma_f32_16x16x32_bf16 v[124:127], a[152:155], v[56:59], v[124:127]\n\t"
  "v_max3_f32 v192, v112, v113, v114\n\t"
  "v_max3_f32 v193, v115, v116, v117\n\t"
  "v_max3_f32 v192, v192, v118, v119\n\t"
  "v_max_f32 v192, v192, v193\n\t"
  "v_mov_b32 v193, v192\n\t"
  "s_nop 1\n\t"
  "v_permlane32_swap_b32 v192, v193\n\t"
  "v_max_f32 v192, v192, v193\n\t"
  "v_mov_b32 v193, v192\n\t"
  "s_nop 1\n\t"
  "v_mfma_f32_16x16x32_bf16 v[120:123], a[140:143], v[60:63], v[120:123]\n\t"
  "v_permlane16_swap_b32 v192, v193\n\t"
  "v_max_f32 v192, v192, v193\n\t"
  "v_sub_f32 v192, 0, v192\n\t"
  "v_mov_b32 v193, v192\n\t"
  "v_mov_b32 v194, v192\n\t"
  "v_mov_b32 v195, v192\n\t"
  "v_add_f32 v112, v112, v192\n\t"
  "v_add_f32 v113, v113, v192\n\t"
  "v_add_f32 v114, v114, v192\n\t"
  "v_add_f32 v115, v115, v192\n\t"
  "v_mfma_f32_16x16x32_bf16 v[124:127], a[156:159], v[60:63], v[124:127]\n\t"
  "v_add_f32 v116, v116, v192\n\t"
  "v_add_f32 v117, v117, v192\n\t"
  "v_add_f32 v118, v118, v192\n\t"
  "v_add_f32 v119, v119, v192\n\t"
  "v_exp_f32 v112, v112\n\t"
  "v_exp_f32 v113, v113\n\t"
  "v_exp_f32 v114, v114\n\t"
  "v_cvt_pk_bf16_f32 v112, v112, v113\n\t"
  "v_exp_f32 v115, v115\n\t"
  "s_nop 0\n\t"
  "v_cvt_pk_bf16_f32 v113, v114, v115\n\t"
  "v_mfma_f32_16x16x32_bf16 v[128:131], a[128:131], v[64:67], 0\n\t"
  "s_waitcnt vmcnt(4)\n\t"
  "buffer_load_dwordx4 v213, s[28:31], s52 offen offset:1024 nt lds\n\t"
  "s_waitcnt vmcnt(4)\n\t"
  "buffer_load_dwordx4 v214, s[28:31], s52 offen offset:2048 nt lds\n\t"
  "v_mul_f32 v80, s50, v80\n\t"
  "v_mul_f32 v81, s50, v81\n\t"
  "v_mul_f32 v82, s50, v82\n\t"
  "v_mul_f32 v83, s50, v83\n\t"
  "v_mul_f32 v160, s50, v160\n\t"
  "v_mul_f32 v161, s50, v161\n\t"
  "v_mul_f32 v162, s50, v162\n\t"
  "v_mul_f32 v163, s50, v163\n\t"
  "v_mfma_f32_16x16x32_bf16 v[132:135], a[144:147], v[64:67], 0\n\t"
  "v_cvt_pk_bf16_f32 v80, v80, v81\n\t"
  "v_cvt_pk_bf16_f32 v81, v82, v83\n\t"
  "v_cvt_pk_bf16_f32 v82, v160, v161\n\t"
  "v_cvt_pk_bf16_f32 v83, v162, v163\n\t"
  "v_mul_f32 v84, s50, v84\n\t"
  "v_mul_f32 v85, s50, v85\n\t"
  "v_mul_f32 v86, s50, v86\n\t"
  "v_mul_f32 v87, s50, v87\n\t"
  "v_mul_f32 v164, s50, v164\n\t"
  "v_mul_f32 v165, s50, v165\n\t"
  "v_mfma_f32_16x16x32_bf16 v[128:131], a[132:135], v[68:71], v[128:131]\n\t"
  "v_mul_f32 v166, s50, v166\n\t"
  "v_mul_f32 v167, s50, v167\n\t"
  "v_cvt_pk_bf16_f32 v84, v84, v85\n\t"
  "v_cvt_pk_bf16_f32 v85, v86, v87\n\t"
  "v_cvt_pk_bf16_f32 v86, v164, v165\n\t"
  "v_cvt_pk_bf16_f32 v87, v166, v167\n\t"
  "v_mul_f32 v88, s50, v88\n\t"
  "v_mul_f32 v89, s50, v89\n\t"
  "v_mul_f32 v90, s50, v90\n\t"
  "v_mul_f32 v91, s50, v91\n\t"
  "v_mfma_f32_16x16x32_bf16 v[132:135], a[148:151], v[68:71], v[132:135]\n\t"
  "v_mul_f32 v168, s50, v168\n\t"
  "v_mul_f32 v169, s50, v169\n\t"
  "v_mul_f32 v170, s50, v170\n\t"
  "v_mul_f32 v171, s50, v171\n\t"
  "v_cvt_pk_bf16_f32 v88, v88, v89\n\t"
  "v_cvt_pk_bf16_f32 v89, v90, v91\n\t"
  "v_cvt_pk_bf16_f32 v90, v168, v169\n\t"
  "v_cvt_pk_bf16_f32 v91, v170, v171\n\t"
  "v_mul_f32 v92, s50, v92\n\t"
  "v_mul_f32 v93, s50, v93\n\t"
  "v_mfma_f32_16x16x32_bf16 v[128:131], a[136:139], v[72:75], v[128:131]\n\t"
  "v_mul_f32 v94, s50, v94\n\t"
  "v_mul_f32 v95, s50, v95\n\t"
  "v_mul_f32 v172, s50, v172\n\t"
  "v_mul_f32 v173, s50, v173\n\t"
  "v_mul_f32 v174, s50, v174\n\t"
  "v_mul_f32 v175, s50, v175\n\t"
  "v_cvt_pk_bf16_f32 v92, v92, v93\n\t"
  "v_cvt_pk_bf16_f32 v93, v94, v95\n\t"
  "v_cvt_pk_bf16_f32 v94, v172, v173\n\t"
  "v_cvt_pk_bf16_f32 v95, v174, v175\n\t"
  "v_mfma_f32_16x16x32_bf16 v[132:135], a[152:155], v[72:75], v[132:135]\n\t"
  "v_max3_f32 v196, v120, v121, v122\n\t"
  "v_max3_f32 v197, v123, v124, v125\n\t"
  "v_max3_f32 v196, v196, v126, v127\n\t"
  "v_max_f32 v196, v196, v197\n\t"
  "v_mov_b32 v197, v196\n\t"
  "s_nop 1\n\t"
  "v_permlane32_swap_b32 v196, v197\n\t"
  "v_max_f32 v196, v196, v197\n\t"
  "v_mov_b32 v197, v196\n\t"
  "s_nop 1\n\t"
  "v_mfma_f32_16x16x32_bf16 v[128:131], a[140:143], v[76:79], v[128:131]\n\t"
  "v_permlane16_swap_b32 v196, v197\n\t"
  "v_max_f32 v196, v196, v197\n\t"
  "v_sub_f32 v196, 0, v196\n\t"
  "v_mov_b32 v197, v196\n\t"
  "v_mov_b32 v198, v196\n\t"
  "v_mov_b32 v199, v196\n\t"
  "v_add_f32 v120, v120, v196\n\t"
  "v_add_f32 v121, v121, v196\n\t"
  "v_add_f32 v122, v122, v196\n\t"
  "v_add_f32 v123, v123, v196\n\t"
  "v_mfma_f32_16x16x32_bf16 v[132:135], a[156:159], v[76:79], v[132:135]\n\t"
  "v_add_f32 v124, v124, v196\n\t"
  "v_add_f32 v125, v125, v196\n\t"
  "v_add_f32 v126, v126, v196\n\t"
  "v_add_f32 v127, v127, v196\n\t"
  "v_exp_f32 v120, v120\n\t"
  "v_exp_f32 v121, v121\n\t"
  "v_exp_f32 v122, v122\n\t"
  "v_cvt_pk_bf16_f32 v120, v120, v121\n\t"
  "v_exp_f32 v123, v123\n\t"
  "s_nop 0\n\t"
  "v_cvt_pk_bf16_f32 v121, v122, v123\n\t"
  "v_mfma_f32_16x16x32_bf16 v[136:139], a[128:131], v[80:83], 0\n\t"
  "s_waitcnt vmcnt(4)\n\t"
  "buffer_load_dwordx4 v215, s[28:31], s52 offen offset:3072 nt lds\n\t"
  "s_waitcnt vmcnt(4)\n\t"
  "s_add_u32 m0, s51, 135168\n\t"
  "s_mov_b32 s52, 28672\n\t"
  "buffer_load_dwordx4 v212, s[28:31], s52 offen offset:0 nt lds\n\t"
  "v_mul_f32 v96, s50, v96\n\t"
  "v_mul_f32 v97, s50, v97\n\t"
  "v_mul_f32 v98, s50, v98\n\t"
  "v_mul_f32 v99, s50, v99\n\t"
  "v_mul_f32 v176, s50, v176\n\t"
  "v_mul_f32 v177, s50, v177\n\t"
  "v_mul_f32 v178, s50, v178\n\t"
  "v_mul_f32 v179, s50, v179\n\t"
  "v_mfma_f32_16x16x32_bf16 v[140:143], a[144:147], v[80:83], 0\n\t"
  "v_cvt_pk_bf16_f32 v96, v96, v97\n\t"
  "v_cvt_pk_bf16_f32 v97, v98, v99\n\t"
  "v_cvt_pk_bf16_f32 v98, v176, v177\n\t"
  "v_cvt_pk_bf16_f32 v99, v178, v179\n\t"
  "v_mul_f32 v100, s50, v100\n\t"
  "v_mul_f32 v101, s50, v101\n\t"
  "v_mul_f32 v102, s50, v102\n\t"
  "v_mul_f32 v103, s50, v103\n\t"
  "v_mul_f32 v180, s50, v180\n\t"
  "v_mul_f32 v181, s50, v181\n\t"
  "v_mfma_f32_16x16x32_bf16 v[136:139], a[132:135], v[84:87], v[136:139]\n\t"
  "v_mul_f32 v182, s50, v182\n\t"
  "v_mul_f32 v183, s50, v183\n\t"
  "v_cvt_pk_bf16_f32 v100, v100, v101\n\t"
  "v_cvt_pk_bf16_f32 v101, v102, v103\n\t"
  "v_cvt_pk_bf16_f32 v102, v180, v181\n\t"
  "v_cvt_pk_bf16_f32 v103, v182, v183\n\t"
  "v_mul_f32 v104, s50, v104\n\t"
  "v_mul_f32 v105, s50, v105\n\t"
  "v_mul_f32 v106, s50, v106\n\t"
  "v_mul_f32 v107, s50, v107\n\t"
  "v_mfma_f32_16x16x32_bf16 v[140:143], a[148:151], v[84:87], v[140:143]\n\t"
  "v_mul_f32 v184, s50, v184\n\t"
  "v_mul_f32 v185, s50, v185\n\t"
  "v_mul_f32 v186, s50, v186\n\t"
  "v_mul_f32 v187, s50, v187\n\t"
  "v_cvt_pk_bf16_f32 v104, v104, v105\n\t"
  "v_cvt_pk_bf16_f32 v105, v106, v107\n\t"
  "v_cvt_pk_bf16_f32 v106, v184, v185\n\t"
  "v_cvt_pk_bf16_f32 v107, v186, v187\n\t"
  "v_mul_f32 v108, s50, v108\n\t"
  "v_mul_f32 v109, s50, v109\n\t"
  "v_mfma_f32_16x16x32_bf16 v[136:139], a[136:139], v[88:91], v[136:139]\n\t"
  "v_mul_f32 v110, s50, v110\n\t"
  "v_mul_f32 v111, s50, v111\n\t"
  "v_mul_f32 v188, s50, v188\n\t"
  "v_mul_f32 v189, s50, v189\n\t"
  "v_mul_f32 v190, s50, v190\n\t"
  "v_mul_f32 v191, s50, v191\n\t"
  "v_cvt_pk_bf16_f32 v108, v108, v109\n\t"
  "v_cvt_pk_bf16_f32 v109, v110, v111\n\t"
  "v_cvt_pk_bf16_f32 v110, v188, v189\n\t"
  "v_cvt_pk_bf16_f32 v111, v190, v191\n\t"
  "v_mfma_f32_16x16x32_bf16 v[140:143], a[152:155], v[88:91], v[140:143]\n\t"
  "v_max3_f32 v200, v128, v129, v130\n\t"
  "v_max3_f32 v201, v131, v132, v133\n\t"
  "v_max3_f32 v200, v200, v134, v135\n\t"
  "v_max_f32 v200, v200, v201\n\t"
  "v_mov_b32 v201, v200\n\t"
  "s_nop 1\n\t"
  "v_permlane32_swap_b32 v200, v201\n\t"
  "v_max_f32 v200, v200, v201\n\t"
  "v_mov_b32 v201, v200\n\t"
  "s_nop 1\n\t"
  "v_mfma_f32_16x16x32_bf16 v[136:139], a[140:143], v[92:95], v[136:139]\n\t"
  "v_permlane16_swap_b32 v200, v201\n\t"
  "v_max_f32 v200, v200, v201\n\t"
  "v_sub_f32 v200, 0, v200\n\t"
  "v_mov_b32 v201, v200\n\t"
  "v_mov_b32 v202, v200\n\t"
  "v_mov_b32 v203, v200\n\t"
  "v_add_f32 v128, v128, v200\n\t"
  "v_add_f32 v129, v129, v200\n\t"
  "v_add_f32 v130, v130, v200\n\t"
  "v_add_f32 v131, v131, v200\n\t"
  "v_mfma_f32_16x16x32_bf16 v[140:143], a[156:159], v[92:95], v[140:143]\n\t"
  "v_add_f32 v132, v132, v200\n\t"
  "v_add_f32 v133, v133, v200\n\t"
  "v_add_f32 v134, v134, v200\n\t"
  "v_add_f32 v135, v135, v200\n\t"
  "v_exp_f32 v128, v128\n\t"
  "v_exp_f32 v129, v129\n\t"
  "v_exp_f32 v130, v130\n\t"
  "v_cvt_pk_bf16_f32 v128, v128, v129\n\t"
  "v_exp_f32 v131, v131\n\t"
  "s_nop 0\n\t"
  "v_cvt_pk_bf16_f32 v129, v130, v131\n\t"
  "v_mfma_f32_16x16x32_bf16 v[144:147], a[128:131], v[96:99], 0\n\t"
  "s_waitcnt vmcnt(4)\n\t"
  "buffer_load_dwordx4 v213, s[28:31], s52 offen offset:1024 nt lds\n\t"
  "s_waitcnt vmcnt(4)\n\t"
  "buffer_load_dwordx4 v214, s[28:31], s52 offen offset:2048 nt lds\n\t"
  "v_max3_f32 v204, v136, v137, v138\n\t"
  "v_max3_f32 v205, v139, v140, v141\n\t"
  "v_mfma_f32_16x16x32_bf16 v[148:151], a[144:147], v[96:99], 0\n\t"
  "v_max3_f32 v204, v204, v142, v143\n\t"
  "v_max_f32 v204, v204, v205\n\t"
  "v_mov_b32 v205, v204\n\t"
  "s_nop 1\n\t"
  "v_mfma_f32_16x16x32_bf16 v[144:147], a[132:135], v[100:103], v[144:147]\n\t"
  "v_permlane32_swap_b32 v204, v205\n\t"
  "v_max_f32 v204, v204, v205\n\t"
  "v_mov_b32 v205, v204\n\t"
  "s_nop 1\n\t"
  "v_mfma_f32_16x16x32_bf16 v[148:151], a[148:151], v[100:103], v[148:151]\n\t"
  "v_permlane16_swap_b32 v204, v205\n\t"
  "v_max_f32 v204, v204, v205\n\t"
  "v_sub_f32 v204, 0, v204\n\t"
  "v_mov_b32 v205, v204\n\t"
  "v_mfma_f32_16x16x32_bf16 v[144:147], a[136:139], v[104:107], v[144:147]\n\t"
  "v_mov_b32 v206, v204\n\t"
  "v_mov_b32 v207, v204\n\t"
  "v_add_f32 v136, v136, v204\n\t"
  "v_add_f32 v137, v137, v204\n\t"
  "v_mfma_f32_16x16x32_bf16 v[148:151], a[152:155], v[104:107], v[148:151]\n\t"
  "v_add_f32 v138, v138, v204\n\t"
  "v_add_f32 v139, v139, v204\n\t"
  "v_add_f32 v140, v140, v204\n\t"
  "v_add_f32 v141, v141, v204\n\t"
  "v_mfma_f32_16x16x32_bf16 v[144:147], a[140:143], v[108:111], v[144:147]\n\t"
  "v_add_f32 v142, v142, v204\n\t"
  "v_add_f32 v143, v143, v204\n\t"
  "v_exp_f32 v136, v136\n\t"
  "v_exp_f32 v137, v137\n\t"
  "v_mfma_f32_16x16x32_bf16 v[148:151], a[156:159], v[108:111], v[148:151]\n\t"
  "v_exp_f32 v138, v138\n\t"
  "v_cvt_pk_bf16_f32 v136, v136, v137\n\t"
  "v_exp_f32 v139, v139\n\t"
  "s_nop 0\n\t"
  "v_cvt_pk_bf16_f32 v137, v138, v139\n\t"
  "s_nop 3\n\t"
  "v_max3_f32 v208, v144, v145, v146\n\t"
  "v_max3_f32 v209, v147, v148, v149\n\t"
  "v_max3_f32 v208, v208, v150, v151\n\t"
  "v_max_f32 v208, v208, v209\n\t"
  "v_mov_b32 v209, v208\n\t"
  "s_nop 1\n\t"
  "v_permlane32_swap_b32 v208, v209\n\t"
  "v_max_f32 v208, v208, v209\n\t"
  "v_mov_b32 v209, v208\n\t"
  "s_nop 1\n\t"
  "v_permlane16_swap_b32 v208, v209\n\t"
  "v_max_f32 v208, v208, v209\n\t"
  "v_sub_f32 v208, 0, v208\n\t"
  "v_mov_b32 v209, v208\n\t"
  "v_mov_b32 v210, v208\n\t"
  "v_mov_b32 v211, v208\n\t"
  "v_add_f32 v144, v144, v208\n\t"
  "v_add_f32 v145, v145, v208\n\t"
  "v_add_f32 v146, v146, v208\n\t"
  "v_add_f32 v147, v147, v208\n\t"
  "v_add_f32 v148, v148, v208\n\t"
  "v_add_f32 v149, v149, v208\n\t"
  "v_add_f32 v150, v150, v208\n\t"
  "v_add_f32 v151, v151, v208\n\t"
  "v_exp_f32 v144, v144\n\t"
  "v_exp_f32 v145, v145\n\t"
  "v_exp_f32 v146, v146\n\t"
  "v_cvt_pk_bf16_f32 v144, v144, v145\n\t"
  "v_exp_f32 v147, v147\n\t"
  "s_nop 0\n\t"
  "v_cvt_pk_bf16_f32 v145, v146, v147\n\t"
  "s_waitcnt lgkmcnt(0)\n\t"
  "v_mfma_f32_16x16x32_bf16 v[152:155], a[180:183], v[32:35], v[192:195]\n\t"
  "v_exp_f32 v116, v116\n\t"
  "v_mfma_f32_16x16x32_bf16 v[160:163], a[180:183], v[48:51], v[196:199]\n\t"
  "ds_read_b64_tr_b16 a[212:213], v220 offset:32768\n\t"
  "v_mfma_f32_16x16x32_bf16 v[168:171], a[180:183], v[64:67], v[200:203]\n\t"
  "v_exp_f32 v117, v117\n\t"
  "v_mfma_f32_16x16x32_bf16 v[176:179], a[180:183], v[80:83], v[204:207]\n\t"
  "ds_read_b64_tr_b16 a[214:215], v220 offset:36864\n\t"
  "v_mfma_f32_16x16x32_bf16 v[184:187], a[180:183], v[96:99], v[208:211]\n\t"
  "v_exp_f32 v118, v118\n\t"
  "s_waitcnt vmcnt(4)\n\t"
  "buffer_load_dwordx4 v215, s[28:31], s52 offen offset:3072 nt lds\n\t"
  "v_mfma_f32_16x16x32_bf16 v[152:155], a[184:187], v[36:39], v[152:155]\n\t"
  "v_mfma_f32_16x16x32_bf16 v[160:163], a[184:187], v[52:55], v[160:163]\n\t"
  "v_mfma_f32_16x16x32_bf16 v[168:171], a[184:187], v[68:71], v[168:171]\n\t"
  "ds_read_b64_tr_b16 a[216:217], v221 offset:32768\n\t"
  "v_cvt_pk_bf16_f32 v114, v116, v117\n\t"
  "v_mfma_f32_16x16x32_bf16 v[176:179], a[184:187], v[84:87], v[176:179]\n\t"
  "v_exp_f32 v119, v119\n\t"
  "v_mfma_f32_16x16x32_bf16 v[184:187], a[184:187], v[100:103], v[184:187]\n\t"
  "ds_read_b64_tr_b16 a[218:219], v221 offset:36864\n\t"
  "v_mfma_f32_16x16x32_bf16 v[152:155], a[188:191], v[40:43], v[152:155]\n\t"
  "v_exp_f32 v124, v124\n\t"
  "v_mfma_f32_16x16x32_bf16 v[160:163], a[188:191], v[56:59], v[160:163]\n\t"
  "ds_read_b64_tr_b16 a[220:221], v220 offset:33280\n\t"
  "v_cvt_pk_bf16_f32 v115, v118, v119\n\t"
  "v_mfma_f32_16x16x32_bf16 v[168:171], a[188:191], v[72:75], v[168:171]\n\t"
  "v_exp_f32 v125, v125\n\t"
  "v_mfma_f32_16x16x32_bf16 v[176:179], a[188:191], v[88:91], v[176:179]\n\t"
  "ds_read_b64_tr_b16 a[222:223], v220 offset:37376\n\t"
  "v_mfma_f32_16x16x32_bf16 v[184:187], a[188:191], v[104:107], v[184:187]\n\t"
  "v_exp_f32 v126, v126\n\t"
  "v_mfma_f32_16x16x32_bf16 v[152:155], a[192:195], v[44:47], v[152:155]\n\t"
  "ds_read_b64_tr_b16 a[224:225], v221 offset:33280\n\t"
  "v_cvt_pk_bf16_f32 v122, v124, v125\n\t"
  "s_waitcnt vmcnt(4)\n\t"
  "s_add_u32 m0, s51, 0\n\t"
  "s_mov_b32 s52, 32768\n\t"
  "buffer_load_dwordx4 v212, s[28:31], s52 offen offset:0 nt lds\n\t"
  "v_mfma_f32_16x16x32_bf16 v[160:163], a[192:195], v[60:63], v[160:163]\n\t"
  "v_mfma_f32_16x16x32_bf16 v[168:171], a[192:195], v[76:79], v[168:171]\n\t"
  "v_mfma_f32_16x16x32_bf16 v[176:179], a[192:195], v[92:95], v[176:179]\n\t"
  "v_mfma_f32_16x16x32_bf16 v[184:187], a[192:195], v[108:111], v[184:187]\n\t"
  "v_exp_f32 v127, v127\n\t"
  "v_mfma_f32_16x16x32_bf16 v[156:159], a[196:199], v[32:35], v[192:195]\n\t"
  "ds_read_b64_tr_b16 a[226:227], v221 offset:37376\n\t"
  "v_mfma_f32_16x16x32_bf16 v[164:167], a[196:199], v[48:51], v[196:199]\n\t"
  "v_exp_f32 v132, v132\n\t"
  "v_mfma_f32_16x16x32_bf16 v[172:175], a[196:199], v[64:67], v[200:203]\n\t"
  "ds_read_b64_tr_b16 a[228:229], v220 offset:33792\n\t"
  "v_cvt_pk_bf16_f32 v123, v126, v127\n\t"
  "v_mfma_f32_16x16x32_bf16 v[180:183], a[196:199], v[80:83], v[204:207]\n\t"
  "v_exp_f32 v133, v133\n\t"
  "v_mfma_f32_16x16x32_bf16 v[188:191], a[196:199], v[96:99], v[208:211]\n\t"
  "ds_read_b64_tr_b16 a[230:231], v220 offset:37888\n\t"
  "v_mfma_f32_16x16x32_bf16 v[156:159], a[200:203], v[36:39], v[156:159]\n\t"
  "v_exp_f32 v134, v134\n\t"
  "v_mfma_f32_16x16x32_bf16 v[164:167], a[200:203], v[52:55], v[164:167]\n\t"
  "ds_read_b64_tr_b16 a[232:233], v221 offset:33792\n\t"
  "v_cvt_pk_bf16_f32 v130, v132, v133\n\t"
  "v_mfma_f32_16x16x32_bf16 v[172:175], a[200:203], v[68:71], v[172:175]\n\t"
  "v_exp_f32 v135, v135\n\t"
  "v_mfma_f32_16x16x32_bf16 v[180:183], a[200:203], v[84:87], v[180:183]\n\t"
  "ds_read_b64_tr_b16 a[234:235], v221 offset:37888\n\t"
  "v_mfma_f32_16x16x32_bf16 v[188:191], a[200:203], v[100:103], v[188:191]\n\t"
  "v_exp_f32 v140, v140\n\t"
  "v_mfma_f32_16x16x32_bf16 v[156:159], a[204:207], v[40:43], v[156:159]\n\t"
  "ds_read_b64_tr_b16 a[236:237], v220 offset:34304\n\t"
  "v_cvt_pk_bf16_f32 v131, v134, v135\n\t"
  "v_mfma_f32_16x16x32_bf16 v[164:167], a[204:207], v[56:59], v[164:167]\n\t"
  "v_exp_f32 v141, v141\n\t"
  "v_mfma_f32_16x16x32_bf16 v[172:175], a[204:207], v[72:75], v[172:175]\n\t"
  "ds_read_b64_tr_b16 a[238:239], v220 offset:38400\n\t"
  "v_mfma_f32_16x16x32_bf16 v[180:183], a[204:207], v[88:91], v[180:183]\n\t"
  "v_exp_f32 v142, v142\n\t"
  "v_mfma_f32_16x16x32_bf16 v[188:191], a[204:207], v[104:107], v[188:191]\n\t"
  "ds_read_b64_tr_b16 a[240:241], v221 offset:34304\n\t"
  "s_waitcnt vmcnt(4)\n\t"
  "buffer_load_dwordx4 v213, s[28:31], s52 offen offset:1024 nt lds\n\t"
  "v_mfma_f32_16x16x32_bf16 v[156:159], a[208:211], v[44:47], v[156:159]\n\t"
  "v_mfma_f32_16x16x32_bf16 v[164:167], a[208:211], v[60:63], v[164:167]\n\t"
  "v_cvt_pk_bf16_f32 v138, v140, v141\n\t"
  "v_mfma_f32_16x16x32_bf16 v[172:175], a[208:211], v[76:79], v[172:175]\n\t"
  "v_exp_f32 v143, v143\n\t"
  "v_mfma_f32_16x16x32_bf16 v[180:183], a[208:211], v[92:95], v[180:183]\n\t"
  "ds_read_b64_tr_b16 a[242:243], v221 offset:38400\n\t"
  "v_exp_f32 v148, v148\n\t"
  "v_cvt_pk_bf16_f32 v139, v142, v143\n\t"
  "v_exp_f32 v149, v149\n\t"
  "v_exp_f32 v150, v150\n\t"
  "v_cvt_pk_bf16_f32 v146, v148, v149\n\t"
  "v_exp_f32 v151, v151\n\t"
  "s_nop 0\n\t"
  "v_cvt_pk_bf16_f32 v147, v150, v151\n\t"
  "v_mfma_f32_16x16x32_bf16 v[188:191], a[208:211], v[108:111], v[188:191]\n\t"
  "s_waitcnt vmcnt(18) lgkmcnt(0)\n\t"
  "s_barrier\n\t"
  "v_mfma_f32_16x16x32_bf16 a[0:3], a[212:215], v[112:115], 0\n\t"
  "v_exp_f32 v152, v152\n\t"
  "v_mfma_f32_16x16x32_bf16 a[4:7], a[212:215], v[120:123], 0\n\t"
  "v_add_u32 v216, 65536, v216\n\t"
  "v_add_u32 v217, 65536, v217\n\t"
  "v_add_u32 v218, 65536, v218\n\t"
  "v_add_u32 v219, 65536, v219\n\t"
  "v_mfma_f32_16x16x32_bf16 a[8:11], a[212:215], v[128:131], 0\n\t"
  "v_mfma_f32_16x16x32_bf16 a[12:15], a[212:215], v[136:139], 0\n\t"
  "s_waitcnt vmcnt(4)\n\t"
  "buffer_load_dwordx4 v214, s[28:31], s52 offen offset:2048 nt lds\n\t"
  "v_mfma_f32_16x16x32_bf16 a[16:19], a[212:215], v[144:147], 0\n\t"
  "v_mfma_f32_16x16x32_bf16 a[20:23], a[216:219], v[112:115], 0\n\t"
  "v_exp_f32 v153, v153\n\t"
  "v_mfma_f32_16x16x32_bf16 a[24:27], a[216:219], v[120:123], 0\n\t"
  "v_exp_f32 v154, v154\n\t"
  "v_mfma_f32_16x16x32_bf16 a[28:31], a[216:219], v[128:131], 0\n\t"
  "v_cvt_pk_bf16_f32 v152, v152, v153\n\t"
  "v_mfma_f32_16x16x32_bf16 a[32:35], a[216:219], v[136:139], 0\n\t"
  "v_exp_f32 v155, v155\n\t"
  "v_mfma_f32_16x16x32_bf16 a[36:39], a[216:219], v[144:147], 0\n\t"
  "v_exp_f32 v160, v160\n\t"
  "v_mfma_f32_16x16x32_bf16 a[40:43], a[220:223], v[112:115], 0\n\t"
  "v_cvt_pk_bf16_f32 v153, v154, v155\n\t"
  "ds_read_b128 a[180:183], v216 offset:0\n\t"
  "v_mfma_f32_16x16x32_bf16 a[44:47], a[220:223], v[120:123], 0\n\t"
  "v_exp_f32 v161, v161\n\t"
  "v_mfma_f32_16x16x32_bf16 a[48:51], a[220:223], v[128:131], 0\n\t"
  "s_waitcnt vmcnt(4)\n\t"
  "buffer_load_dwordx4 v215, s[28:31], s52 offen offset:3072 nt lds\n\t"
  "v_mfma_f32_16x16x32_bf16 a[52:55], a[220:223], v[136:139], 0\n\t"
  "v_mfma_f32_16x16x32_bf16 a[56:59], a[220:223], v[144:147], 0\n\t"
  "v_exp_f32 v162, v162\n\t"
  "v_mfma_f32_16x16x32_bf16 a[60:63], a[224:227], v[112:115], 0\n\t"
  "ds_read_b128 a[184:187], v217 offset:0\n\t"
  "v_cvt_pk_bf16_f32 v160, v160, v161\n\t"
  "v_mfma_f32_16x16x32_bf16 a[64:67], a[224:227], v[120:123], 0\n\t"
  "v_exp_f32 v163, v163\n\t"
  "v_mfma_f32_16x16x32_bf16 a[68:71], a[224:227], v[128:131], 0\n\t"
  "ds_read_b128 a[188:191], v218 offset:0\n\t"
  "v_mfma_f32_16x16x32_bf16 a[72:75], a[224:227], v[136:139], 0\n\t"
  "v_exp_f32 v168, v168\n\t"
  "v_mfma_f32_16x16x32_bf16 a[76:79], a[224:227], v[144:147], 0\n\t"
  "ds_read_b128 a[192:195], v219 offset:0\n\t"
  "v_cvt_pk_bf16_f32 v161, v162, v163\n\t"
  "v_mfma_f32_16x16x32_bf16 a[80:83], a[228:231], v[112:115], 0\n\t"
  "v_exp_f32 v169, v169\n\t"
  "v_mfma_f32_16x16x32_bf16 a[84:87], a[228:231], v[120:123], 0\n\t"
  "s_waitcnt vmcnt(4)\n\t"
  "s_add_u32 m0, s51, 4096\n\t"
  "s_mov_b32 s52, 36864\n\t"
  "buffer_load_dwordx4 v212, s[28:31], s52 offen offset:0 nt lds\n\t"
  "v_mfma_f32_16x16x32_bf16 a[88:91], a[228:231], v[128:131], 0\n\t"
  "v_mfma_f32_16x16x32_bf16 a[92:95], a[228:231], v[136:139], 0\n\t"
  "v_mfma_f32_16x16x32_bf16 a[96:99], a[228:231], v[144:147], 0\n\t"
  "ds_read_b128 a[196:199], v216 offset:4096\n\t"
  "v_mfma_f32_16x16x32_bf16 a[100:103], a[232:235], v[112:115], 0\n\t"
  "v_exp_f32 v170, v170\n\t"
  "v_mfma_f32_16x16x32_bf16 a[104:107], a[232:235], v[120:123], 0\n\t"
  "v_cvt_pk_bf16_f32 v168, v168, v169\n\t"
  "ds_read_b128 a[200:203], v217 offset:4096\n\t"
  "v_mfma_f32_16x16x32_bf16 a[108:111], a[232:235], v[128:131], 0\n\t"
  "v_exp_f32 v171, v171\n\t"
  "v_mfma_f32_16x16x32_bf16 a[112:115], a[232:235], v[136:139], 0\n\t"
  "ds_read_b128 a[204:207], v218 offset:4096\n\t"
  "v_mfma_f32_16x16x32_bf16 a[116:119], a[232:235], v[144:147], 0\n\t"
  "v_exp_f32 v176, v176\n\t"
  "v_mfma_f32_16x16x32_bf16 a[120:123], a[236:239], v[112:115], 0\n\t"
  "v_cvt_pk_bf16_f32 v169, v170, v171\n\t"
  "ds_read_b128 a[208:211], v219 offset:4096\n\t"
  "v_mfma_f32_16x16x32_bf16 a[124:127], a[236:239], v[120:123], 0\n\t"
  "v_exp_f32 v177, v177\n\t"
  "v_mfma_f32_16x16x32_bf16 a[128:131], a[236:239], v[128:131], 0\n\t"
  "v_exp_f32 v178, v178\n\t"
  "v_mfma_f32_16x16x32_bf16 a[132:135], a[236:239], v[136:139], 0\n\t"
  "v_cvt_pk_bf16_f32 v176, v176, v177\n\t"
  "v_mfma_f32_16x16x32_bf16 a[136:139], a[236:239], v[144:147], 0\n\t"
  "v_exp_f32 v179, v179\n\t"
  "v_mfma_f32_16x16x32_bf16 a[140:143], a[240:243], v[112:115], 0\n\t"
  "s_waitcnt vmcnt(4)\n\t"
  "buffer_load_dwordx4 v213, s[28:31], s52 offen offset:1024 nt lds\n\t"
  "v_mfma_f32_16x16x32_bf16 a[144:147], a[240:243], v[120:123], 0\n\t"
  "v_mfma_f32_16x16x32_bf16 a[148:151], a[240:243], v[128:131], 0\n\t"
  "v_exp_f32 v184, v184\n\t"
  "v_mfma_f32_16x16x32_bf16 a[152:155], a[240:243], v[136:139], 0\n\t"
  "v_cvt_pk_bf16_f32 v177, v178, v179\n\t"
  "v_mfma_f32_16x16x32_bf16 a[156:159], a[240:243], v[144:147], 0\n\t"
  "v_exp_f32 v185, v185\n\t"
  "v_mfma_f32_16x16x32_bf16 a[160:163], a[244:247], v[112:115], 0\n\t"
  "v_exp_f32 v186, v186\n\t"
  "v_mfma_f32_16x16x32_bf16 a[164:167], a[244:247], v[120:123], 0\n\t"
  "v_cvt_pk_bf16_f32 v184, v184, v185\n\t"
  "v_mfma_f32_16x16x32_bf16 a[168:171], a[244:247], v[128:131], 0\n\t"
  "v_exp_f32 v187, v187\n\t"
  "v_mfma_f32_16x16x32_bf16 a[172:175], a[244:247], v[136:139], 0\n\t"
  "s_nop 0\n\t"
  "v_cvt_pk_bf16_f32 v185, v186, v187\n\t"
  "v_mfma_f32_16x16x32_bf16 a[176:179], a[244:247], v[144:147], 0\n\t"
  "s_waitcnt lgkmcnt(0)\n\t"
  "v_mfma_f32_16x16x32_bf16 v[112:115], a[180:183], v[32:35], v[192:195]\n\t"
  "v_exp_f32 v156, v156\n\t"
  "v_mfma_f32_16x16x32_bf16 v[120:123], a[180:183], v[48:51], v[196:199]\n\t"
  "v_add_u32 v220, 65536, v220\n\t"
  "v_add_u32 v221, 65536, v221\n\t"
  "v_mfma_f32_16x16x32_bf16 v[128:131], a[180:183], v[64:67], v[200:203]\n\t"
  "v_exp_f32 v157, v157\n\t"
  "v_mfma_f32_16x16x32_bf16 v[136:139], a[180:183], v[80:83], v[204:207]\n\t"
  "v_exp_f32 v158, v158\n\t"
  "s_waitcnt vmcnt(4)\n\t"
  "buffer_load_dwordx4 v214, s[28:31], s52 offen offset:2048 nt lds\n\t"
  "v_mfma_f32_16x16x32_bf16 v[144:147], a[180:183], v[96:99], v[208:211]\n\t"
  "v_mfma_f32_16x16x32_bf16 v[112:115], a[184:187], v[36:39], v[112:115]\n\t"
  "v_mfma_f32_16x16x32_bf16 v[120:123], a[184:187], v[52:55], v[120:123]\n\t"
  "ds_read_b64_tr_b16 a[212:213], v220 offset:0\n\t"
  "v_cvt_pk_bf16_f32 v154, v156, v157\n\t"
  "v_mfma_f32_16x16x32_bf16 v[128:131], a[184:187], v[68:71], v[128:131]\n\t"
  "ds_read_b64_tr_b16 a[214:215], v220 offset:4096\n\t"
  "v_mfma_f32_16x16x32_bf16 v[136:139], a[184:187], v[84:87], v[136:139]\n\t"
  "v_exp_f32 v159, v159\n\t"
  "v_mfma_f32_16x16x32_bf16 v[144:147], a[184:187], v[100:103], v[144:147]\n\t"
  "ds_read_b64_tr_b16 a[216:217], v221 offset:0\n\t"
  "v_mfma_f32_16x16x32_bf16 v[112:115], a[188:191], v[40:43], v[112:115]\n\t"
  "v_exp_f32 v164, v164\n\t"
  "v_mfma_f32_16x16x32_bf16 v[120:123], a[188:191], v[56:59], v[120:123]\n\t"
  "v_cvt_pk_bf16_f32 v155, v158, v159\n\t"
  "ds_read_b64_tr_b16 a[218:219], v221 offset:4096\n\t"
  "v_mfma_f32_16x16x32_bf16 v[128:131], a[188:191], v[72:75], v[128:131]\n\t"
  "v_exp_f32 v165, v165\n\t"
  "v_mfma_f32_16x16x32_bf16 v[136:139], a[188:191], v[88:91], v[136:139]\n\t"
  "ds_read_b64_tr_b16 a[220:221], v220 offset:512\n\t"
  "v_mfma_f32_16x16x32_bf16 v[144:147], a[188:191], v[104:107], v[144:147]\n\t"
  "v_exp_f32 v166, v166\n\t"
  "v_mfma_f32_16x16x32_bf16 v[112:115], a[192:195], v[44:47], v[112:115]\n\t"
  "ds_read_b64_tr_b16 a[222:223], v220 offset:4608\n\t"
  "v_cvt_pk_bf16_f32 v162, v164, v165\n\t"
  "s_waitcnt vmcnt(4)\n\t"
  "buffer_load_dwordx4 v215, s[28:31], s52 offen offset:3072 nt lds\n\t"
  "v_mfma_f32_16x16x32_bf16 v[120:123], a[192:195], v[60:63], v[120:123]\n\t"
  "v_mfma_f32_16x16x32_bf16 v[128:131], a[192:195], v[76:79], v[128:131]\n\t"
  "v_mfma_f32_16x16x32_bf16 v[136:139], a[192:195], v[92:95], v[136:139]\n\t"
  "v_exp_f32 v167, v167\n\t"
  "v_mfma_f32_16x16x32_bf16 v[144:147], a[192:195], v[108:111], v[144:147]\n\t"
  "ds_read_b64_tr_b16 a[224:225], v221 offset:512\n\t"
  "v_mfma_f32_16x16x32_bf16 v[116:119], a[196:199], v[32:35], v[192:195]\n\t"
  "v_exp_f32 v172, v172\n\t"
  "v_mfma_f32_16x16x32_bf16 v[124:127], a[196:199], v[48:51], v[196:199]\n\t"
  "ds_read_b64_tr_b16 a[226:227], v221 offset:4608\n\t"
  "v_cvt_pk_bf16_f32 v163, v166, v167\n\t"
  "v_mfma_f32_16x16x32_bf16 v[132:135], a[196:199], v[64:67], v[200:203]\n\t"
  "v_exp_f32 v173, v173\n\t"
  "v_mfma_f32_16x16x32_bf16 v[140:143], a[196:199], v[80:83], v[204:207]\n\t"
  "ds_read_b64_tr_b16 a[228:229], v220 offset:1024\n\t"
  "v_mfma_f32_16x16x32_bf16 v[148:151], a[196:199], v[96:99], v[208:211]\n\t"
  "v_exp_f32 v174, v174\n\t"
  "v_mfma_f32_16x16x32_bf16 v[116:119], a[200:203], v[36:39], v[116:119]\n\t"
  "ds_read_b64_tr_b16 a[230:231], v220 offset:5120\n\t"
  "v_cvt_pk_bf16_f32 v170, v172, v173\n\t"
  "v_mfma_f32_16x16x32_bf16 v[124:127], a[200:203], v[52:55], v[124:127]\n\t"
  "ds_read_b64_tr_b16 a[232:233], v221 offset:1024\n\t"
  "v_mfma_f32_16x16x32_bf16 v[132:135], a[200:203], v[68:71], v[132:135]\n\t"
  "v_exp_f32 v175, v175\n\t"
  "v_mfma_f32_16x16x32_bf16 v[140:143], a[200:203], v[84:87], v[140:143]\n\t"
  "v_exp_f32 v180, v180\n\t"
  "v_mfma_f32_16x16x32_bf16 v[148:151], a[200:203], v[100:103], v[148:151]\n\t"
  "ds_read_b64_tr_b16 a[234:235], v221 offset:5120\n\t"
  "s_waitcnt vmcnt(4)\n\t"
  "s_add_u32 m0, s51, 32768\n\t"
  "s_mov_b32 s52, 40960\n\t"
  "buffer_load_dwordx4 v212, s[28:31], s52 offen offset:0 nt lds\n\t"
  "v_mfma_f32_16x16x32_bf16 v[116:119], a[204:207], v[40:43], v[116:119]\n\t"
  "v_mfma_f32_16x16x32_bf16 v[124:127], a[204:207], v[56:59], v[124:127]\n\t"
  "v_mfma_f32_16x16x32_bf16 v[132:135], a[204:207], v[72:75], v[132:135]\n\t"
  "v_cvt_pk_bf16_f32 v171, v174, v175\n\t"
  "v_mfma_f32_16x16x32_bf16 v[140:143], a[204:207], v[88:91], v[140:143]\n\t"
  "ds_read_b64_tr_b16 a[236:237], v220 offset:1536\n\t"
  "v_mfma_f32_16x16x32_bf16 v[148:151], a[204:207], v[104:107], v[148:151]\n\t"
  "v_exp_f32 v181, v181\n\t"
  "v_mfma_f32_16x16x32_bf16 v[116:119], a[208:211], v[44:47], v[116:119]\n\t"
  "ds_read_b64_tr_b16 a[238:239], v220 offset:5632\n\t"
  "v_mfma_f32_16x16x32_bf16 v[124:127], a[208:211], v[60:63], v[124:127]\n\t"
  "v_exp_f32 v182, v182\n\t"
  "v_mfma_f32_16x16x32_bf16 v[132:135], a[208:211], v[76:79], v[132:135]\n\t"
  "ds_read_b64_tr_b16 a[240:241], v221 offset:1536\n\t"
  "v_cvt_pk_bf16_f32 v178, v180, v181\n\t"
  "v_mfma_f32_16x16x32_bf16 v[140:143], a[208:211], v[92:95], v[140:143]\n\t"
  "v_exp_f32 v183, v183\n\t"
  "ds_read_b64_tr_b16 a[242:243], v221 offset:5632\n\t"
  "v_exp_f32 v188, v188\n\t"
  "v_cvt_pk_bf16_f32 v179, v182, v183\n\t"
  "v_exp_f32 v189, v189\n\t"
  "v_exp_f32 v190, v190\n\t"
  "v_cvt_pk_bf16_f32 v186, v188, v189\n\t"
  "v_exp_f32 v191, v191\n\t"
  "s_nop 0\n\t"
  "v_cvt_pk_bf16_f32 v187, v190, v191\n\t"
  "v_mfma_f32_16x16x32_bf16 v[148:151], a[208:211], v[108:111], v[148:151]\n\t"
  "s_waitcnt vmcnt(17) lgkmcnt(0)\n\t"
  "s_barrier\n\t"
  "v_mfma_f32_16x16x32_bf16 a[0:3], a[212:215], v[152:155], a[0:3]\n\t"
  "v_exp_f32 v112, v112\n\t"
  "ds_read_b128 a[180:183], v216 offset:32768\n\t"
  "v_mfma_f32_16x16x32_bf16 a[4:7], a[212:215], v[160:163], a[4:7]\n\t"
  "s_waitcnt vmcnt(4)\n\t"
  "buffer_load_dwordx4 v213, s[28:31], s52 offen offset:1024 nt lds\n\t"
  "v_mfma_f32_16x16x32_bf16 a[8:11], a[212:215], v[168:171], a[8:11]\n\t"
  "v_mfma_f32_16x16x32_bf16 a[12:15], a[212:215], v[176:179], a[12:15]\n\t"
  "v_exp_f32 v113, v113\n\t"
  "v_mfma_f32_16x16x32_bf16 a[16:19], a[212:215], v[184:187], a[16:19]\n\t"
  "v_exp_f32 v114, v114\n\t"
  "ds_read_b128 a[184:187], v217 offset:32768\n\t"
  "v_mfma_f32_16x16x32_bf16 a[20:23], a[216:219], v[152:155], a[20:23]\n\t"
  "s_waitcnt vmcnt(4)\n\t"
  "buffer_load_dwordx4 v214, s[28:31], s52 offen offset:2048 nt lds\n\t"
  "v_mfma_f32_16x16x32_bf16 a[24:27], a[216:219], v[160:163], a[24:27]\n\t"
  "v_mfma_f32_16x16x32_bf16 a[28:31], a[216:219], v[168:171], a[28:31]\n\t"
  "v_cvt_pk_bf16_f32 v112, v112, v113\n\t"
  "v_exp_f32 v115, v115\n\t"
  "v_mfma_f32_16x16x32_bf16 a[32:35], a[216:219], v[176:179], a[32:35]\n\t"
  "ds_read_b128 a[188:191], v218 offset:32768\n\t"
  "v_mfma_f32_16x16x32_bf16 a[36:39], a[216:219], v[184:187], a[36:39]\n\t"
  "v_exp_f32 v120, v120\n\t"
  "s_waitcnt vmcnt(4)\n\t"
  "buffer_load_dwordx4 v215, s[28:31], s52 offen offset:3072 nt lds\n\t"
  "v_mfma_f32_16x16x32_bf16 a[40:43], a[220:223], v[152:155], a[40:43]\n\t"
  "v_mfma_f32_16x16x32_bf16 a[44:47], a[220:223], v[160:163], a[44:47]\n\t"
  "v_cvt_pk_bf16_f32 v113, v114, v115\n\t"
  "v_mfma_f32_16x16x32_bf16 a[48:51], a[220:223], v[168:171], a[48:51]\n\t"
  "v_exp_f32 v121, v121\n\t"
  "ds_read_b128 a[192:195], v219 offset:32768\n\t"
  "v_mfma_f32_16x16x32_bf16 a[52:55], a[220:223], v[176:179], a[52:55]\n\t"
  "s_waitcnt vmcnt(4)\n\t"
  "s_add_u32 m0, s51, 36864\n\t"
  "s_mov_b32 s52, 45056\n\t"
  "buffer_load_dwordx4 v212, s[28:31], s52 offen offset:0 nt lds\n\t"
  "v_mfma_f32_16x16x32_bf16 a[56:59], a[220:223], v[184:187], a[56:59]\n\t"
  "v_mfma_f32_16x16x32_bf16 a[60:63], a[224:227], v[152:155], a[60:63]\n\t"
  "v_mfma_f32_16x16x32_bf16 a[64:67], a[224:227], v[160:163], a[64:67]\n\t"
  "v_exp_f32 v122, v122\n\t"
  "v_cvt_pk_bf16_f32 v120, v120, v121\n\t"
  "v_mfma_f32_16x16x32_bf16 a[68:71], a[224:227], v[168:171], a[68:71]\n\t"
  "v_exp_f32 v123, v123\n\t"
  "v_mfma_f32_16x16x32_bf16 a[72:75], a[224:227], v[176:179], a[72:75]\n\t"
  "ds_read_b128 a[196:199], v216 offset:36864\n\t"
  "v_exp_f32 v128, v128\n\t"
  "v_mfma_f32_16x16x32_bf16 a[76:79], a[224:227], v[184:187], a[76:79]\n\t"
  "s_mov_b32 s20, s72\n\t"
  "s_mov_b32 s21, s73\n\t"
  "v_mfma_f32_16x16x32_bf16 a[80:83], a[228:231], v[152:155], a[80:83]\n\t"
  "v_cvt_pk_bf16_f32 v121, v122, v123\n\t"
  "s_mov_b32 s22, 1311808\n\t"
  "ds_read_b128 a[200:203], v217 offset:36864\n\t"
  "v_mfma_f32_16x16x32_bf16 a[84:87], a[228:231], v[160:163], a[84:87]\n\t"
  "v_exp_f32 v129, v129\n\t"
  "v_mfma_f32_16x16x32_bf16 a[88:91], a[228:231], v[168:171], a[88:91]\n\t"
  "s_mov_b32 s23, 0x20000\n\t"
  "s_lshl_b32 s48, s76, 3\n\t"
  "v_mfma_f32_16x16x32_bf16 a[92:95], a[228:231], v[176:179], a[92:95]\n\t"
  "v_exp_f32 v130, v130\n\t"
  "v_add_u32 v248, s48, v246\n\t"
  "v_mfma_f32_16x16x32_bf16 a[96:99], a[228:231], v[184:187], a[96:99]\n\t"
  "v_mul_u32_u24 v248, 0x500, v248\n\t"
  "v_bfe_u32 v249, v244, 4, 1\n\t"
  "v_mfma_f32_16x16x32_bf16 a[100:103], a[232:235], v[152:155], a[100:103]\n\t"
  "v_cvt_pk_bf16_f32 v128, v128, v129\n\t"
  "ds_read_b128 a[204:207], v218 offset:36864\n\t"
  "v_lshlrev_b32 v249, 1, v249\n\t"
  "v_mfma_f32_16x16x32_bf16 a[104:107], a[232:235], v[160:163], a[104:107]\n\t"
  "v_exp_f32 v131, v131\n\t"
  "v_mfma_f32_16x16x32_bf16 a[108:111], a[232:235], v[168:171], a[108:111]\n\t"
  "v_xor_b32 v249, v249, v247\n\t"
  "v_lshl_add_u32 v248, v249, 4, v248\n\t"
  "v_mfma_f32_16x16x32_bf16 a[112:115], a[232:235], v[176:179], a[112:115]\n\t"
  "v_exp_f32 v136, v136\n\t"
  "v_lshrrev_b32 v249, 5, v244\n\t"
  "v_mfma_f32_16x16x32_bf16 a[116:119], a[232:235], v[184:187], a[116:119]\n\t"
  "ds_read_b128 a[208:211], v219 offset:36864\n\t"
  "s_and_b32 s49, s77, 1\n\t"
  "v_mfma_f32_16x16x32_bf16 a[120:123], a[236:239], v[152:155], a[120:123]\n\t"
  "v_cvt_pk_bf16_f32 v129, v130, v131\n\t"
  "s_lshl_b32 s48, s49, 6\n\t"
  "v_mfma_f32_16x16x32_bf16 a[124:127], a[236:239], v[160:163], a[124:127]\n\t"
  "v_exp_f32 v137, v137\n\t"
  "s_add_u32 s48, s48, 2048\n\t"
  "v_mfma_f32_16x16x32_bf16 a[128:131], a[236:239], v[168:171], a[128:131]\n\t"
  "v_lshl_add_u32 v16, v249, 6, v248\n\t"
  "v_mfma_f32_16x16x32_bf16 a[132:135], a[236:239], v[176:179], a[132:135]\n\t"
  "v_exp_f32 v138, v138\n\t"
  "v_add_u32 v16, s48, v16\n\t"
  "v_mfma_f32_16x16x32_bf16 a[136:139], a[236:239], v[184:187], a[136:139]\n\t"
  "v_add_u32 v17, -896, v16\n\t"
  "v_cvt_pk_bf16_f32 v136, v136, v137\n\t"
  "v_mfma_f32_16x16x32_bf16 a[140:143], a[240:243], v[152:155], a[140:143]\n\t"
  "s_lshl_b32 s48, s49, 8\n\t"
  "s_sub_u32 s48, 256, s48\n\t"
  "v_mfma_f32_16x16x32_bf16 a[144:147], a[240:243], v[160:163], a[144:147]\n\t"
  "v_exp_f32 v139, v139\n\t"
  "v_add_u32 v18, s48, v248\n\t"
  "v_mfma_f32_16x16x32_bf16 a[148:151], a[240:243], v[168:171], a[148:151]\n\t"
  "v_lshl_add_u32 v18, v249, 24, v18\n\t"
  "v_mfma_f32_16x16x32_bf16 a[152:155], a[240:243], v[176:179], a[152:155]\n\t"
  "v_exp_f32 v144, v144\n\t"
  "s_mul_i32 s55, s76, 3072\n\t"
  "v_mfma_f32_16x16x32_bf16 a[156:159], a[240:243], v[184:187], a[156:159]\n\t"
  "s_mul_i32 s56, s77, 327680\n\t"
  "v_cvt_pk_bf16_f32 v137, v138, v139\n\t"
  "v_mfma_f32_16x16x32_bf16 a[160:163], a[244:247], v[152:155], a[160:163]\n\t"
  "s_mul_i32 s48, s47, 10240\n\t"
  "v_exp_f32 v145, v145\n\t"
  "v_mfma_f32_16x16x32_bf16 a[164:167], a[244:247], v[160:163], a[164:167]\n\t"
  "s_mul_i32 s49, s46, 40960\n\t"
  "s_add_u32 s49, s49, s48\n\t"
  "v_mfma_f32_16x16x32_bf16 a[168:171], a[244:247], v[168:171], a[168:171]\n\t"
  "v_exp_f32 v146, v146\n\t"
  "v_add_u32 v23, s49, v222\n\t"
  "v_mfma_f32_16x16x32_bf16 a[172:175], a[244:247], v[176:179], a[172:175]\n\t"
  "s_mul_i32 s49, s46, 118784\n\t"
  "v_cvt_pk_bf16_f32 v144, v144, v145\n\t"
  "s_add_u32 s49, s49, s48\n\t"
  "v_exp_f32 v147, v147\n\t"
  "s_add_u32 s49, s49, 20480\n\t"
  "v_add_u32 v24, s49, v222\n\t"
  "s_load_dwordx2 s[62:63], %13, 0x58\n\t"
  "s_nop 0\n\t"
  "v_cvt_pk_bf16_f32 v145, v146, v147\n\t"
  "v_mfma_f32_16x16x32_bf16 a[176:179], a[244:247], v[184:187], a[176:179]\n\t"
  "s_waitcnt lgkmcnt(0)\n\t"
  "v_mfma_f32_16x16x32_bf16 v[152:155], a[180:183], v[32:35], v[192:195]\n\t"
  "v_exp_f32 v116, v116\n\t"
  "v_mfma_f32_16x16x32_bf16 v[160:163], a[180:183], v[48:51], v[196:199]\n\t"
  "ds_read_b64_tr_b16 a[212:213], v220 offset:32768\n\t"
  "v_mfma_f32_16x16x32_bf16 v[168:171], a[180:183], v[64:67], v[200:203]\n\t"
  "v_exp_f32 v117, v117\n\t"
  "v_mfma_f32_16x16x32_bf16 v[176:179], a[180:183], v[80:83], v[204:207]\n\t"
  "ds_read_b64_tr_b16 a[214:215], v220 offset:36864\n\t"
  "v_mfma_f32_16x16x32_bf16 v[184:187], a[180:183], v[96:99], v[208:211]\n\t"
  "v_exp_f32 v118, v118\n\t"
  "s_waitcnt vmcnt(4)\n\t"
  "buffer_load_dwordx4 v213, s[28:31], s52 offen offset:1024 nt lds\n\t"
  "v_mfma_f32_16x16x32_bf16 v[152:155], a[184:187], v[36:39], v[152:155]\n\t"
  "v_mfma_f32_16x16x32_bf16 v[160:163], a[184:187], v[52:55], v[160:163]\n\t"
  "v_mfma_f32_16x16x32_bf16 v[168:171], a[184:187], v[68:71], v[168:171]\n\t"
  "ds_read_b64_tr_b16 a[216:217], v221 offset:32768\n\t"
  "v_cvt_pk_bf16_f32 v114, v116, v117\n\t"
  "v_mfma_f32_16x16x32_bf16 v[176:179], a[184:187], v[84:87], v[176:179]\n\t"
  "v_exp_f32 v119, v119\n\t"
  "v_mfma_f32_16x16x32_bf16 v[184:187], a[184:187], v[100:103], v[184:187]\n\t"
  "ds_read_b64_tr_b16 a[218:219], v221 offset:36864\n\t"
  "v_mfma_f32_16x16x32_bf16 v[152:155], a[188:191], v[40:43], v[152:155]\n\t"
  "v_exp_f32 v124, v124\n\t"
  "v_mfma_f32_16x16x32_bf16 v[160:163], a[188:191], v[56:59], v[160:163]\n\t"
  "ds_read_b64_tr_b16 a[220:221], v220 offset:33280\n\t"
  "v_cvt_pk_bf16_f32 v115, v118, v119\n\t"
  "v_mfma_f32_16x16x32_bf16 v[168:171], a[188:191], v[72:75], v[168:171]\n\t"
  "v_exp_f32 v125, v125\n\t"
  "v_mfma_f32_16x16x32_bf16 v[176:179], a[188:191], v[88:91], v[176:179]\n\t"
  "ds_read_b64_tr_b16 a[222:223], v220 offset:37376\n\t"
  "v_mfma_f32_16x16x32_bf16 v[184:187], a[188:191], v[104:107], v[184:187]\n\t"
  "v_exp_f32 v126, v126\n\t"
  "v_mfma_f32_16x16x32_bf16 v[152:155], a[192:195], v[44:47], v[152:155]\n\t"
  "ds_read_b64_tr_b16 a[224:225], v221 offset:33280\n\t"
  "v_cvt_pk_bf16_f32 v122, v124, v125\n\t"
  "v_mfma_f32_16x16x32_bf16 v[160:163], a[192:195], v[60:63], v[160:163]\n\t"
  "v_exp_f32 v127, v127\n\t"
  "v_mfma_f32_16x16x32_bf16 v[168:171], a[192:195], v[76:79], v[168:171]\n\t"
  "ds_read_b64_tr_b16 a[226:227], v221 offset:37376\n\t"
  "s_waitcnt vmcnt(4)\n\t"
  "buffer_load_dwordx4 v214, s[28:31], s52 offen offset:2048 nt lds\n\t"
  "v_mfma_f32_16x16x32_bf16 v[176:179], a[192:195], v[92:95], v[176:179]\n\t"
  "v_mfma_f32_16x16x32_bf16 v[184:187], a[192:195], v[108:111], v[184:187]\n\t"
  "v_mfma_f32_16x16x32_bf16 v[156:159], a[196:199], v[32:35], v[192:195]\n\t"
  "v_exp_f32 v132, v132\n\t"
  "v_mfma_f32_16x16x32_bf16 v[164:167], a[196:199], v[48:51], v[196:199]\n\t"
  "ds_read_b64_tr_b16 a[228:229], v220 offset:33792\n\t"
  "v_cvt_pk_bf16_f32 v123, v126, v127\n\t"
  "v_mfma_f32_16x16x32_bf16 v[172:175], a[196:199], v[64:67], v[200:203]\n\t"
  "v_exp_f32 v133, v133\n\t"
  "v_mfma_f32_16x16x32_bf16 v[180:183], a[196:199], v[80:83], v[204:207]\n\t"
  "ds_read_b64_tr_b16 a[230:231], v220 offset:37888\n\t"
  "v_mfma_f32_16x16x32_bf16 v[188:191], a[196:199], v[96:99], v[208:211]\n\t"
  "v_exp_f32 v134, v134\n\t"
  "v_mfma_f32_16x16x32_bf16 v[156:159], a[200:203], v[36:39], v[156:159]\n\t"
  "ds_read_b64_tr_b16 a[232:233], v221 offset:33792\n\t"
  "v_cvt_pk_bf16_f32 v130, v132, v133\n\t"
  "v_mfma_f32_16x16x32_bf16 v[164:167], a[200:203], v[52:55], v[164:167]\n\t"
  "v_exp_f32 v135, v135\n\t"
  "v_mfma_f32_16x16x32_bf16 v[172:175], a[200:203], v[68:71], v[172:175]\n\t"
  "ds_read_b64_tr_b16 a[234:235], v221 offset:37888\n\t"
  "v_mfma_f32_16x16x32_bf16 v[180:183], a[200:203], v[84:87], v[180:183]\n\t"
  "v_exp_f32 v140, v140\n\t"
  "v_mfma_f32_16x16x32_bf16 v[188:191], a[200:203], v[100:103], v[188:191]\n\t"
  "ds_read_b64_tr_b16 a[236:237], v220 offset:34304\n\t"
  "v_cvt_pk_bf16_f32 v131, v134, v135\n\t"
  "v_mfma_f32_16x16x32_bf16 v[156:159], a[204:207], v[40:43], v[156:159]\n\t"
  "v_exp_f32 v141, v141\n\t"
  "v_mfma_f32_16x16x32_bf16 v[164:167], a[204:207], v[56:59], v[164:167]\n\t"
  "ds_read_b64_tr_b16 a[238:239], v220 offset:38400\n\t"
  "s_waitcnt vmcnt(4)\n\t"
  "buffer_load_dwordx4 v215, s[28:31], s52 offen offset:3072 nt lds\n\t"
  "v_mfma_f32_16x16x32_bf16 v[172:175], a[204:207], v[72:75], v[172:175]\n\t"
  "v_mfma_f32_16x16x32_bf16 v[180:183], a[204:207], v[88:91], v[180:183]\n\t"
  "v_mfma_f32_16x16x32_bf16 v[188:191], a[204:207], v[104:107], v[188:191]\n\t"
  "v_exp_f32 v142, v142\n\t"
  "v_mfma_f32_16x16x32_bf16 v[156:159], a[208:211], v[44:47], v[156:159]\n\t"
  "ds_read_b64_tr_b16 a[240:241], v221 offset:34304\n\t"
  "v_cvt_pk_bf16_f32 v138, v140, v141\n\t"
  "v_mfma_f32_16x16x32_bf16 v[164:167], a[208:211], v[60:63], v[164:167]\n\t"
  "v_exp_f32 v143, v143\n\t"
  "v_mfma_f32_16x16x32_bf16 v[172:175], a[208:211], v[76:79], v[172:175]\n\t"
  "ds_read_b64_tr_b16 a[242:243], v221 offset:38400\n\t"
  "v_mfma_f32_16x16x32_bf16 v[180:183], a[208:211], v[92:95], v[180:183]\n\t"
  "v_exp_f32 v148, v148\n\t"
  "v_cvt_pk_bf16_f32 v139, v142, v143\n\t"
  "v_exp_f32 v149, v149\n\t"
  "v_exp_f32 v150, v150\n\t"
  "v_cvt_pk_bf16_f32 v146, v148, v149\n\t"
  "v_exp_f32 v151, v151\n\t"
  "s_nop 0\n\t"
  "v_cvt_pk_bf16_f32 v147, v150, v151\n\t"
  "v_mfma_f32_16x16x32_bf16 v[188:191], a[208:211], v[108:111], v[188:191]\n\t"
  "s_waitcnt vmcnt(16) lgkmcnt(0)\n\t"
  "s_barrier\n\t"
  "v_mfma_f32_16x16x32_bf16 a[0:3], a[212:215], v[112:115], a[0:3]\n\t"
  "v_exp_f32 v152, v152\n\t"
  "v_mfma_f32_16x16x32_bf16 a[4:7], a[212:215], v[120:123], a[4:7]\n\t"
  "v_add_u32 v216, 65536, v216\n\t"
  "v_add_u32 v217, 65536, v217\n\t"
  "v_add_u32 v218, 65536, v218\n\t"
  "v_add_u32 v219, 65536, v219\n\t"
  "v_mfma_f32_16x16x32_bf16 a[8:11], a[212:215], v[128:131], a[8:11]\n\t"
  "v_mfma_f32_16x16x32_bf16 a[12:15], a[212:215], v[136:139], a[12:15]\n\t"
  "s_waitcnt vmcnt(4)\n\t"
  "s_add_u32 m0, s51, 65536\n\t"
  "s_mov_b32 s52, 49152\n\t"
  "buffer_load_dwordx4 v212, s[28:31], s52 offen offset:0 nt lds\n\t"
  "v_mfma_f32_16x16x32_bf16 a[16:19], a[212:215], v[144:147], a[16:19]\n\t"
  "v_mfma_f32_16x16x32_bf16 a[20:23], a[216:219], v[112:115], a[20:23]\n\t"
  "v_mfma_f32_16x16x32_bf16 a[24:27], a[216:219], v[120:123], a[24:27]\n\t"
  "v_exp_f32 v153, v153\n\t"
  "v_mfma_f32_16x16x32_bf16 a[28:31], a[216:219], v[128:131], a[28:31]\n\t"
  "v_exp_f32 v154, v154\n\t"
  "v_mfma_f32_16x16x32_bf16 a[32:35], a[216:219], v[136:139], a[32:35]\n\t"
  "v_cvt_pk_bf16_f32 v152, v152, v153\n\t"
  "v_mfma_f32_16x16x32_bf16 a[36:39], a[216:219], v[144:147], a[36:39]\n\t"
  "v_exp_f32 v155, v155\n\t"
  "v_mfma_f32_16x16x32_bf16 a[40:43], a[220:223], v[112:115], a[40:43]\n\t"
  "v_exp_f32 v160, v160\n\t"
  "v_mfma_f32_16x16x32_bf16 a[44:47], a[220:223], v[120:123], a[44:47]\n\t"
  "v_cvt_pk_bf16_f32 v153, v154, v155\n\t"
  "ds_read_b128 a[180:183], v216 offset:0\n\t"
  "v_mfma_f32_16x16x32_bf16 a[48:51], a[220:223], v[128:131], a[48:51]\n\t"
  "v_exp_f32 v161, v161\n\t"
  "v_mfma_f32_16x16x32_bf16 a[52:55], a[220:223], v[136:139], a[52:55]\n\t"
  "v_exp_f32 v162, v162\n\t"
  "v_mfma_f32_16x16x32_bf16 a[56:59], a[220:223], v[144:147], a[56:59]\n\t"
  "ds_read_b128 a[184:187], v217 offset:0\n\t"
  "v_cvt_pk_bf16_f32 v160, v160, v161\n\t"
  "v_mfma_f32_16x16x32_bf16 a[60:63], a[224:227], v[112:115], a[60:63]\n\t"
  "s_waitcnt vmcnt(4)\n\t"
  "buffer_load_dwordx4 v213, s[28:31], s52 offen offset:1024 nt lds\n\t"
  "v_mfma_f32_16x16x32_bf16 a[64:67], a[224:227], v[120:123], a[64:67]\n\t"
  "v_mfma_f32_16x16x32_bf16 a[68:71], a[224:227], v[128:131], a[68:71]\n\t"
  "v_exp_f32 v163, v163\n\t"
  "v_mfma_f32_16x16x32_bf16 a[72:75], a[224:227], v[136:139], a[72:75]\n\t"
  "ds_read_b128 a[188:191], v218 offset:0\n\t"
  "v_mfma_f32_16x16x32_bf16 a[76:79], a[224:227], v[144:147], a[76:79]\n\t"
  "v_exp_f32 v168, v168\n\t"
  "v_mfma_f32_16x16x32_bf16 a[80:83], a[228:231], v[112:115], a[80:83]\n\t"
  "ds_read_b128 a[192:195], v219 offset:0\n\t"
  "v_cvt_pk_bf16_f32 v161, v162, v163\n\t"
  "v_mfma_f32_16x16x32_bf16 a[84:87], a[228:231], v[120:123], a[84:87]\n\t"
  "v_exp_f32 v169, v169\n\t"
  "v_mfma_f32_16x16x32_bf16 a[88:91], a[228:231], v[128:131], a[88:91]\n\t"
  "ds_read_b128 a[196:199], v216 offset:4096\n\t"
  "v_mfma_f32_16x16x32_bf16 a[92:95], a[228:231], v[136:139], a[92:95]\n\t"
  "v_exp_f32 v170, v170\n\t"
  "v_mfma_f32_16x16x32_bf16 a[96:99], a[228:231], v[144:147], a[96:99]\n\t"
  "v_cvt_pk_bf16_f32 v168, v168, v169\n\t"
  "ds_read_b128 a[200:203], v217 offset:4096\n\t"
  "v_mfma_f32_16x16x32_bf16 a[100:103], a[232:235], v[112:115], a[100:103]\n\t"
  "v_exp_f32 v171, v171\n\t"
  "v_mfma_f32_16x16x32_bf16 a[104:107], a[232:235], v[120:123], a[104:107]\n\t"
  "s_waitcnt vmcnt(4)\n\t"
  "buffer_load_dwordx4 v214, s[28:31], s52 offen offset:2048 nt lds\n\t"
  "v_mfma_f32_16x16x32_bf16 a[108:111], a[232:235], v[128:131], a[108:111]\n\t"
  "v_mfma_f32_16x16x32_bf16 a[112:115], a[232:235], v[136:139], a[112:115]\n\t"
  "ds_read_b128 a[204:207], v218 offset:4096\n\t"
  "v_mfma_f32_16x16x32_bf16 a[116:119], a[232:235], v[144:147], a[116:119]\n\t"
  "v_exp_f32 v176, v176\n\t"
  "v_mfma_f32_16x16x32_bf16 a[120:123], a[236:239], v[112:115], a[120:123]\n\t"
  "v_cvt_pk_bf16_f32 v169, v170, v171\n\t"
  "ds_read_b128 a[208:211], v219 offset:4096\n\t"
  "v_mfma_f32_16x16x32_bf16 a[124:127], a[236:239], v[120:123], a[124:127]\n\t"
  "v_exp_f32 v177, v177\n\t"
  "v_mfma_f32_16x16x32_bf16 a[128:131], a[236:239], v[128:131], a[128:131]\n\t"
  "v_exp_f32 v178, v178\n\t"
  "v_mfma_f32_16x16x32_bf16 a[132:135], a[236:239], v[136:139], a[132:135]\n\t"
  "v_cvt_pk_bf16_f32 v176, v176, v177\n\t"
  "v_mfma_f32_16x16x32_bf16 a[136:139], a[236:239], v[144:147], a[136:139]\n\t"
  "v_exp_f32 v179, v179\n\t"
  "v_mfma_f32_16x16x32_bf16 a[140:143], a[240:243], v[112:115], a[140:143]\n\t"
  "s_waitcnt vmcnt(4)\n\t"
  "buffer_load_dwordx4 v215, s[28:31], s52 offen offset:3072 nt lds\n\t"
  "v_mfma_f32_16x16x32_bf16 a[144:147], a[240:243], v[120:123], a[144:147]\n\t"
  "v_mfma_f32_16x16x32_bf16 a[148:151], a[240:243], v[128:131], a[148:151]\n\t"
  "v_exp_f32 v184, v184\n\t"
  "v_mfma_f32_16x16x32_bf16 a[152:155], a[240:243], v[136:139], a[152:155]\n\t"
  "v_cvt_pk_bf16_f32 v177, v178, v179\n\t"
  "v_mfma_f32_16x16x32_bf16 a[156:159], a[240:243], v[144:147], a[156:159]\n\t"
  "v_exp_f32 v185, v185\n\t"
  "v_mfma_f32_16x16x32_bf16 a[160:163], a[244:247], v[112:115], a[160:163]\n\t"
  "v_exp_f32 v186, v186\n\t"
  "v_mfma_f32_16x16x32_bf16 a[164:167], a[244:247], v[120:123], a[164:167]\n\t"
  "v_cvt_pk_bf16_f32 v184, v184, v185\n\t"
  "v_mfma_f32_16x16x32_bf16 a[168:171], a[244:247], v[128:131], a[168:171]\n\t"
  "v_exp_f32 v187, v187\n\t"
  "v_mfma_f32_16x16x32_bf16 a[172:175], a[244:247], v[136:139], a[172:175]\n\t"
  "s_nop 0\n\t"
  "v_cvt_pk_bf16_f32 v185, v186, v187\n\t"
  "v_mfma_f32_16x16x32_bf16 a[176:179], a[244:247], v[144:147], a[176:179]\n\t"
  "s_waitcnt lgkmcnt(0)\n\t"
  "v_mfma_f32_16x16x32_bf16 v[112:115], a[180:183], v[32:35], v[192:195]\n\t"
  "v_exp_f32 v156, v156\n\t"
  "v_mfma_f32_16x16x32_bf16 v[120:123], a[180:183], v[48:51], v[196:199]\n\t"
  "v_add_u32 v220, 65536, v220\n\t"
  "v_add_u32 v221, 65536, v221\n\t"
  "v_mfma_f32_16x16x32_bf16 v[128:131], a[180:183], v[64:67], v[200:203]\n\t"
  "v_exp_f32 v157, v157\n\t"
  "v_mfma_f32_16x16x32_bf16 v[136:139], a[180:183], v[80:83], v[204:207]\n\t"
  "v_exp_f32 v158, v158\n\t"
  "s_waitcnt vmcnt(4)\n\t"
  "s_add_u32 m0, s51, 69632\n\t"
  "s_mov_b32 s52, 53248\n\t"
  "buffer_load_dwordx4 v212, s[28:31], s52 offen offset:0 nt lds\n\t"
  "v_mfma_f32_16x16x32_bf16 v[144:147], a[180:183], v[96:99], v[208:211]\n\t"
  "v_mfma_f32_16x16x32_bf16 v[112:115], a[184:187], v[36:39], v[112:115]\n\t"
  "v_mfma_f32_16x16x32_bf16 v[120:123], a[184:187], v[52:55], v[120:123]\n\t"
  "v_mfma_f32_16x16x32_bf16 v[128:131], a[184:187], v[68:71], v[128:131]\n\t"
  "ds_read_b64_tr_b16 a[212:213], v220 offset:0\n\t"
  "v_cvt_pk_bf16_f32 v154, v156, v157\n\t"
  "v_mfma_f32_16x16x32_bf16 v[136:139], a[184:187], v[84:87], v[136:139]\n\t"
  "ds_read_b64_tr_b16 a[214:215], v220 offset:4096\n\t"
  "v_mfma_f32_16x16x32_bf16 v[144:147], a[184:187], v[100:103], v[144:147]\n\t"
  "v_exp_f32 v159, v159\n\t"
  "v_mfma_f32_16x16x32_bf16 v[112:115], a[188:191], v[40:43], v[112:115]\n\t"
  "ds_read_b64_tr_b16 a[216:217], v221 offset:0\n\t"
  "v_mfma_f32_16x16x32_bf16 v[120:123], a[188:191], v[56:59], v[120:123]\n\t"
  "v_exp_f32 v164, v164\n\t"
  "v_mfma_f32_16x16x32_bf16 v[128:131], a[188:191], v[72:75], v[128:131]\n\t"
  "v_cvt_pk_bf16_f32 v155, v158, v159\n\t"
  "ds_read_b64_tr_b16 a[218:219], v221 offset:4096\n\t"
  "v_mfma_f32_16x16x32_bf16 v[136:139], a[188:191], v[88:91], v[136:139]\n\t"
  "v_exp_f32 v165, v165\n\t"
  "v_mfma_f32_16x16x32_bf16 v[144:147], a[188:191], v[104:107], v[144:147]\n\t"
  "ds_read_b64_tr_b16 a[220:221], v220 offset:512\n\t"
  "v_mfma_f32_16x16x32_bf16 v[112:115], a[192:195], v[44:47], v[112:115]\n\t"
  "v_exp_f32 v166, v166\n\t"
  "v_mfma_f32_16x16x32_bf16 v[120:123], a[192:195], v[60:63], v[120:123]\n\t"
  "ds_read_b64_tr_b16 a[222:223], v220 offset:4608\n\t"
  "v_cvt_pk_bf16_f32 v162, v164, v165\n\t"
  "v_mfma_f32_16x16x32_bf16 v[128:131], a[192:195], v[76:79], v[128:131]\n\t"
  "v_exp_f32 v167, v167\n\t"
  "v_mfma_f32_16x16x32_bf16 v[136:139], a[192:195], v[92:95], v[136:139]\n\t"
  "ds_read_b64_tr_b16 a[224:225], v221 offset:512\n\t"
  "v_mfma_f32_16x16x32_bf16 v[144:147], a[192:195], v[108:111], v[144:147]\n\t"
  "v_exp_f32 v172, v172\n\t"
  "v_mfma_f32_16x16x32_bf16 v[116:119], a[196:199], v[32:35], v[192:195]\n\t"
  "ds_read_b64_tr_b16 a[226:227], v221 offset:4608\n\t"
  "v_cvt_pk_bf16_f32 v163, v166, v167\n\t"
  "v_mfma_f32_16x16x32_bf16 v[124:127], a[196:199], v[48:51], v[196:199]\n\t"
  "v_exp_f32 v173, v173\n\t"
  "v_mfma_f32_16x16x32_bf16 v[132:135], a[196:199], v[64:67], v[200:203]\n\t"
  "ds_read_b64_tr_b16 a[228:229], v220 offset:1024\n\t"
  "s_waitcnt vmcnt(4)\n\t"
  "buffer_load_dwordx4 v213, s[28:31], s52 offen offset:1024 nt lds\n\t"
  "v_mfma_f32_16x16x32_bf16 v[140:143], a[196:199], v[80:83], v[204:207]\n\t"
  "v_mfma_f32_16x16x32_bf16 v[148:151], a[196:199], v[96:99], v[208:211]\n\t"
  "v_mfma_f32_16x16x32_bf16 v[116:119], a[200:203], v[36:39], v[116:119]\n\t"
  "v_exp_f32 v174, v174\n\t"
  "v_mfma_f32_16x16x32_bf16 v[124:127], a[200:203], v[52:55], v[124:127]\n\t"
  "ds_read_b64_tr_b16 a[230:231], v220 offset:5120\n\t"
  "v_cvt_pk_bf16_f32 v170, v172, v173\n\t"
  "v_mfma_f32_16x16x32_bf16 v[132:135], a[200:203], v[68:71], v[132:135]\n\t"
  "ds_read_b64_tr_b16 a[232:233], v221 offset:1024\n\t"
  "v_mfma_f32_16x16x32_bf16 v[140:143], a[200:203], v[84:87], v[140:143]\n\t"
  "v_exp_f32 v175, v175\n\t"
  "v_mfma_f32_16x16x32_bf16 v[148:151], a[200:203], v[100:103], v[148:151]\n\t"
  "v_exp_f32 v180, v180\n\t"
  "v_mfma_f32_16x16x32_bf16 v[116:119], a[204:207], v[40:43], v[116:119]\n\t"
  "ds_read_b64_tr_b16 a[234:235], v221 offset:5120\n\t"
  "v_cvt_pk_bf16_f32 v171, v174, v175\n\t"
  "v_mfma_f32_16x16x32_bf16 v[124:127], a[204:207], v[56:59], v[124:127]\n\t"
  "ds_read_b64_tr_b16 a[236:237], v220 offset:1536\n\t"
  "v_mfma_f32_16x16x32_bf16 v[132:135], a[204:207], v[72:75], v[132:135]\n\t"
  "v_exp_f32 v181, v181\n\t"
  "v_mfma_f32_16x16x32_bf16 v[140:143], a[204:207], v[88:91], v[140:143]\n\t"
  "ds_read_b64_tr_b16 a[238:239], v220 offset:5632\n\t"
  "v_mfma_f32_16x16x32_bf16 v[148:151], a[204:207], v[104:107], v[148:151]\n\t"
  "v_exp_f32 v182, v182\n\t"
  "s_waitcnt vmcnt(4)\n\t"
  "buffer_load_dwordx4 v214, s[28:31], s52 offen offset:2048 nt lds\n\t"
  "v_mfma_f32_16x16x32_bf16 v[116:119], a[208:211], v[44:47], v[116:119]\n\t"
  "v_mfma_f32_16x16x32_bf16 v[124:127], a[208:211], v[60:63], v[124:127]\n\t"
  "v_mfma_f32_16x16x32_bf16 v[132:135], a[208:211], v[76:79], v[132:135]\n\t"
  "ds_read_b64_tr_b16 a[240:241], v221 offset:1536\n\t"
  "v_cvt_pk_bf16_f32 v178, v180, v181\n\t"
  "v_mfma_f32_16x16x32_bf16 v[140:143], a[208:211], v[92:95], v[140:143]\n\t"
  "v_exp_f32 v183, v183\n\t"
  "ds_read_b64_tr_b16 a[242:243], v221 offset:5632\n\t"
  "v_exp_f32 v188, v188\n\t"
  "v_cvt_pk_bf16_f32 v179, v182, v183\n\t"
  "v_exp_f32 v189, v189\n\t"
  "v_exp_f32 v190, v190\n\t"
  "v_cvt_pk_bf16_f32 v186, v188, v189\n\t"
  "v_exp_f32 v191, v191\n\t"
  "s_nop 0\n\t"
  "v_cvt_pk_bf16_f32 v187, v190, v191\n\t"
  "v_mfma_f32_16x16x32_bf16 v[148:151], a[208:211], v[108:111], v[148:151]\n\t"
  "s_waitcnt vmcnt(15) lgkmcnt(0)\n\t"
  "s_barrier\n\t"
  "v_mfma_f32_16x16x32_bf16 a[0:3], a[212:215], v[152:155], a[0:3]\n\t"
  "v_exp_f32 v112, v112\n\t"
  "v_add_u32 v216, 0xfffe0000, v216\n\t"
  "v_add_u32 v217, 0xfffe0000, v217\n\t"
  "v_add_u32 v218, 0xfffe0000, v218\n\t"
  "v_add_u32 v219, 0xfffe0000, v219\n\t"
  "v_mfma_f32_16x16x32_bf16 a[4:7], a[212:215], v[160:163], a[4:7]\n\t"
  "v_mfma_f32_16x16x32_bf16 a[8:11], a[212:215], v[168:171], a[8:11]\n\t"
  "s_waitcnt vmcnt(4)\n\t"
  "buffer_load_dwordx4 v215, s[28:31], s52 offen offset:3072 nt lds\n\t"
  "v_mfma_f32_16x16x32_bf16 a[12:15], a[212:215], v[176:179], a[12:15]\n\t"
  "v_mfma_f32_16x16x32_bf16 a[16:19], a[212:215], v[184:187], a[16:19]\n\t"
  "v_exp_f32 v113, v113\n\t"
  "v_mfma_f32_16x16x32_bf16 a[20:23], a[216:219], v[152:155], a[20:23]\n\t"
  "v_exp_f32 v114, v114\n\t"
  "s_waitcnt vmcnt(4)\n\t"
  "s_add_u32 m0, s51, 98304\n\t"
  "s_mov_b32 s52, 57344\n\t"
  "buffer_load_dwordx4 v212, s[28:31], s52 offen offset:0 nt lds\n\t"
  "v_mfma_f32_16x16x32_bf16 a[24:27], a[216:219], v[160:163], a[24:27]\n\t"
  "v_mfma_f32_16x16x32_bf16 a[28:31], a[216:219], v[168:171], a[28:31]\n\t"
  "v_mfma_f32_16x16x32_bf16 a[32:35], a[216:219], v[176:179], a[32:35]\n\t"
  "v_cvt_pk_bf16_f32 v112, v112, v113\n\t"
  "v_mfma_f32_16x16x32_bf16 a[36:39], a[216:219], v[184:187], a[36:39]\n\t"
  "v_exp_f32 v115, v115\n\t"
  "v_mfma_f32_16x16x32_bf16 a[40:43], a[220:223], v[152:155], a[40:43]\n\t"
  "v_exp_f32 v120, v120\n\t"
  "v_cvt_pk_bf16_f32 v113, v114, v115\n\t"
  "v_mfma_f32_16x16x32_bf16 a[44:47], a[220:223], v[160:163], a[44:47]\n\t"
  "ds_read_b128 a[180:183], v216 offset:0\n\t"
  "s_waitcnt vmcnt(4)\n\t"
  "buffer_load_dwordx4 v213, s[28:31], s52 offen offset:1024 nt lds\n\t"
  "v_mfma_f32_16x16x32_bf16 a[48:51], a[220:223], v[168:171], a[48:51]\n\t"
  "v_mfma_f32_16x16x32_bf16 a[52:55], a[220:223], v[176:179], a[52:55]\n\t"
  "v_exp_f32 v121, v121\n\t"
  "v_mfma_f32_16x16x32_bf16 a[56:59], a[220:223], v[184:187], a[56:59]\n\t"
  "v_exp_f32 v122, v122\n\t"
  "v_mfma_f32_16x16x32_bf16 a[60:63], a[224:227], v[152:155], a[60:63]\n\t"
  "ds_read_b128 a[184:187], v217 offset:0\n\t"
  "v_cvt_pk_bf16_f32 v120, v120, v121\n\t"
  "s_waitcnt vmcnt(4)\n\t"
  "buffer_load_dwordx4 v214, s[28:31], s52 offen offset:2048 nt lds\n\t"
  "v_mfma_f32_16x16x32_bf16 a[64:67], a[224:227], v[160:163], a[64:67]\n\t"
  "v_mfma_f32_16x16x32_bf16 a[68:71], a[224:227], v[168:171], a[68:71]\n\t"
  "v_mfma_f32_16x16x32_bf16 a[72:75], a[224:227], v[176:179], a[72:75]\n\t"
  "v_exp_f32 v123, v123\n\t"
  "ds_read_b128 a[188:191], v218 offset:0\n\t"
  "v_mfma_f32_16x16x32_bf16 a[76:79], a[224:227], v[184:187], a[76:79]\n\t"
  "v_exp_f32 v128, v128\n\t"
  "v_mfma_f32_16x16x32_bf16 a[80:83], a[228:231], v[152:155], a[80:83]\n\t"
  "ds_read_b128 a[192:195], v219 offset:0\n\t"
  "s_mov_b32 %12, s76\n\t"
  "v_cvt_pk_bf16_f32 v121, v122, v123\n\t"
  "v_mfma_f32_16x16x32_bf16 a[84:87], a[228:231], v[160:163], a[84:87]\n\t"
  "v_and_b32 v9, 3, v244\n\t"
  "v_mfma_f32_16x16x32_bf16 a[88:91], a[228:231], v[168:171], a[88:91]\n\t"
  "v_exp_f32 v129, v129\n\t"
  "s_and_b32 s48, %14, 63\n\t"
  "v_mfma_f32_16x16x32_bf16 a[92:95], a[228:231], v[176:179], a[92:95]\n\t"
  "ds_read_b128 a[196:199], v216 offset:4096\n\t"
  "v_lshl_add_u32 v9, v9, 6, s48\n\t"
  "v_mfma_f32_16x16x32_bf16 a[96:99], a[228:231], v[184:187], a[96:99]\n\t"
  "v_exp_f32 v130, v130\n\t"
  "v_lshlrev_b32 v9, 7, v9\n\t"
  "v_mfma_f32_16x16x32_bf16 a[100:103], a[232:235], v[152:155], a[100:103]\n\t"
  "s_mov_b64 %11, s[70:71]\n\t"
  "v_cvt_pk_bf16_f32 v128, v128, v129\n\t"
  "v_mfma_f32_16x16x32_bf16 a[104:107], a[232:235], v[160:163], a[104:107]\n\t"
  "ds_read_b128 a[200:203], v217 offset:4096\n\t"
  "s_mov_b32 %5, s72\n\t"
  "v_mfma_f32_16x16x32_bf16 a[108:111], a[232:235], v[168:171], a[108:111]\n\t"
  "v_exp_f32 v131, v131\n\t"
  "s_mov_b32 %6, s73\n\t"
  "v_mfma_f32_16x16x32_bf16 a[112:115], a[232:235], v[176:179], a[112:115]\n\t"
  "s_bfe_u32 s48, %14, 0x10002\n\t"
  "ds_read_b128 a[204:207], v218 offset:4096\n\t"
  "v_mfma_f32_16x16x32_bf16 a[116:119], a[232:235], v[184:187], a[116:119]\n\t"
  "v_exp_f32 v136, v136\n\t"
  "s_lshl_b32 s49, s48, 3\n\t"
  "v_mfma_f32_16x16x32_bf16 a[120:123], a[236:239], v[152:155], a[120:123]\n\t"
  "s_add_u32 s49, s49, s57\n\t"
  "v_cvt_pk_bf16_f32 v129, v130, v131\n\t"
  "v_mfma_f32_16x16x32_bf16 a[124:127], a[236:239], v[160:163], a[124:127]\n\t"
  "s_mul_i32 s48, s48, 1280\n\t"
  "ds_read_b128 a[208:211], v219 offset:4096\n\t"
  "v_mfma_f32_16x16x32_bf16 a[128:131], a[236:239], v[168:171], a[128:131]\n\t"
  "v_exp_f32 v137, v137\n\t"
  "s_mul_i32 s64, s57, 409600\n\t"
  "v_mfma_f32_16x16x32_bf16 a[132:135], a[236:239], v[176:179], a[132:135]\n\t"
  "s_add_u32 s48, s48, s64\n\t"
  "v_mfma_f32_16x16x32_bf16 a[136:139], a[236:239], v[184:187], a[136:139]\n\t"
  "v_exp_f32 v138, v138\n\t"
  "s_mul_i32 s64, s77, 320\n\t"
  "v_mfma_f32_16x16x32_bf16 a[140:143], a[240:243], v[152:155], a[140:143]\n\t"
  "s_add_u32 s48, s48, s64\n\t"
  "v_cvt_pk_bf16_f32 v136, v136, v137\n\t"
  "v_mfma_f32_16x16x32_bf16 a[144:147], a[240:243], v[160:163], a[144:147]\n\t"
  "s_add_u32 %7, s62, s48\n\t"
  "s_addc_u32 %8, s63, 0\n\t"
  "v_mfma_f32_16x16x32_bf16 a[148:151], a[240:243], v[168:171], a[148:151]\n\t"
  "v_exp_f32 v139, v139\n\t"
  "s_lshl_b32 s48, s49, 2\n\t"
  "v_mfma_f32_16x16x32_bf16 a[152:155], a[240:243], v[176:179], a[152:155]\n\t"
  "v_exp_f32 v144, v144\n\t"
  "v_mfma_f32_16x16x32_bf16 a[156:159], a[240:243], v[184:187], a[156:159]\n\t"
  "s_add_u32 s48, s48, s77\n\t"
  "s_mul_i32 s48, s48, 327680\n\t"
  "v_cvt_pk_bf16_f32 v137, v138, v139\n\t"
  "v_mfma_f32_16x16x32_bf16 a[160:163], a[244:247], v[152:155], a[160:163]\n\t"
  "s_sub_u32 s48, 33554432, s48\n\t"
  "v_mfma_f32_16x16x32_bf16 a[164:167], a[244:247], v[160:163], a[164:167]\n\t"
  "v_exp_f32 v145, v145\n\t"
  "v_mfma_f32_16x16x32_bf16 a[168:171], a[244:247], v[168:171], a[168:171]\n\t"
  "s_sub_u32 %9, s70, s48\n\t"
  "s_subb_u32 %10, s71, 0\n\t"
  "s_lshl_b32 s48, s49, 3\n\t"
  "v_mfma_f32_16x16x32_bf16 a[172:175], a[244:247], v[176:179], a[172:175]\n\t"
  "v_exp_f32 v146, v146\n\t"
  "s_add_u32 s48, s48, s79\n\t"
  "s_mul_i32 s48, s48, 163840\n\t"
  "v_cvt_pk_bf16_f32 v144, v144, v145\n\t"
  "s_sub_u32 s48, 67108864, s48\n\t"
  "v_exp_f32 v147, v147\n\t"
  "s_sub_u32 %3, s70, s48\n\t"
  "s_subb_u32 %4, s71, 0\n\t"
  "s_nop 0\n\t"
  "v_cvt_pk_bf16_f32 v145, v146, v147\n\t"
  "v_mfma_f32_16x16x32_bf16 a[176:179], a[244:247], v[184:187], a[176:179]\n\t"
  "s_waitcnt lgkmcnt(0)\n\t"
  "v_mfma_f32_16x16x32_bf16 v[152:155], a[180:183], v[32:35], v[192:195]\n\t"
  "v_exp_f32 v116, v116\n\t"
  "v_mfma_f32_16x16x32_bf16 v[160:163], a[180:183], v[48:51], v[196:199]\n\t"
  "v_add_u32 v220, 0xfffe0000, v220\n\t"
  "v_add_u32 v221, 0xfffe0000, v221\n\t"
  "v_mfma_f32_16x16x32_bf16 v[168:171], a[180:183], v[64:67], v[200:203]\n\t"
  "v_exp_f32 v117, v117\n\t"
  "v_mfma_f32_16x16x32_bf16 v[176:179], a[180:183], v[80:83], v[204:207]\n\t"
  "v_exp_f32 v118, v118\n\t"
  "s_waitcnt vmcnt(4)\n\t"
  "buffer_load_dwordx4 v215, s[28:31], s52 offen offset:3072 nt lds\n\t"
  "v_mfma_f32_16x16x32_bf16 v[184:187], a[180:183], v[96:99], v[208:211]\n\t"
  "v_mfma_f32_16x16x32_bf16 v[152:155], a[184:187], v[36:39], v[152:155]\n\t"
  "v_mfma_f32_16x16x32_bf16 v[160:163], a[184:187], v[52:55], v[160:163]\n\t"
  "ds_read_b64_tr_b16 a[212:213], v220 offset:0\n\t"
  "v_cvt_pk_bf16_f32 v114, v116, v117\n\t"
  "v_mfma_f32_16x16x32_bf16 v[168:171], a[184:187], v[68:71], v[168:171]\n\t"
  "ds_read_b64_tr_b16 a[214:215], v220 offset:4096\n\t"
  "v_mfma_f32_16x16x32_bf16 v[176:179], a[184:187], v[84:87], v[176:179]\n\t"
  "v_exp_f32 v119, v119\n\t"
  "v_mfma_f32_16x16x32_bf16 v[184:187], a[184:187], v[100:103], v[184:187]\n\t"
  "ds_read_b64_tr_b16 a[216:217], v221 offset:0\n\t"
  "v_mfma_f32_16x16x32_bf16 v[152:155], a[188:191], v[40:43], v[152:155]\n\t"
  "v_exp_f32 v124, v124\n\t"
  "v_mfma_f32_16x16x32_bf16 v[160:163], a[188:191], v[56:59], v[160:163]\n\t"
  "v_cvt_pk_bf16_f32 v115, v118, v119\n\t"
  "ds_read_b64_tr_b16 a[218:219], v221 offset:4096\n\t"
  "v_mfma_f32_16x16x32_bf16 v[168:171], a[188:191], v[72:75], v[168:171]\n\t"
  "v_exp_f32 v125, v125\n\t"
  "v_mfma_f32_16x16x32_bf16 v[176:179], a[188:191], v[88:91], v[176:179]\n\t"
  "ds_read_b64_tr_b16 a[220:221], v220 offset:512\n\t"
  "v_mfma_f32_16x16x32_bf16 v[184:187], a[188:191], v[104:107], v[184:187]\n\t"
  "v_exp_f32 v126, v126\n\t"
  "v_mfma_f32_16x16x32_bf16 v[152:155], a[192:195], v[44:47], v[152:155]\n\t"
  "ds_read_b64_tr_b16 a[222:223], v220 offset:4608\n\t"
  "v_cvt_pk_bf16_f32 v122, v124, v125\n\t"
  "s_waitcnt vmcnt(4)\n\t"
  "s_add_u32 m0, s51, 102400\n\t"
  "s_mov_b32 s52, 61440\n\t"
  "buffer_load_dwordx4 v212, s[28:31], s52 offen offset:0 nt lds\n\t"
  "v_mfma_f32_16x16x32_bf16 v[160:163], a[192:195], v[60:63], v[160:163]\n\t"
  "v_mfma_f32_16x16x32_bf16 v[168:171], a[192:195], v[76:79], v[168:171]\n\t"
  "v_mfma_f32_16x16x32_bf16 v[176:179], a[192:195], v[92:95], v[176:179]\n\t"
  "v_mfma_f32_16x16x32_bf16 v[184:187], a[192:195], v[108:111], v[184:187]\n\t"
  "v_exp_f32 v127, v127\n\t"
  "v_mfma_f32_16x16x32_bf16 v[156:159], a[196:199], v[32:35], v[192:195]\n\t"
  "ds_read_b64_tr_b16 a[224:225], v221 offset:512\n\t"
  "v_mfma_f32_16x16x32_bf16 v[164:167], a[196:199], v[48:51], v[196:199]\n\t"
  "v_exp_f32 v132, v132\n\t"
  "v_mfma_f32_16x16x32_bf16 v[172:175], a[196:199], v[64:67], v[200:203]\n\t"
  "ds_read_b64_tr_b16 a[226:227], v221 offset:4608\n\t"
  "v_cvt_pk_bf16_f32 v123, v126, v127\n\t"
  "v_mfma_f32_16x16x32_bf16 v[180:183], a[196:199], v[80:83], v[204:207]\n\t"
  "v_exp_f32 v133, v133\n\t"
  "v_mfma_f32_16x16x32_bf16 v[188:191], a[196:199], v[96:99], v[208:211]\n\t"
  "ds_read_b64_tr_b16 a[228:229], v220 offset:1024\n\t"
  "v_mfma_f32_16x16x32_bf16 v[156:159], a[200:203], v[36:39], v[156:159]\n\t"
  "v_exp_f32 v134, v134\n\t"
  "v_mfma_f32_16x16x32_bf16 v[164:167], a[200:203], v[52:55], v[164:167]\n\t"
  "ds_read_b64_tr_b16 a[230:231], v220 offset:5120\n\t"
  "v_cvt_pk_bf16_f32 v130, v132, v133\n\t"
  "v_mfma_f32_16x16x32_bf16 v[172:175], a[200:203], v[68:71], v[172:175]\n\t"
  "ds_read_b64_tr_b16 a[232:233], v221 offset:1024\n\t"
  "v_mfma_f32_16x16x32_bf16 v[180:183], a[200:203], v[84:87], v[180:183]\n\t"
  "v_exp_f32 v135, v135\n\t"
  "v_mfma_f32_16x16x32_bf16 v[188:191], a[200:203], v[100:103], v[188:191]\n\t"
  "v_exp_f32 v140, v140\n\t"
  "v_mfma_f32_16x16x32_bf16 v[156:159], a[204:207], v[40:43], v[156:159]\n\t"
  "ds_read_b64_tr_b16 a[234:235], v221 offset:5120\n\t"
  "v_cvt_pk_bf16_f32 v131, v134, v135\n\t"
  "v_mfma_f32_16x16x32_bf16 v[164:167], a[204:207], v[56:59], v[164:167]\n\t"
  "ds_read_b64_tr_b16 a[236:237], v220 offset:1536\n\t"
  "v_mfma_f32_16x16x32_bf16 v[172:175], a[204:207], v[72:75], v[172:175]\n\t"
  "v_exp_f32 v141, v141\n\t"
  "v_mfma_f32_16x16x32_bf16 v[180:183], a[204:207], v[88:91], v[180:183]\n\t"
  "ds_read_b64_tr_b16 a[238:239], v220 offset:5632\n\t"
  "v_mfma_f32_16x16x32_bf16 v[188:191], a[204:207], v[104:107], v[188:191]\n\t"
  "v_exp_f32 v142, v142\n\t"
  "s_waitcnt vmcnt(4)\n\t"
  "buffer_load_dwordx4 v213, s[28:31], s52 offen offset:1024 nt lds\n\t"
  "v_mfma_f32_16x16x32_bf16 v[156:159], a[208:211], v[44:47], v[156:159]\n\t"
  "v_mfma_f32_16x16x32_bf16 v[164:167], a[208:211], v[60:63], v[164:167]\n\t"
  "v_mfma_f32_16x16x32_bf16 v[172:175], a[208:211], v[76:79], v[172:175]\n\t"
  "ds_read_b64_tr_b16 a[240:241], v221 offset:1536\n\t"
  "v_cvt_pk_bf16_f32 v138, v140, v141\n\t"
  "v_mfma_f32_16x16x32_bf16 v[180:183], a[208:211], v[92:95], v[180:183]\n\t"
  "v_exp_f32 v143, v143\n\t"
  "ds_read_b64_tr_b16 a[242:243], v221 offset:5632\n\t"
  "v_exp_f32 v148, v148\n\t"
  "v_cvt_pk_bf16_f32 v139, v142, v143\n\t"
  "v_exp_f32 v149, v149\n\t"
  "v_exp_f32 v150, v150\n\t"
  "v_cvt_pk_bf16_f32 v146, v148, v149\n\t"
  "v_exp_f32 v151, v151\n\t"
  "s_nop 0\n\t"
  "v_cvt_pk_bf16_f32 v147, v150, v151\n\t"
  "v_mfma_f32_16x16x32_bf16 v[188:191], a[208:211], v[108:111], v[188:191]\n\t"
  "s_waitcnt vmcnt(14) lgkmcnt(0)\n\t"
  "s_barrier\n\t"
  "v_mfma_f32_16x16x32_bf16 a[0:3], a[212:215], v[112:115], a[0:3]\n\t"
  "v_exp_f32 v152, v152\n\t"
  "v_mfma_f32_16x16x32_bf16 a[4:7], a[212:215], v[120:123], a[4:7]\n\t"
  "ds_read_b128 a[180:183], v216 offset:32768\n\t"
  "s_waitcnt vmcnt(4)\n\t"
  "buffer_load_dwordx4 v214, s[28:31], s52 offen offset:2048 nt lds\n\t"
  "v_mfma_f32_16x16x32_bf16 a[8:11], a[212:215], v[128:131], a[8:11]\n\t"
  "v_mfma_f32_16x16x32_bf16 a[12:15], a[212:215], v[136:139], a[12:15]\n\t"
  "v_mfma_f32_16x16x32_bf16 a[16:19], a[212:215], v[144:147], a[16:19]\n\t"
  "v_exp_f32 v153, v153\n\t"
  "v_mfma_f32_16x16x32_bf16 a[20:23], a[216:219], v[112:115], a[20:23]\n\t"
  "v_exp_f32 v154, v154\n\t"
  "v_mfma_f32_16x16x32_bf16 a[24:27], a[216:219], v[120:123], a[24:27]\n\t"
  "ds_read_b128 a[184:187], v217 offset:32768\n\t"
  "v_cvt_pk_bf16_f32 v152, v152, v153\n\t"
  "v_mfma_f32_16x16x32_bf16 a[28:31], a[216:219], v[128:131], a[28:31]\n\t"
  "v_exp_f32 v155, v155\n\t"
  "v_mfma_f32_16x16x32_bf16 a[32:35], a[216:219], v[136:139], a[32:35]\n\t"
  "ds_read_b128 a[188:191], v218 offset:32768\n\t"
  "v_mfma_f32_16x16x32_bf16 a[36:39], a[216:219], v[144:147], a[36:39]\n\t"
  "v_exp_f32 v160, v160\n\t"
  "v_mfma_f32_16x16x32_bf16 a[40:43], a[220:223], v[112:115], a[40:43]\n\t"
  "v_cvt_pk_bf16_f32 v153, v154, v155\n\t"
  "v_mfma_f32_16x16x32_bf16 a[44:47], a[220:223], v[120:123], a[44:47]\n\t"
  "v_exp_f32 v161, v161\n\t"
  "v_mfma_f32_16x16x32_bf16 a[48:51], a[220:223], v[128:131], a[48:51]\n\t"
  "s_waitcnt vmcnt(4)\n\t"
  "buffer_load_dwordx4 v215, s[28:31], s52 offen offset:3072 nt lds\n\t"
  "v_mfma_f32_16x16x32_bf16 a[52:55], a[220:223], v[136:139], a[52:55]\n\t"
  "v_mfma_f32_16x16x32_bf16 a[56:59], a[220:223], v[144:147], a[56:59]\n\t"
  "ds_read_b128 a[192:195], v219 offset:32768\n\t"
  "v_mfma_f32_16x16x32_bf16 a[60:63], a[224:227], v[112:115], a[60:63]\n\t"
  "v_exp_f32 v162, v162\n\t"
  "v_mfma_f32_16x16x32_bf16 a[64:67], a[224:227], v[120:123], a[64:67]\n\t"
  "v_cvt_pk_bf16_f32 v160, v160, v161\n\t"
  "v_mfma_f32_16x16x32_bf16 a[68:71], a[224:227], v[128:131], a[68:71]\n\t"
  "v_exp_f32 v163, v163\n\t"
  "v_mfma_f32_16x16x32_bf16 a[72:75], a[224:227], v[136:139], a[72:75]\n\t"
  "ds_read_b128 a[196:199], v216 offset:36864\n\t"
  "v_mfma_f32_16x16x32_bf16 a[76:79], a[224:227], v[144:147], a[76:79]\n\t"
  "v_exp_f32 v168, v168\n\t"
  "v_mfma_f32_16x16x32_bf16 a[80:83], a[228:231], v[112:115], a[80:83]\n\t"
  "v_cvt_pk_bf16_f32 v161, v162, v163\n\t"
  "ds_read_b128 a[200:203], v217 offset:36864\n\t"
  "v_mfma_f32_16x16x32_bf16 a[84:87], a[228:231], v[120:123], a[84:87]\n\t"
  "v_exp_f32 v169, v169\n\t"
  "v_mfma_f32_16x16x32_bf16 a[88:91], a[228:231], v[128:131], a[88:91]\n\t"
  "s_waitcnt vmcnt(4)\n\t"
  "s_add_u32 m0, s51, 131072\n\t"
  "s_mov_b32 s52, 65536\n\t"
  "buffer_load_dwordx4 v212, s[28:31], s52 offen offset:0 nt lds\n\t"
  "v_mfma_f32_16x16x32_bf16 a[92:95], a[228:231], v[136:139], a[92:95]\n\t"
  "v_mfma_f32_16x16x32_bf16 a[96:99], a[228:231], v[144:147], a[96:99]\n\t"
  "v_mfma_f32_16x16x32_bf16 a[100:103], a[232:235], v[112:115], a[100:103]\n\t"
  "v_exp_f32 v170, v170\n\t"
  "v_mfma_f32_16x16x32_bf16 a[104:107], a[232:235], v[120:123], a[104:107]\n\t"
  "v_cvt_pk_bf16_f32 v168, v168, v169\n\t"
  "ds_read_b128 a[204:207], v218 offset:36864\n\t"
  "v_mfma_f32_16x16x32_bf16 a[108:111], a[232:235], v[128:131], a[108:111]\n\t"
  "v_exp_f32 v171, v171\n\t"
  "v_mfma_f32_16x16x32_bf16 a[112:115], a[232:235], v[136:139], a[112:115]\n\t"
  "v_exp_f32 v176, v176\n\t"
  "v_mfma_f32_16x16x32_bf16 a[116:119], a[232:235], v[144:147], a[116:119]\n\t"
  "ds_read_b128 a[208:211], v219 offset:36864\n\t"
  "v_cvt_pk_bf16_f32 v169, v170, v171\n\t"
  "v_mfma_f32_16x16x32_bf16 a[120:123], a[236:239], v[112:115], a[120:123]\n\t"
  "v_exp_f32 v177, v177\n\t"
  "v_mfma_f32_16x16x32_bf16 a[124:127], a[236:239], v[120:123], a[124:127]\n\t"
  "v_exp_f32 v178, v178\n\t"
  "v_mfma_f32_16x16x32_bf16 a[128:131], a[236:239], v[128:131], a[128:131]\n\t"
  "v_cvt_pk_bf16_f32 v176, v176, v177\n\t"
  "v_mfma_f32_16x16x32_bf16 a[132:135], a[236:239], v[136:139], a[132:135]\n\t"
  "v_exp_f32 v179, v179\n\t"
  "v_mfma_f32_16x16x32_bf16 a[136:139], a[236:239], v[144:147], a[136:139]\n\t"
  "s_waitcnt vmcnt(4)\n\t"
  "buffer_load_dwordx4 v213, s[28:31], s52 offen offset:1024 nt lds\n\t"
  "v_mfma_f32_16x16x32_bf16 a[140:143], a[240:243], v[112:115], a[140:143]\n\t"
  "v_mfma_f32_16x16x32_bf16 a[144:147], a[240:243], v[120:123], a[144:147]\n\t"
  "v_exp_f32 v184, v184\n\t"
  "v_mfma_f32_16x16x32_bf16 a[148:151], a[240:243], v[128:131], a[148:151]\n\t"
  "v_cvt_pk_bf16_f32 v177, v178, v179\n\t"
  "v_mfma_f32_16x16x32_bf16 a[152:155], a[240:243], v[136:139], a[152:155]\n\t"
  "v_exp_f32 v185, v185\n\t"
  "v_mfma_f32_16x16x32_bf16 a[156:159], a[240:243], v[144:147], a[156:159]\n\t"
  "v_exp_f32 v186, v186\n\t"
  "v_mfma_f32_16x16x32_bf16 a[160:163], a[244:247], v[112:115], a[160:163]\n\t"
  "v_cvt_pk_bf16_f32 v184, v184, v185\n\t"
  "v_mfma_f32_16x16x32_bf16 a[164:167], a[244:247], v[120:123], a[164:167]\n\t"
  "v_exp_f32 v187, v187\n\t"
  "v_mfma_f32_16x16x32_bf16 a[168:171], a[244:247], v[128:131], a[168:171]\n\t"
  "v_mfma_f32_16x16x32_bf16 a[172:175], a[244:247], v[136:139], a[172:175]\n\t"
  "s_nop 0\n\t"
  "v_cvt_pk_bf16_f32 v185, v186, v187\n\t"
  "v_mfma_f32_16x16x32_bf16 a[176:179], a[244:247], v[144:147], a[176:179]\n\t"
  "s_waitcnt lgkmcnt(0)\n\t"
  "v_mfma_f32_16x16x32_bf16 v[112:115], a[180:183], v[32:35], v[192:195]\n\t"
  "v_exp_f32 v156, v156\n\t"
  "v_mfma_f32_16x16x32_bf16 v[120:123], a[180:183], v[48:51], v[196:199]\n\t"
  "ds_read_b64_tr_b16 a[212:213], v220 offset:32768\n\t"
  "v_mfma_f32_16x16x32_bf16 v[128:131], a[180:183], v[64:67], v[200:203]\n\t"
  "v_exp_f32 v157, v157\n\t"
  "v_mfma_f32_16x16x32_bf16 v[136:139], a[180:183], v[80:83], v[204:207]\n\t"
  "ds_read_b64_tr_b16 a[214:215], v220 offset:36864\n\t"
  "v_mfma_f32_16x16x32_bf16 v[144:147], a[180:183], v[96:99], v[208:211]\n\t"
  "v_exp_f32 v158, v158\n\t"
  "s_waitcnt vmcnt(4)\n\t"
  "buffer_load_dwordx4 v214, s[28:31], s52 offen offset:2048 nt lds\n\t"
  "v_mfma_f32_16x16x32_bf16 v[112:115], a[184:187], v[36:39], v[112:115]\n\t"
  "v_mfma_f32_16x16x32_bf16 v[120:123], a[184:187], v[52:55], v[120:123]\n\t"
  "v_mfma_f32_16x16x32_bf16 v[128:131], a[184:187], v[68:71], v[128:131]\n\t"
  "ds_read_b64_tr_b16 a[216:217], v221 offset:32768\n\t"
  "v_cvt_pk_bf16_f32 v154, v156, v157\n\t"
  "v_mfma_f32_16x16x32_bf16 v[136:139], a[184:187], v[84:87], v[136:139]\n\t"
  "v_exp_f32 v159, v159\n\t"
  "v_mfma_f32_16x16x32_bf16 v[144:147], a[184:187], v[100:103], v[144:147]\n\t"
  "ds_read_b64_tr_b16 a[218:219], v221 offset:36864\n\t"
  "v_mfma_f32_16x16x32_bf16 v[112:115], a[188:191], v[40:43], v[112:115]\n\t"
  "v_exp_f32 v164, v164\n\t"
  "v_mfma_f32_16x16x32_bf16 v[120:123], a[188:191], v[56:59], v[120:123]\n\t"
  "ds_read_b64_tr_b16 a[220:221], v220 offset:33280\n\t"
  "v_cvt_pk_bf16_f32 v155, v158, v159\n\t"
  "v_mfma_f32_16x16x32_bf16 v[128:131], a[188:191], v[72:75], v[128:131]\n\t"
  "v_exp_f32 v165, v165\n\t"
  "v_mfma_f32_16x16x32_bf16 v[136:139], a[188:191], v[88:91], v[136:139]\n\t"
  "ds_read_b64_tr_b16 a[222:223], v220 offset:37376\n\t"
  "v_mfma_f32_16x16x32_bf16 v[144:147], a[188:191], v[104:107], v[144:147]\n\t"
  "v_exp_f32 v166, v166\n\t"
  "v_mfma_f32_16x16x32_bf16 v[112:115], a[192:195], v[44:47], v[112:115]\n\t"
  "ds_read_b64_tr_b16 a[224:225], v221 offset:33280\n\t"
  "v_cvt_pk_bf16_f32 v162, v164, v165\n\t"
  "s_waitcnt vmcnt(4)\n\t"
  "buffer_load_dwordx4 v215, s[28:31], s52 offen offset:3072 nt lds\n\t"
  "v_mfma_f32_16x16x32_bf16 v[120:123], a[192:195], v[60:63], v[120:123]\n\t"
  "v_mfma_f32_16x16x32_bf16 v[128:131], a[192:195], v[76:79], v[128:131]\n\t"
  "v_mfma_f32_16x16x32_bf16 v[136:139], a[192:195], v[92:95], v[136:139]\n\t"
  "v_exp_f32 v167, v167\n\t"
  "v_mfma_f32_16x16x32_bf16 v[144:147], a[192:195], v[108:111], v[144:147]\n\t"
  "ds_read_b64_tr_b16 a[226:227], v221 offset:37376\n\t"
  "v_mfma_f32_16x16x32_bf16 v[116:119], a[196:199], v[32:35], v[192:195]\n\t"
  "v_exp_f32 v172, v172\n\t"
  "v_mfma_f32_16x16x32_bf16 v[124:127], a[196:199], v[48:51], v[196:199]\n\t"
  "ds_read_b64_tr_b16 a[228:229], v220 offset:33792\n\t"
  "v_cvt_pk_bf16_f32 v163, v166, v167\n\t"
  "v_mfma_f32_16x16x32_bf16 v[132:135], a[196:199], v[64:67], v[200:203]\n\t"
  "v_exp_f32 v173, v173\n\t"
  "v_mfma_f32_16x16x32_bf16 v[140:143], a[196:199], v[80:83], v[204:207]\n\t"
  "ds_read_b64_tr_b16 a[230:231], v220 offset:37888\n\t"
  "v_mfma_f32_16x16x32_bf16 v[148:151], a[196:199], v[96:99], v[208:211]\n\t"
  "v_exp_f32 v174, v174\n\t"
  "v_mfma_f32_16x16x32_bf16 v[116:119], a[200:203], v[36:39], v[116:119]\n\t"
  "ds_read_b64_tr_b16 a[232:233], v221 offset:33792\n\t"
  "v_cvt_pk_bf16_f32 v170, v172, v173\n\t"
  "v_mfma_f32_16x16x32_bf16 v[124:127], a[200:203], v[52:55], v[124:127]\n\t"
  "v_exp_f32 v175, v175\n\t"
  "v_mfma_f32_16x16x32_bf16 v[132:135], a[200:203], v[68:71], v[132:135]\n\t"
  "ds_read_b64_tr_b16 a[234:235], v221 offset:37888\n\t"
  "v_mfma_f32_16x16x32_bf16 v[140:143], a[200:203], v[84:87], v[140:143]\n\t"
  "v_exp_f32 v180, v180\n\t"
  "s_waitcnt vmcnt(4)\n\t"
  "s_add_u32 m0, s51, 135168\n\t"
  "s_mov_b32 s52, 69632\n\t"
  "buffer_load_dwordx4 v212, s[28:31], s52 offen offset:0 nt lds\n\t"
  "v_mfma_f32_16x16x32_bf16 v[148:151], a[200:203], v[100:103], v[148:151]\n\t"
  "v_mfma_f32_16x16x32_bf16 v[116:119], a[204:207], v[40:43], v[116:119]\n\t"
  "v_mfma_f32_16x16x32_bf16 v[124:127], a[204:207], v[56:59], v[124:127]\n\t"
  "v_mfma_f32_16x16x32_bf16 v[132:135], a[204:207], v[72:75], v[132:135]\n\t"
  "ds_read_b64_tr_b16 a[236:237], v220 offset:34304\n\t"
  "v_cvt_pk_bf16_f32 v171, v174, v175\n\t"
  "v_mfma_f32_16x16x32_bf16 v[140:143], a[204:207], v[88:91], v[140:143]\n\t"
  "v_exp_f32 v181, v181\n\t"
  "v_mfma_f32_16x16x32_bf16 v[148:151], a[204:207], v[104:107], v[148:151]\n\t"
  "ds_read_b64_tr_b16 a[238:239], v220 offset:38400\n\t"
  "v_mfma_f32_16x16x32_bf16 v[116:119], a[208:211], v[44:47], v[116:119]\n\t"
  "v_exp_f32 v182, v182\n\t"
  "v_mfma_f32_16x16x32_bf16 v[124:127], a[208:211], v[60:63], v[124:127]\n\t"
  "ds_read_b64_tr_b16 a[240:241], v221 offset:34304\n\t"
  "v_cvt_pk_bf16_f32 v178, v180, v181\n\t"
  "v_mfma_f32_16x16x32_bf16 v[132:135], a[208:211], v[76:79], v[132:135]\n\t"
  "v_exp_f32 v183, v183\n\t"
  "v_mfma_f32_16x16x32_bf16 v[140:143], a[208:211], v[92:95], v[140:143]\n\t"
  "ds_read_b64_tr_b16 a[242:243], v221 offset:38400\n\t"
  "v_exp_f32 v188, v188\n\t"
  "v_cvt_pk_bf16_f32 v179, v182, v183\n\t"
  "v_exp_f32 v189, v189\n\t"
  "v_exp_f32 v190, v190\n\t"
  "v_cvt_pk_bf16_f32 v186, v188, v189\n\t"
  "v_exp_f32 v191, v191\n\t"
  "s_nop 0\n\t"
  "v_cvt_pk_bf16_f32 v187, v190, v191\n\t"
  "v_mfma_f32_16x16x32_bf16 v[148:151], a[208:211], v[108:111], v[148:151]\n\t"
  "s_waitcnt vmcnt(13) lgkmcnt(0)\n\t"
  "s_barrier\n\t"
  "v_mfma_f32_16x16x32_bf16 a[0:3], a[212:215], v[152:155], a[0:3]\n\t"
  "v_exp_f32 v112, v112\n\t"
  "v_mfma_f32_16x16x32_bf16 a[4:7], a[212:215], v[160:163], a[4:7]\n\t"
  "v_add_u32 v216, 65536, v216\n\t"
  "v_add_u32 v217, 65536, v217\n\t"
  "v_add_u32 v218, 65536, v218\n\t"
  "v_add_u32 v219, 65536, v219\n\t"
  "v_mfma_f32_16x16x32_bf16 a[8:11], a[212:215], v[168:171], a[8:11]\n\t"
  "v_mfma_f32_16x16x32_bf16 a[12:15], a[212:215], v[176:179], a[12:15]\n\t"
  "s_waitcnt vmcnt(4)\n\t"
  "buffer_load_dwordx4 v213, s[28:31], s52 offen offset:1024 nt lds\n\t"
  "v_mfma_f32_16x16x32_bf16 a[16:19], a[212:215], v[184:187], a[16:19]\n\t"
  "v_mfma_f32_16x16x32_bf16 a[20:23], a[216:219], v[152:155], a[20:23]\n\t"
  "v_exp_f32 v113, v113\n\t"
  "v_mfma_f32_16x16x32_bf16 a[24:27], a[216:219], v[160:163], a[24:27]\n\t"
  "v_exp_f32 v114, v114\n\t"
  "v_mfma_f32_16x16x32_bf16 a[28:31], a[216:219], v[168:171], a[28:31]\n\t"
  "v_cvt_pk_bf16_f32 v112, v112, v113\n\t"
  "v_mfma_f32_16x16x32_bf16 a[32:35], a[216:219], v[176:179], a[32:35]\n\t"
  "v_exp_f32 v115, v115\n\t"
  "v_mfma_f32_16x16x32_bf16 a[36:39], a[216:219], v[184:187], a[36:39]\n\t"
  "v_exp_f32 v120, v120\n\t"
  "v_mfma_f32_16x16x32_bf16 a[40:43], a[220:223], v[152:155], a[40:43]\n\t"
  "v_cvt_pk_bf16_f32 v113, v114, v115\n\t"
  "ds_read_b128 a[180:183], v216 offset:0\n\t"
  "v_mfma_f32_16x16x32_bf16 a[44:47], a[220:223], v[160:163], a[44:47]\n\t"
  "v_exp_f32 v121, v121\n\t"
  "v_mfma_f32_16x16x32_bf16 a[48:51], a[220:223], v[168:171], a[48:51]\n\t"
  "s_waitcnt vmcnt(4)\n\t"
  "buffer_load_dwordx4 v214, s[28:31], s52 offen offset:2048 nt lds\n\t"
  "v_mfma_f32_16x16x32_bf16 a[52:55], a[220:223], v[176:179], a[52:55]\n\t"
  "v_mfma_f32_16x16x32_bf16 a[56:59], a[220:223], v[184:187], a[56:59]\n\t"
  "v_exp_f32 v122, v122\n\t"
  "v_mfma_f32_16x16x32_bf16 a[60:63], a[224:227], v[152:155], a[60:63]\n\t"
  "ds_read_b128 a[184:187], v217 offset:0\n\t"
  "v_cvt_pk_bf16_f32 v120, v120, v121\n\t"
  "v_mfma_f32_16x16x32_bf16 a[64:67], a[224:227], v[160:163], a[64:67]\n\t"
  "v_exp_f32 v123, v123\n\t"
  "v_mfma_f32_16x16x32_bf16 a[68:71], a[224:227], v[168:171], a[68:71]\n\t"
  "ds_read_b128 a[188:191], v218 offset:0\n\t"
  "v_mfma_f32_16x16x32_bf16 a[72:75], a[224:227], v[176:179], a[72:75]\n\t"
  "v_exp_f32 v128, v128\n\t"
  "v_mfma_f32_16x16x32_bf16 a[76:79], a[224:227], v[184:187], a[76:79]\n\t"
  "ds_read_b128 a[192:195], v219 offset:0\n\t"
  "v_cvt_pk_bf16_f32 v121, v122, v123\n\t"
  "v_mfma_f32_16x16x32_bf16 a[80:83], a[228:231], v[152:155], a[80:83]\n\t"
  "v_exp_f32 v129, v129\n\t"
  "v_mfma_f32_16x16x32_bf16 a[84:87], a[228:231], v[160:163], a[84:87]\n\t"
  "s_waitcnt vmcnt(4)\n\t"
  "buffer_load_dwordx4 v215, s[28:31], s52 offen offset:3072 nt lds\n\t"
  "v_mfma_f32_16x16x32_bf16 a[88:91], a[228:231], v[168:171], a[88:91]\n\t"
  "v_mfma_f32_16x16x32_bf16 a[92:95], a[228:231], v[176:179], a[92:95]\n\t"
  "ds_read_b128 a[196:199], v216 offset:4096\n\t"
  "v_mfma_f32_16x16x32_bf16 a[96:99], a[228:231], v[184:187], a[96:99]\n\t"
  "v_exp_f32 v130, v130\n\t"
  "v_mfma_f32_16x16x32_bf16 a[100:103], a[232:235], v[152:155], a[100:103]\n\t"
  "v_cvt_pk_bf16_f32 v128, v128, v129\n\t"
  "ds_read_b128 a[200:203], v217 offset:4096\n\t"
  "v_mfma_f32_16x16x32_bf16 a[104:107], a[232:235], v[160:163], a[104:107]\n\t"
  "v_exp_f32 v131, v131\n\t"
  "v_mfma_f32_16x16x32_bf16 a[108:111], a[232:235], v[168:171], a[108:111]\n\t"
  "ds_read_b128 a[204:207], v218 offset:4096\n\t"
  "v_mfma_f32_16x16x32_bf16 a[112:115], a[232:235], v[176:179], a[112:115]\n\t"
  "v_exp_f32 v136, v136\n\t"
  "v_mfma_f32_16x16x32_bf16 a[116:119], a[232:235], v[184:187], a[116:119]\n\t"
  "v_cvt_pk_bf16_f32 v129, v130, v131\n\t"
  "ds_read_b128 a[208:211], v219 offset:4096\n\t"
  "v_mfma_f32_16x16x32_bf16 a[120:123], a[236:239], v[152:155], a[120:123]\n\t"
  "v_exp_f32 v137, v137\n\t"
  "v_mfma_f32_16x16x32_bf16 a[124:127], a[236:239], v[160:163], a[124:127]\n\t"
  "v_exp_f32 v138, v138\n\t"
  "v_mfma_f32_16x16x32_bf16 a[128:131], a[236:239], v[168:171], a[128:131]\n\t"
  "s_waitcnt vmcnt(4)\n\t"
  "s_add_u32 m0, s51, 0\n\t"
  "s_mov_b32 s52, 73728\n\t"
  "buffer_load_dwordx4 v212, s[28:31], s52 offen offset:0 nt lds\n\t"
  "v_mfma_f32_16x16x32_bf16 a[132:135], a[236:239], v[176:179], a[132:135]\n\t"
  "v_mfma_f32_16x16x32_bf16 a[136:139], a[236:239], v[184:187], a[136:139]\n\t"
  "v_mfma_f32_16x16x32_bf16 a[140:143], a[240:243], v[152:155], a[140:143]\n\t"
  "v_cvt_pk_bf16_f32 v136, v136, v137\n\t"
  "v_mfma_f32_16x16x32_bf16 a[144:147], a[240:243], v[160:163], a[144:147]\n\t"
  "v_exp_f32 v139, v139\n\t"
  "v_mfma_f32_16x16x32_bf16 a[148:151], a[240:243], v[168:171], a[148:151]\n\t"
  "v_exp_f32 v144, v144\n\t"
  "v_mfma_f32_16x16x32_bf16 a[152:155], a[240:243], v[176:179], a[152:155]\n\t"
  "v_cvt_pk_bf16_f32 v137, v138, v139\n\t"
  "v_mfma_f32_16x16x32_bf16 a[156:159], a[240:243], v[184:187], a[156:159]\n\t"
  "v_exp_f32 v145, v145\n\t"
  "v_mfma_f32_16x16x32_bf16 a[160:163], a[244:247], v[152:155], a[160:163]\n\t"
  "v_exp_f32 v146, v146\n\t"
  "v_mfma_f32_16x16x32_bf16 a[164:167], a[244:247], v[160:163], a[164:167]\n\t"
  "v_cvt_pk_bf16_f32 v144, v144, v145\n\t"
  "v_mfma_f32_16x16x32_bf16 a[168:171], a[244:247], v[168:171], a[168:171]\n\t"
  "v_exp_f32 v147, v147\n\t"
  "v_mfma_f32_16x16x32_bf16 a[172:175], a[244:247], v[176:179], a[172:175]\n\t"
  "s_nop 0\n\t"
  "v_cvt_pk_bf16_f32 v145, v146, v147\n\t"
  "v_mfma_f32_16x16x32_bf16 a[176:179], a[244:247], v[184:187], a[176:179]\n\t"
  "s_waitcnt lgkmcnt(0)\n\t"
  "v_mfma_f32_16x16x32_bf16 v[152:155], a[180:183], v[32:35], v[192:195]\n\t"
  "v_exp_f32 v116, v116\n\t"
  "v_mfma_f32_16x16x32_bf16 v[160:163], a[180:183], v[48:51], v[196:199]\n\t"
  "v_add_u32 v220, 65536, v220\n\t"
  "v_add_u32 v221, 65536, v221\n\t"
  "v_mfma_f32_16x16x32_bf16 v[168:171], a[180:183], v[64:67], v[200:203]\n\t"
  "v_exp_f32 v117, v117\n\t"
  "v_mfma_f32_16x16x32_bf16 v[176:179], a[180:183], v[80:83], v[204:207]\n\t"
  "v_exp_f32 v118, v118\n\t"
  "s_waitcnt vmcnt(4)\n\t"
  "buffer_load_dwordx4 v213, s[28:31], s52 offen offset:1024 nt lds\n\t"
  "v_mfma_f32_16x16x32_bf16 v[184:187], a[180:183], v[96:99], v[208:211]\n\t"
  "v_mfma_f32_16x16x32_bf16 v[152:155], a[184:187], v[36:39], v[152:155]\n\t"
  "v_mfma_f32_16x16x32_bf16 v[160:163], a[184:187], v[52:55], v[160:163]\n\t"
  "ds_read_b64_tr_b16 a[212:213], v220 offset:0\n\t"
  "v_cvt_pk_bf16_f32 v114, v116, v117\n\t"
  "v_mfma_f32_16x16x32_bf16 v[168:171], a[184:187], v[68:71], v[168:171]\n\t"
  "ds_read_b64_tr_b16 a[214:215], v220 offset:4096\n\t"
  "v_mfma_f32_16x16x32_bf16 v[176:179], a[184:187], v[84:87], v[176:179]\n\t"
  "v_exp_f32 v119, v119\n\t"
  "v_mfma_f32_16x16x32_bf16 v[184:187], a[184:187], v[100:103], v[184:187]\n\t"
  "ds_read_b64_tr_b16 a[216:217], v221 offset:0\n\t"
  "v_mfma_f32_16x16x32_bf16 v[152:155], a[188:191], v[40:43], v[152:155]\n\t"
  "v_exp_f32 v124, v124\n\t"
  "v_mfma_f32_16x16x32_bf16 v[160:163], a[188:191], v[56:59], v[160:163]\n\t"
  "v_cvt_pk_bf16_f32 v115, v118, v119\n\t"
  "ds_read_b64_tr_b16 a[218:219], v221 offset:4096\n\t"
  "v_mfma_f32_16x16x32_bf16 v[168:171], a[188:191], v[72:75], v[168:171]\n\t"
  "v_exp_f32 v125, v125\n\t"
  "v_mfma_f32_16x16x32_bf16 v[176:179], a[188:191], v[88:91], v[176:179]\n\t"
  "ds_read_b64_tr_b16 a[220:221], v220 offset:512\n\t"
  "v_mfma_f32_16x16x32_bf16 v[184:187], a[188:191], v[104:107], v[184:187]\n\t"
  "v_exp_f32 v126, v126\n\t"
  "v_mfma_f32_16x16x32_bf16 v[152:155], a[192:195], v[44:47], v[152:155]\n\t"
  "ds_read_b64_tr_b16 a[222:223], v220 offset:4608\n\t"
  "v_cvt_pk_bf16_f32 v122, v124, v125\n\t"
  "v_mfma_f32_16x16x32_bf16 v[160:163], a[192:195], v[60:63], v[160:163]\n\t"
  "v_exp_f32 v127, v127\n\t"
  "v_mfma_f32_16x16x32_bf16 v[168:171], a[192:195], v[76:79], v[168:171]\n\t"
  "ds_read_b64_tr_b16 a[224:225], v221 offset:512\n\t"
  "s_waitcnt vmcnt(4)\n\t"
  "buffer_load_dwordx4 v214, s[28:31], s52 offen offset:2048 nt lds\n\t"
  "v_mfma_f32_16x16x32_bf16 v[176:179], a[192:195], v[92:95], v[176:179]\n\t"
  "v_mfma_f32_16x16x32_bf16 v[184:187], a[192:195], v[108:111], v[184:187]\n\t"
  "v_mfma_f32_16x16x32_bf16 v[156:159], a[196:199], v[32:35], v[192:195]\n\t"
  "v_exp_f32 v132, v132\n\t"
  "v_mfma_f32_16x16x32_bf16 v[164:167], a[196:199], v[48:51], v[196:199]\n\t"
  "ds_read_b64_tr_b16 a[226:227], v221 offset:4608\n\t"
  "v_cvt_pk_bf16_f32 v123, v126, v127\n\t"
  "v_mfma_f32_16x16x32_bf16 v[172:175], a[196:199], v[64:67], v[200:203]\n\t"
  "v_exp_f32 v133, v133\n\t"
  "v_mfma_f32_16x16x32_bf16 v[180:183], a[196:199], v[80:83], v[204:207]\n\t"
  "ds_read_b64_tr_b16 a[228:229], v220 offset:1024\n\t"
  "v_mfma_f32_16x16x32_bf16 v[188:191], a[196:199], v[96:99], v[208:211]\n\t"
  "v_exp_f32 v134, v134\n\t"
  "v_mfma_f32_16x16x32_bf16 v[156:159], a[200:203], v[36:39], v[156:159]\n\t"
  "ds_read_b64_tr_b16 a[230:231], v220 offset:5120\n\t"
  "v_cvt_pk_bf16_f32 v130, v132, v133\n\t"
  "v_mfma_f32_16x16x32_bf16 v[164:167], a[200:203], v[52:55], v[164:167]\n\t"
  "ds_read_b64_tr_b16 a[232:233], v221 offset:1024\n\t"
  "v_mfma_f32_16x16x32_bf16 v[172:175], a[200:203], v[68:71], v[172:175]\n\t"
  "v_exp_f32 v135, v135\n\t"
  "v_mfma_f32_16x16x32_bf16 v[180:183], a[200:203], v[84:87], v[180:183]\n\t"
  "v_exp_f32 v140, v140\n\t"
  "v_mfma_f32_16x16x32_bf16 v[188:191], a[200:203], v[100:103], v[188:191]\n\t"
  "ds_read_b64_tr_b16 a[234:235], v221 offset:5120\n\t"
  "v_cvt_pk_bf16_f32 v131, v134, v135\n\t"
  "v_mfma_f32_16x16x32_bf16 v[156:159], a[204:207], v[40:43], v[156:159]\n\t"
  "ds_read_b64_tr_b16 a[236:237], v220 offset:1536\n\t"
  "v_mfma_f32_16x16x32_bf16 v[164:167], a[204:207], v[56:59], v[164:167]\n\t"
  "v_exp_f32 v141, v141\n\t"
  "v_mfma_f32_16x16x32_bf16 v[172:175], a[204:207], v[72:75], v[172:175]\n\t"
  "ds_read_b64_tr_b16 a[238:239], v220 offset:5632\n\t"
  "s_waitcnt vmcnt(4)\n\t"
  "buffer_load_dwordx4 v215, s[28:31], s52 offen offset:3072 nt lds\n\t"
  "v_mfma_f32_16x16x32_bf16 v[180:183], a[204:207], v[88:91], v[180:183]\n\t"
  "v_mfma_f32_16x16x32_bf16 v[188:191], a[204:207], v[104:107], v[188:191]\n\t"
  "v_mfma_f32_16x16x32_bf16 v[156:159], a[208:211], v[44:47], v[156:159]\n\t"
  "v_exp_f32 v142, v142\n\t"
  "v_mfma_f32_16x16x32_bf16 v[164:167], a[208:211], v[60:63], v[164:167]\n\t"
  "ds_read_b64_tr_b16 a[240:241], v221 offset:1536\n\t"
  "v_cvt_pk_bf16_f32 v138, v140, v141\n\t"
  "v_mfma_f32_16x16x32_bf16 v[172:175], a[208:211], v[76:79], v[172:175]\n\t"
  "v_exp_f32 v143, v143\n\t"
  "v_mfma_f32_16x16x32_bf16 v[180:183], a[208:211], v[92:95], v[180:183]\n\t"
  "ds_read_b64_tr_b16 a[242:243], v221 offset:5632\n\t"
  "v_exp_f32 v148, v148\n\t"
  "v_cvt_pk_bf16_f32 v139, v142, v143\n\t"
  "v_exp_f32 v149, v149\n\t"
  "v_exp_f32 v150, v150\n\t"
  "v_cvt_pk_bf16_f32 v146, v148, v149\n\t"
  "v_exp_f32 v151, v151\n\t"
  "s_nop 0\n\t"
  "v_cvt_pk_bf16_f32 v147, v150, v151\n\t"
  "v_mfma_f32_16x16x32_bf16 v[188:191], a[208:211], v[108:111], v[188:191]\n\t"
  "s_waitcnt vmcnt(12) lgkmcnt(0)\n\t"
  "s_barrier\n\t"
  "v_mfma_f32_16x16x32_bf16 a[0:3], a[212:215], v[112:115], a[0:3]\n\t"
  "v_exp_f32 v152, v152\n\t"
  "v_mfma_f32_16x16x32_bf16 a[4:7], a[212:215], v[120:123], a[4:7]\n\t"
  "ds_read_b128 a[180:183], v216 offset:32768\n\t"
  "s_waitcnt vmcnt(4)\n\t"
  "s_add_u32 m0, s51, 4096\n\t"
  "s_mov_b32 s52, 77824\n\t"
  "buffer_load_dwordx4 v212, s[28:31], s52 offen offset:0 nt lds\n\t"
  "v_mfma_f32_16x16x32_bf16 a[8:11], a[212:215], v[128:131], a[8:11]\n\t"
  "v_mfma_f32_16x16x32_bf16 a[12:15], a[212:215], v[136:139], a[12:15]\n\t"
  "v_mfma_f32_16x16x32_bf16 a[16:19], a[212:215], v[144:147], a[16:19]\n\t"
  "v_mfma_f32_16x16x32_bf16 a[20:23], a[216:219], v[112:115], a[20:23]\n\t"
  "v_exp_f32 v153, v153\n\t"
  "v_mfma_f32_16x16x32_bf16 a[24:27], a[216:219], v[120:123], a[24:27]\n\t"
  "v_exp_f32 v154, v154\n\t"
  "v_mfma_f32_16x16x32_bf16 a[28:31], a[216:219], v[128:131], a[28:31]\n\t"
  "ds_read_b128 a[184:187], v217 offset:32768\n\t"
  "v_cvt_pk_bf16_f32 v152, v152, v153\n\t"
  "v_mfma_f32_16x16x32_bf16 a[32:35], a[216:219], v[136:139], a[32:35]\n\t"
  "v_exp_f32 v155, v155\n\t"
  "v_mfma_f32_16x16x32_bf16 a[36:39], a[216:219], v[144:147], a[36:39]\n\t"
  "ds_read_b128 a[188:191], v218 offset:32768\n\t"
  "v_mfma_f32_16x16x32_bf16 a[40:43], a[220:223], v[112:115], a[40:43]\n\t"
  "v_exp_f32 v160, v160\n\t"
  "v_mfma_f32_16x16x32_bf16 a[44:47], a[220:223], v[120:123], a[44:47]\n\t"
  "v_cvt_pk_bf16_f32 v153, v154, v155\n\t"
  "v_mfma_f32_16x16x32_bf16 a[48:51], a[220:223], v[128:131], a[48:51]\n\t"
  "v_exp_f32 v161, v161\n\t"
  "v_mfma_f32_16x16x32_bf16 a[52:55], a[220:223], v[136:139], a[52:55]\n\t"
  "ds_read_b128 a[192:195], v219 offset:32768\n\t"
  "v_mfma_f32_16x16x32_bf16 a[56:59], a[220:223], v[144:147], a[56:59]\n\t"
  "v_exp_f32 v162, v162\n\t"
  "v_mfma_f32_16x16x32_bf16 a[60:63], a[224:227], v[112:115], a[60:63]\n\t"
  "v_cvt_pk_bf16_f32 v160, v160, v161\n\t"
  "s_waitcnt vmcnt(4)\n\t"
  "buffer_load_dwordx4 v213, s[28:31], s52 offen offset:1024 nt lds\n\t"
  "v_mfma_f32_16x16x32_bf16 a[64:67], a[224:227], v[120:123], a[64:67]\n\t"
  "v_mfma_f32_16x16x32_bf16 a[68:71], a[224:227], v[128:131], a[68:71]\n\t"
  "v_mfma_f32_16x16x32_bf16 a[72:75], a[224:227], v[136:139], a[72:75]\n\t"
  "v_exp_f32 v163, v163\n\t"
  "v_mfma_f32_16x16x32_bf16 a[76:79], a[224:227], v[144:147], a[76:79]\n\t"
  "ds_read_b128 a[196:199], v216 offset:36864\n\t"
  "v_mfma_f32_16x16x32_bf16 a[80:83], a[228:231], v[112:115], a[80:83]\n\t"
  "v_exp_f32 v168, v168\n\t"
  "v_mfma_f32_16x16x32_bf16 a[84:87], a[228:231], v[120:123], a[84:87]\n\t"
  "v_cvt_pk_bf16_f32 v161, v162, v163\n\t"
  "ds_read_b128 a[200:203], v217 offset:36864\n\t"
  "v_mfma_f32_16x16x32_bf16 a[88:91], a[228:231], v[128:131], a[88:91]\n\t"
  "v_exp_f32 v169, v169\n\t"
  "v_mfma_f32_16x16x32_bf16 a[92:95], a[228:231], v[136:139], a[92:95]\n\t"
  "v_exp_f32 v170, v170\n\t"
  "v_mfma_f32_16x16x32_bf16 a[96:99], a[228:231], v[144:147], a[96:99]\n\t"
  "v_cvt_pk_bf16_f32 v168, v168, v169\n\t"
  "ds_read_b128 a[204:207], v218 offset:36864\n\t"
  "v_mfma_f32_16x16x32_bf16 a[100:103], a[232:235], v[112:115], a[100:103]\n\t"
  "v_exp_f32 v171, v171\n\t"
  "v_mfma_f32_16x16x32_bf16 a[104:107], a[232:235], v[120:123], a[104:107]\n\t"
  "s_waitcnt vmcnt(4)\n\t"
  "buffer_load_dwordx4 v214, s[28:31], s52 offen offset:2048 nt lds\n\t"
  "v_mfma_f32_16x16x32_bf16 a[108:111], a[232:235], v[128:131], a[108:111]\n\t"
  "v_mfma_f32_16x16x32_bf16 a[112:115], a[232:235], v[136:139], a[112:115]\n\t"
  "v_exp_f32 v176, v176\n\t"
  "v_mfma_f32_16x16x32_bf16 a[116:119], a[232:235], v[144:147], a[116:119]\n\t"
  "ds_read_b128 a[208:211], v219 offset:36864\n\t"
  "v_cvt_pk_bf16_f32 v169, v170, v171\n\t"
  "v_mfma_f32_16x16x32_bf16 a[120:123], a[236:239], v[112:115], a[120:123]\n\t"
  "v_exp_f32 v177, v177\n\t"
  "v_mfma_f32_16x16x32_bf16 a[124:127], a[236:239], v[120:123], a[124:127]\n\t"
  "v_exp_f32 v178, v178\n\t"
  "v_mfma_f32_16x16x32_bf16 a[128:131], a[236:239], v[128:131], a[128:131]\n\t"
  "v_cvt_pk_bf16_f32 v176, v176, v177\n\t"
  "v_mfma_f32_16x16x32_bf16 a[132:135], a[236:239], v[136:139], a[132:135]\n\t"
  "v_exp_f32 v179, v179\n\t"
  "v_mfma_f32_16x16x32_bf16 a[136:139], a[236:239], v[144:147], a[136:139]\n\t"
  "s_waitcnt vmcnt(4)\n\t"
  "buffer_load_dwordx4 v215, s[28:31], s52 offen offset:3072 nt lds\n\t"
  "v_mfma_f32_16x16x32_bf16 a[140:143], a[240:243], v[112:115], a[140:143]\n\t"
  "v_mfma_f32_16x16x32_bf16 a[144:147], a[240:243], v[120:123], a[144:147]\n\t"
  "v_exp_f32 v184, v184\n\t"
  "v_mfma_f32_16x16x32_bf16 a[148:151], a[240:243], v[128:131], a[148:151]\n\t"
  "v_cvt_pk_bf16_f32 v177, v178, v179\n\t"
  "v_mfma_f32_16x16x32_bf16 a[152:155], a[240:243], v[136:139], a[152:155]\n\t"
  "v_exp_f32 v185, v185\n\t"
  "v_mfma_f32_16x16x32_bf16 a[156:159], a[240:243], v[144:147], a[156:159]\n\t"
  "v_exp_f32 v186, v186\n\t"
  "v_mfma_f32_16x16x32_bf16 a[160:163], a[244:247], v[112:115], a[160:163]\n\t"
  "v_cvt_pk_bf16_f32 v184, v184, v185\n\t"
  "v_mfma_f32_16x16x32_bf16 a[164:167], a[244:247], v[120:123], a[164:167]\n\t"
  "v_exp_f32 v187, v187\n\t"
  "v_mfma_f32_16x16x32_bf16 a[168:171], a[244:247], v[128:131], a[168:171]\n\t"
  "v_mfma_f32_16x16x32_bf16 a[172:175], a[244:247], v[136:139], a[172:175]\n\t"
  "s_nop 0\n\t"
  "v_cvt_pk_bf16_f32 v185, v186, v187\n\t"
  "v_mfma_f32_16x16x32_bf16 a[176:179], a[244:247], v[144:147], a[176:179]\n\t"
  "s_waitcnt lgkmcnt(0)\n\t"
  "v_mfma_f32_16x16x32_bf16 v[112:115], a[180:183], v[32:35], v[192:195]\n\t"
  "v_exp_f32 v156, v156\n\t"
  "v_mfma_f32_16x16x32_bf16 v[120:123], a[180:183], v[48:51], v[196:199]\n\t"
  "ds_read_b64_tr_b16 a[212:213], v220 offset:32768\n\t"
  "v_mfma_f32_16x16x32_bf16 v[128:131], a[180:183], v[64:67], v[200:203]\n\t"
  "v_exp_f32 v157, v157\n\t"
  "v_mfma_f32_16x16x32_bf16 v[136:139], a[180:183], v[80:83], v[204:207]\n\t"
  "ds_read_b64_tr_b16 a[214:215], v220 offset:36864\n\t"
  "v_mfma_f32_16x16x32_bf16 v[144:147], a[180:183], v[96:99], v[208:211]\n\t"
  "v_exp_f32 v158, v158\n\t"
  "s_waitcnt vmcnt(4)\n\t"
  "s_add_u32 m0, s51, 32768\n\t"
  "s_mov_b32 s52, 81920\n\t"
  "buffer_load_dwordx4 v212, s[28:31], s52 offen offset:0 nt lds\n\t"
  "v_mfma_f32_16x16x32_bf16 v[112:115], a[184:187], v[36:39], v[112:115]\n\t"
  "v_mfma_f32_16x16x32_bf16 v[120:123], a[184:187], v[52:55], v[120:123]\n\t"
  "v_mfma_f32_16x16x32_bf16 v[128:131], a[184:187], v[68:71], v[128:131]\n\t"
  "v_mfma_f32_16x16x32_bf16 v[136:139], a[184:187], v[84:87], v[136:139]\n\t"
  "ds_read_b64_tr_b16 a[216:217], v221 offset:32768\n\t"
  "v_cvt_pk_bf16_f32 v154, v156, v157\n\t"
  "v_mfma_f32_16x16x32_bf16 v[144:147], a[184:187], v[100:103], v[144:147]\n\t"
  "v_exp_f32 v159, v159\n\t"
  "v_mfma_f32_16x16x32_bf16 v[112:115], a[188:191], v[40:43], v[112:115]\n\t"
  "ds_read_b64_tr_b16 a[218:219], v221 offset:36864\n\t"
  "v_mfma_f32_16x16x32_bf16 v[120:123], a[188:191], v[56:59], v[120:123]\n\t"
  "v_exp_f32 v164, v164\n\t"
  "v_mfma_f32_16x16x32_bf16 v[128:131], a[188:191], v[72:75], v[128:131]\n\t"
  "ds_read_b64_tr_b16 a[220:221], v220 offset:33280\n\t"
  "v_cvt_pk_bf16_f32 v155, v158, v159\n\t"
  "v_mfma_f32_16x16x32_bf16 v[136:139], a[188:191], v[88:91], v[136:139]\n\t"
  "v_exp_f32 v165, v165\n\t"
  "v_mfma_f32_16x16x32_bf16 v[144:147], a[188:191], v[104:107], v[144:147]\n\t"
  "ds_read_b64_tr_b16 a[222:223], v220 offset:37376\n\t"
  "v_mfma_f32_16x16x32_bf16 v[112:115], a[192:195], v[44:47], v[112:115]\n\t"
  "v_exp_f32 v166, v166\n\t"
  "v_mfma_f32_16x16x32_bf16 v[120:123], a[192:195], v[60:63], v[120:123]\n\t"
  "ds_read_b64_tr_b16 a[224:225], v221 offset:33280\n\t"
  "v_cvt_pk_bf16_f32 v162, v164, v165\n\t"
  "v_mfma_f32_16x16x32_bf16 v[128:131], a[192:195], v[76:79], v[128:131]\n\t"
  "v_exp_f32 v167, v167\n\t"
  "v_mfma_f32_16x16x32_bf16 v[136:139], a[192:195], v[92:95], v[136:139]\n\t"
  "ds_read_b64_tr_b16 a[226:227], v221 offset:37376\n\t"
  "v_mfma_f32_16x16x32_bf16 v[144:147], a[192:195], v[108:111], v[144:147]\n\t"
  "v_exp_f32 v172, v172\n\t"
  "v_mfma_f32_16x16x32_bf16 v[116:119], a[196:199], v[32:35], v[192:195]\n\t"
  "ds_read_b64_tr_b16 a[228:229], v220 offset:33792\n\t"
  "v_cvt_pk_bf16_f32 v163, v166, v167\n\t"
  "v_mfma_f32_16x16x32_bf16 v[124:127], a[196:199], v[48:51], v[196:199]\n\t"
  "v_exp_f32 v173, v173\n\t"
  "s_waitcnt vmcnt(4)\n\t"
  "buffer_load_dwordx4 v213, s[28:31], s52 offen offset:1024 nt lds\n\t"
  "v_mfma_f32_16x16x32_bf16 v[132:135], a[196:199], v[64:67], v[200:203]\n\t"
  "v_mfma_f32_16x16x32_bf16 v[140:143], a[196:199], v[80:83], v[204:207]\n\t"
  "v_mfma_f32_16x16x32_bf16 v[148:151], a[196:199], v[96:99], v[208:211]\n\t"
  "ds_read_b64_tr_b16 a[230:231], v220 offset:37888\n\t"
  "v_mfma_f32_16x16x32_bf16 v[116:119], a[200:203], v[36:39], v[116:119]\n\t"
  "v_exp_f32 v174, v174\n\t"
  "v_mfma_f32_16x16x32_bf16 v[124:127], a[200:203], v[52:55], v[124:127]\n\t"
  "ds_read_b64_tr_b16 a[232:233], v221 offset:33792\n\t"
  "v_cvt_pk_bf16_f32 v170, v172, v173\n\t"
  "v_mfma_f32_16x16x32_bf16 v[132:135], a[200:203], v[68:71], v[132:135]\n\t"
  "v_exp_f32 v175, v175\n\t"
  "v_mfma_f32_16x16x32_bf16 v[140:143], a[200:203], v[84:87], v[140:143]\n\t"
  "ds_read_b64_tr_b16 a[234:235], v221 offset:37888\n\t"
  "v_mfma_f32_16x16x32_bf16 v[148:151], a[200:203], v[100:103], v[148:151]\n\t"
  "v_exp_f32 v180, v180\n\t"
  "v_mfma_f32_16x16x32_bf16 v[116:119], a[204:207], v[40:43], v[116:119]\n\t"
  "ds_read_b64_tr_b16 a[236:237], v220 offset:34304\n\t"
  "v_cvt_pk_bf16_f32 v171, v174, v175\n\t"
  "v_mfma_f32_16x16x32_bf16 v[124:127], a[204:207], v[56:59], v[124:127]\n\t"
  "v_exp_f32 v181, v181\n\t"
  "v_mfma_f32_16x16x32_bf16 v[132:135], a[204:207], v[72:75], v[132:135]\n\t"
  "ds_read_b64_tr_b16 a[238:239], v220 offset:38400\n\t"
  "v_mfma_f32_16x16x32_bf16 v[140:143], a[204:207], v[88:91], v[140:143]\n\t"
  "v_exp_f32 v182, v182\n\t"
  "v_mfma_f32_16x16x32_bf16 v[148:151], a[204:207], v[104:107], v[148:151]\n\t"
  "ds_read_b64_tr_b16 a[240:241], v221 offset:34304\n\t"
  "s_waitcnt vmcnt(4)\n\t"
  "buffer_load_dwordx4 v214, s[28:31], s52 offen offset:2048 nt lds\n\t"
  "v_mfma_f32_16x16x32_bf16 v[116:119], a[208:211], v[44:47], v[116:119]\n\t"
  "v_mfma_f32_16x16x32_bf16 v[124:127], a[208:211], v[60:63], v[124:127]\n\t"
  "v_cvt_pk_bf16_f32 v178, v180, v181\n\t"
  "v_mfma_f32_16x16x32_bf16 v[132:135], a[208:211], v[76:79], v[132:135]\n\t"
  "v_exp_f32 v183, v183\n\t"
  "v_mfma_f32_16x16x32_bf16 v[140:143], a[208:211], v[92:95], v[140:143]\n\t"
  "ds_read_b64_tr_b16 a[242:243], v221 offset:38400\n\t"
  "v_exp_f32 v188, v188\n\t"
  "v_cvt_pk_bf16_f32 v179, v182, v183\n\t"
  "v_exp_f32 v189, v189\n\t"
  "v_exp_f32 v190, v190\n\t"
  "v_cvt_pk_bf16_f32 v186, v188, v189\n\t"
  "v_exp_f32 v191, v191\n\t"
  "s_nop 0\n\t"
  "v_cvt_pk_bf16_f32 v187, v190, v191\n\t"
  "v_mfma_f32_16x16x32_bf16 v[148:151], a[208:211], v[108:111], v[148:151]\n\t"
  "s_waitcnt vmcnt(11) lgkmcnt(0)\n\t"
  "s_barrier\n\t"
  "v_mfma_f32_16x16x32_bf16 a[0:3], a[212:215], v[152:155], a[0:3]\n\t"
  "v_exp_f32 v112, v112\n\t"
  "v_mfma_f32_16x16x32_bf16 a[4:7], a[212:215], v[160:163], a[4:7]\n\t"
  "v_add_u32 v216, 65536, v216\n\t"
  "v_add_u32 v217, 65536, v217\n\t"
  "v_add_u32 v218, 65536, v218\n\t"
  "v_add_u32 v219, 65536, v219\n\t"
  "v_mfma_f32_16x16x32_bf16 a[8:11], a[212:215], v[168:171], a[8:11]\n\t"
  "v_mfma_f32_16x16x32_bf16 a[12:15], a[212:215], v[176:179], a[12:15]\n\t"
  "s_waitcnt vmcnt(4)\n\t"
  "buffer_load_dwordx4 v215, s[28:31], s52 offen offset:3072 nt lds\n\t"
  "v_mfma_f32_16x16x32_bf16 a[16:19], a[212:215], v[184:187], a[16:19]\n\t"
  "v_mfma_f32_16x16x32_bf16 a[20:23], a[216:219], v[152:155], a[20:23]\n\t"
  "v_exp_f32 v113, v113\n\t"
  "v_mfma_f32_16x16x32_bf16 a[24:27], a[216:219], v[160:163], a[24:27]\n\t"
  "v_exp_f32 v114, v114\n\t"
  "v_mfma_f32_16x16x32_bf16 a[28:31], a[216:219], v[168:171], a[28:31]\n\t"
  "v_cvt_pk_bf16_f32 v112, v112, v113\n\t"
  "v_mfma_f32_16x16x32_bf16 a[32:35], a[216:219], v[176:179], a[32:35]\n\t"
  "v_exp_f32 v115, v115\n\t"
  "v_mfma_f32_16x16x32_bf16 a[36:39], a[216:219], v[184:187], a[36:39]\n\t"
  "v_exp_f32 v120, v120\n\t"
  "v_mfma_f32_16x16x32_bf16 a[40:43], a[220:223], v[152:155], a[40:43]\n\t"
  "v_cvt_pk_bf16_f32 v113, v114, v115\n\t"
  "ds_read_b128 a[180:183], v216 offset:0\n\t"
  "v_mfma_f32_16x16x32_bf16 a[44:47], a[220:223], v[160:163], a[44:47]\n\t"
  "v_exp_f32 v121, v121\n\t"
  "v_mfma_f32_16x16x32_bf16 a[48:51], a[220:223], v[168:171], a[48:51]\n\t"
  "s_waitcnt vmcnt(4)\n\t"
  "s_add_u32 m0, s51, 36864\n\t"
  "s_mov_b32 s52, 86016\n\t"
  "buffer_load_dwordx4 v212, s[28:31], s52 offen offset:0 nt lds\n\t"
  "v_mfma_f32_16x16x32_bf16 a[52:55], a[220:223], v[176:179], a[52:55]\n\t"
  "v_mfma_f32_16x16x32_bf16 a[56:59], a[220:223], v[184:187], a[56:59]\n\t"
  "v_mfma_f32_16x16x32_bf16 a[60:63], a[224:227], v[152:155], a[60:63]\n\t"
  "v_exp_f32 v122, v122\n\t"
  "v_mfma_f32_16x16x32_bf16 a[64:67], a[224:227], v[160:163], a[64:67]\n\t"
  "ds_read_b128 a[184:187], v217 offset:0\n\t"
  "v_cvt_pk_bf16_f32 v120, v120, v121\n\t"
  "v_mfma_f32_16x16x32_bf16 a[68:71], a[224:227], v[168:171], a[68:71]\n\t"
  "v_exp_f32 v123, v123\n\t"
  "v_mfma_f32_16x16x32_bf16 a[72:75], a[224:227], v[176:179], a[72:75]\n\t"
  "ds_read_b128 a[188:191], v218 offset:0\n\t"
  "v_mfma_f32_16x16x32_bf16 a[76:79], a[224:227], v[184:187], a[76:79]\n\t"
  "v_exp_f32 v128, v128\n\t"
  "v_mfma_f32_16x16x32_bf16 a[80:83], a[228:231], v[152:155], a[80:83]\n\t"
  "ds_read_b128 a[192:195], v219 offset:0\n\t"
  "v_cvt_pk_bf16_f32 v121, v122, v123\n\t"
  "v_mfma_f32_16x16x32_bf16 a[84:87], a[228:231], v[160:163], a[84:87]\n\t"
  "v_exp_f32 v129, v129\n\t"
  "v_mfma_f32_16x16x32_bf16 a[88:91], a[228:231], v[168:171], a[88:91]\n\t"
  "ds_read_b128 a[196:199], v216 offset:4096\n\t"
  "v_mfma_f32_16x16x32_bf16 a[92:95], a[228:231], v[176:179], a[92:95]\n\t"
  "v_exp_f32 v130, v130\n\t"
  "v_mfma_f32_16x16x32_bf16 a[96:99], a[228:231], v[184:187], a[96:99]\n\t"
  "v_cvt_pk_bf16_f32 v128, v128, v129\n\t"
  "ds_read_b128 a[200:203], v217 offset:4096\n\t"
  "v_mfma_f32_16x16x32_bf16 a[100:103], a[232:235], v[152:155], a[100:103]\n\t"
  "v_exp_f32 v131, v131\n\t"
  "v_mfma_f32_16x16x32_bf16 a[104:107], a[232:235], v[160:163], a[104:107]\n\t"
  "s_waitcnt vmcnt(4)\n\t"
  "buffer_load_dwordx4 v213, s[28:31], s52 offen offset:1024 nt lds\n\t"
  "v_mfma_f32_16x16x32_bf16 a[108:111], a[232:235], v[168:171], a[108:111]\n\t"
  "v_mfma_f32_16x16x32_bf16 a[112:115], a[232:235], v[176:179], a[112:115]\n\t"
  "ds_read_b128 a[204:207], v218 offset:4096\n\t"
  "v_mfma_f32_16x16x32_bf16 a[116:119], a[232:235], v[184:187], a[116:119]\n\t"
  "v_exp_f32 v136, v136\n\t"
  "v_mfma_f32_16x16x32_bf16 a[120:123], a[236:239], v[152:155], a[120:123]\n\t"
  "v_cvt_pk_bf16_f32 v129, v130, v131\n\t"
  "ds_read_b128 a[208:211], v219 offset:4096\n\t"
  "v_mfma_f32_16x16x32_bf16 a[124:127], a[236:239], v[160:163], a[124:127]\n\t"
  "v_exp_f32 v137, v137\n\t"
  "v_mfma_f32_16x16x32_bf16 a[128:131], a[236:239], v[168:171], a[128:131]\n\t"
  "v_exp_f32 v138, v138\n\t"
  "v_mfma_f32_16x16x32_bf16 a[132:135], a[236:239], v[176:179], a[132:135]\n\t"
  "v_cvt_pk_bf16_f32 v136, v136, v137\n\t"
  "v_mfma_f32_16x16x32_bf16 a[136:139], a[236:239], v[184:187], a[136:139]\n\t"
  "v_exp_f32 v139, v139\n\t"
  "v_mfma_f32_16x16x32_bf16 a[140:143], a[240:243], v[152:155], a[140:143]\n\t"
  "s_waitcnt vmcnt(4)\n\t"
  "buffer_load_dwordx4 v214, s[28:31], s52 offen offset:2048 nt lds\n\t"
  "v_mfma_f32_16x16x32_bf16 a[144:147], a[240:243], v[160:163], a[144:147]\n\t"
  "v_mfma_f32_16x16x32_bf16 a[148:151], a[240:243], v[168:171], a[148:151]\n\t"
  "v_exp_f32 v144, v144\n\t"
  "v_mfma_f32_16x16x32_bf16 a[152:155], a[240:243], v[176:179], a[152:155]\n\t"
  "v_cvt_pk_bf16_f32 v137, v138, v139\n\t"
  "v_mfma_f32_16x16x32_bf16 a[156:159], a[240:243], v[184:187], a[156:159]\n\t"
  "v_exp_f32 v145, v145\n\t"
  "v_mfma_f32_16x16x32_bf16 a[160:163], a[244:247], v[152:155], a[160:163]\n\t"
  "v_exp_f32 v146, v146\n\t"
  "v_mfma_f32_16x16x32_bf16 a[164:167], a[244:247], v[160:163], a[164:167]\n\t"
  "v_cvt_pk_bf16_f32 v144, v144, v145\n\t"
  "v_mfma_f32_16x16x32_bf16 a[168:171], a[244:247], v[168:171], a[168:171]\n\t"
  "v_exp_f32 v147, v147\n\t"
  "v_mfma_f32_16x16x32_bf16 a[172:175], a[244:247], v[176:179], a[172:175]\n\t"
  "s_nop 0\n\t"
  "v_cvt_pk_bf16_f32 v145, v146, v147\n\t"
  "v_mfma_f32_16x16x32_bf16 a[176:179], a[244:247], v[184:187], a[176:179]\n\t"
  "s_waitcnt lgkmcnt(0)\n\t"
  "v_mfma_f32_16x16x32_bf16 v[152:155], a[180:183], v[32:35], v[192:195]\n\t"
  "v_exp_f32 v116, v116\n\t"
  "v_mfma_f32_16x16x32_bf16 v[160:163], a[180:183], v[48:51], v[196:199]\n\t"
  "v_add_u32 v220, 65536, v220\n\t"
  "v_add_u32 v221, 65536, v221\n\t"
  "v_mfma_f32_16x16x32_bf16 v[168:171], a[180:183], v[64:67], v[200:203]\n\t"
  "v_exp_f32 v117, v117\n\t"
  "v_mfma_f32_16x16x32_bf16 v[176:179], a[180:183], v[80:83], v[204:207]\n\t"
  "v_exp_f32 v118, v118\n\t"
  "s_waitcnt vmcnt(4)\n\t"
  "buffer_load_dwordx4 v215, s[28:31], s52 offen offset:3072 nt lds\n\t"
  "v_mfma_f32_16x16x32_bf16 v[184:187], a[180:183], v[96:99], v[208:211]\n\t"
  "v_mfma_f32_16x16x32_bf16 v[152:155], a[184:187], v[36:39], v[152:155]\n\t"
  "v_mfma_f32_16x16x32_bf16 v[160:163], a[184:187], v[52:55], v[160:163]\n\t"
  "ds_read_b64_tr_b16 a[212:213], v220 offset:0\n\t"
  "v_cvt_pk_bf16_f32 v114, v116, v117\n\t"
  "v_mfma_f32_16x16x32_bf16 v[168:171], a[184:187], v[68:71], v[168:171]\n\t"
  "ds_read_b64_tr_b16 a[214:215], v220 offset:4096\n\t"
  "v_mfma_f32_16x16x32_bf16 v[176:179], a[184:187], v[84:87], v[176:179]\n\t"
  "v_exp_f32 v119, v119\n\t"
  "v_mfma_f32_16x16x32_bf16 v[184:187], a[184:187], v[100:103], v[184:187]\n\t"
  "ds_read_b64_tr_b16 a[216:217], v221 offset:0\n\t"
  "v_mfma_f32_16x16x32_bf16 v[152:155], a[188:191], v[40:43], v[152:155]\n\t"
  "v_exp_f32 v124, v124\n\t"
  "v_mfma_f32_16x16x32_bf16 v[160:163], a[188:191], v[56:59], v[160:163]\n\t"
  "v_cvt_pk_bf16_f32 v115, v118, v119\n\t"
  "ds_read_b64_tr_b16 a[218:219], v221 offset:4096\n\t"
  "v_mfma_f32_16x16x32_bf16 v[168:171], a[188:191], v[72:75], v[168:171]\n\t"
  "v_exp_f32 v125, v125\n\t"
  "v_mfma_f32_16x16x32_bf16 v[176:179], a[188:191], v[88:91], v[176:179]\n\t"
  "ds_read_b64_tr_b16 a[220:221], v220 offset:512\n\t"
  "v_mfma_f32_16x16x32_bf16 v[184:187], a[188:191], v[104:107], v[184:187]\n\t"
  "v_exp_f32 v126, v126\n\t"
  "v_mfma_f32_16x16x32_bf16 v[152:155], a[192:195], v[44:47], v[152:155]\n\t"
  "ds_read_b64_tr_b16 a[222:223], v220 offset:4608\n\t"
  "v_cvt_pk_bf16_f32 v122, v124, v125\n\t"
  "s_waitcnt vmcnt(4)\n\t"
  "s_add_u32 m0, s51, 65536\n\t"
  "s_mov_b32 s52, 90112\n\t"
  "buffer_load_dwordx4 v212, s[28:31], s52 offen offset:0 nt lds\n\t"
  "v_mfma_f32_16x16x32_bf16 v[160:163], a[192:195], v[60:63], v[160:163]\n\t"
  "v_mfma_f32_16x16x32_bf16 v[168:171], a[192:195], v[76:79], v[168:171]\n\t"
  "v_mfma_f32_16x16x32_bf16 v[176:179], a[192:195], v[92:95], v[176:179]\n\t"
  "v_mfma_f32_16x16x32_bf16 v[184:187], a[192:195], v[108:111], v[184:187]\n\t"
  "v_exp_f32 v127, v127\n\t"
  "v_mfma_f32_16x16x32_bf16 v[156:159], a[196:199], v[32:35], v[192:195]\n\t"
  "ds_read_b64_tr_b16 a[224:225], v221 offset:512\n\t"
  "v_mfma_f32_16x16x32_bf16 v[164:167], a[196:199], v[48:51], v[196:199]\n\t"
  "v_exp_f32 v132, v132\n\t"
  "v_mfma_f32_16x16x32_bf16 v[172:175], a[196:199], v[64:67], v[200:203]\n\t"
  "ds_read_b64_tr_b16 a[226:227], v221 offset:4608\n\t"
  "v_cvt_pk_bf16_f32 v123, v126, v127\n\t"
  "v_mfma_f32_16x16x32_bf16 v[180:183], a[196:199], v[80:83], v[204:207]\n\t"
  "v_exp_f32 v133, v133\n\t"
  "v_mfma_f32_16x16x32_bf16 v[188:191], a[196:199], v[96:99], v[208:211]\n\t"
  "ds_read_b64_tr_b16 a[228:229], v220 offset:1024\n\t"
  "v_mfma_f32_16x16x32_bf16 v[156:159], a[200:203], v[36:39], v[156:159]\n\t"
  "v_exp_f32 v134, v134\n\t"
  "v_mfma_f32_16x16x32_bf16 v[164:167], a[200:203], v[52:55], v[164:167]\n\t"
  "ds_read_b64_tr_b16 a[230:231], v220 offset:5120\n\t"
  "v_cvt_pk_bf16_f32 v130, v132, v133\n\t"
  "v_mfma_f32_16x16x32_bf16 v[172:175], a[200:203], v[68:71], v[172:175]\n\t"
  "ds_read_b64_tr_b16 a[232:233], v221 offset:1024\n\t"
  "v_mfma_f32_16x16x32_bf16 v[180:183], a[200:203], v[84:87], v[180:183]\n\t"
  "v_exp_f32 v135, v135\n\t"
  "v_mfma_f32_16x16x32_bf16 v[188:191], a[200:203], v[100:103], v[188:191]\n\t"
  "v_exp_f32 v140, v140\n\t"
  "v_mfma_f32_16x16x32_bf16 v[156:159], a[204:207], v[40:43], v[156:159]\n\t"
  "ds_read_b64_tr_b16 a[234:235], v221 offset:5120\n\t"
  "v_cvt_pk_bf16_f32 v131, v134, v135\n\t"
  "v_mfma_f32_16x16x32_bf16 v[164:167], a[204:207], v[56:59], v[164:167]\n\t"
  "ds_read_b64_tr_b16 a[236:237], v220 offset:1536\n\t"
  "v_mfma_f32_16x16x32_bf16 v[172:175], a[204:207], v[72:75], v[172:175]\n\t"
  "v_exp_f32 v141, v141\n\t"
  "v_mfma_f32_16x16x32_bf16 v[180:183], a[204:207], v[88:91], v[180:183]\n\t"
  "ds_read_b64_tr_b16 a[238:239], v220 offset:5632\n\t"
  "v_mfma_f32_16x16x32_bf16 v[188:191], a[204:207], v[104:107], v[188:191]\n\t"
  "v_exp_f32 v142, v142\n\t"
  "s_waitcnt vmcnt(4)\n\t"
  "buffer_load_dwordx4 v213, s[28:31], s52 offen offset:1024 nt lds\n\t"
  "v_mfma_f32_16x16x32_bf16 v[156:159], a[208:211], v[44:47], v[156:159]\n\t"
  "v_mfma_f32_16x16x32_bf16 v[164:167], a[208:211], v[60:63], v[164:167]\n\t"
  "v_mfma_f32_16x16x32_bf16 v[172:175], a[208:211], v[76:79], v[172:175]\n\t"
  "ds_read_b64_tr_b16 a[240:241], v221 offset:1536\n\t"
  "v_cvt_pk_bf16_f32 v138, v140, v141\n\t"
  "v_mfma_f32_16x16x32_bf16 v[180:183], a[208:211], v[92:95], v[180:183]\n\t"
  "v_exp_f32 v143, v143\n\t"
  "ds_read_b64_tr_b16 a[242:243], v221 offset:5632\n\t"
  "v_exp_f32 v148, v148\n\t"
  "v_cvt_pk_bf16_f32 v139, v142, v143\n\t"
  "v_exp_f32 v149, v149\n\t"
  "v_exp_f32 v150, v150\n\t"
  "v_cvt_pk_bf16_f32 v146, v148, v149\n\t"
  "v_exp_f32 v151, v151\n\t"
  "s_nop 0\n\t"
  "v_cvt_pk_bf16_f32 v147, v150, v151\n\t"
  "v_mfma_f32_16x16x32_bf16 v[188:191], a[208:211], v[108:111], v[188:191]\n\t"
  "s_waitcnt vmcnt(10) lgkmcnt(0)\n\t"
  "s_barrier\n\t"
  "v_mfma_f32_16x16x32_bf16 a[0:3], a[212:215], v[112:115], a[0:3]\n\t"
  "v_exp_f32 v152, v152\n\t"
  "v_mfma_f32_16x16x32_bf16 a[4:7], a[212:215], v[120:123], a[4:7]\n\t"
  "v_add_u32 v216, 0xfffe0000, v216\n\t"
  "v_add_u32 v217, 0xfffe0000, v217\n\t"
  "v_add_u32 v218, 0xfffe0000, v218\n\t"
  "v_add_u32 v219, 0xfffe0000, v219\n\t"
  "v_mfma_f32_16x16x32_bf16 a[8:11], a[212:215], v[128:131], a[8:11]\n\t"
  "v_mfma_f32_16x16x32_bf16 a[12:15], a[212:215], v[136:139], a[12:15]\n\t"
  "s_waitcnt vmcnt(4)\n\t"
  "buffer_load_dwordx4 v214, s[28:31], s52 offen offset:2048 nt lds\n\t"
  "v_mfma_f32_16x16x32_bf16 a[16:19], a[212:215], v[144:147], a[16:19]\n\t"
  "v_mfma_f32_16x16x32_bf16 a[20:23], a[216:219], v[112:115], a[20:23]\n\t"
  "v_exp_f32 v153, v153\n\t"
  "v_mfma_f32_16x16x32_bf16 a[24:27], a[216:219], v[120:123], a[24:27]\n\t"
  "v_exp_f32 v154, v154\n\t"
  "v_mfma_f32_16x16x32_bf16 a[28:31], a[216:219], v[128:131], a[28:31]\n\t"
  "v_cvt_pk_bf16_f32 v152, v152, v153\n\t"
  "v_mfma_f32_16x16x32_bf16 a[32:35], a[216:219], v[136:139], a[32:35]\n\t"
  "v_exp_f32 v155, v155\n\t"
  "v_mfma_f32_16x16x32_bf16 a[36:39], a[216:219], v[144:147], a[36:39]\n\t"
  "v_exp_f32 v160, v160\n\t"
  "v_mfma_f32_16x16x32_bf16 a[40:43], a[220:223], v[112:115], a[40:43]\n\t"
  "v_cvt_pk_bf16_f32 v153, v154, v155\n\t"
  "ds_read_b128 a[180:183], v216 offset:0\n\t"
  "v_mfma_f32_16x16x32_bf16 a[44:47], a[220:223], v[120:123], a[44:47]\n\t"
  "v_exp_f32 v161, v161\n\t"
  "v_mfma_f32_16x16x32_bf16 a[48:51], a[220:223], v[128:131], a[48:51]\n\t"
  "s_waitcnt vmcnt(4)\n\t"
  "buffer_load_dwordx4 v215, s[28:31], s52 offen offset:3072 nt lds\n\t"
  "v_mfma_f32_16x16x32_bf16 a[52:55], a[220:223], v[136:139], a[52:55]\n\t"
  "v_mfma_f32_16x16x32_bf16 a[56:59], a[220:223], v[144:147], a[56:59]\n\t"
  "v_exp_f32 v162, v162\n\t"
  "v_mfma_f32_16x16x32_bf16 a[60:63], a[224:227], v[112:115], a[60:63]\n\t"
  "ds_read_b128 a[184:187], v217 offset:0\n\t"
  "v_cvt_pk_bf16_f32 v160, v160, v161\n\t"
  "v_mfma_f32_16x16x32_bf16 a[64:67], a[224:227], v[120:123], a[64:67]\n\t"
  "v_exp_f32 v163, v163\n\t"
  "v_mfma_f32_16x16x32_bf16 a[68:71], a[224:227], v[128:131], a[68:71]\n\t"
  "ds_read_b128 a[188:191], v218 offset:0\n\t"
  "v_mfma_f32_16x16x32_bf16 a[72:75], a[224:227], v[136:139], a[72:75]\n\t"
  "v_exp_f32 v168, v168\n\t"
  "v_mfma_f32_16x16x32_bf16 a[76:79], a[224:227], v[144:147], a[76:79]\n\t"
  "ds_read_b128 a[192:195], v219 offset:0\n\t"
  "v_cvt_pk_bf16_f32 v161, v162, v163\n\t"
  "v_mfma_f32_16x16x32_bf16 a[80:83], a[228:231], v[112:115], a[80:83]\n\t"
  "v_exp_f32 v169, v169\n\t"
  "v_mfma_f32_16x16x32_bf16 a[84:87], a[228:231], v[120:123], a[84:87]\n\t"
  "s_waitcnt vmcnt(4)\n\t"
  "s_add_u32 m0, s51, 69632\n\t"
  "s_mov_b32 s52, 94208\n\t"
  "buffer_load_dwordx4 v212, s[28:31], s52 offen offset:0 nt lds\n\t"
  "v_mfma_f32_16x16x32_bf16 a[88:91], a[228:231], v[128:131], a[88:91]\n\t"
  "v_mfma_f32_16x16x32_bf16 a[92:95], a[228:231], v[136:139], a[92:95]\n\t"
  "v_mfma_f32_16x16x32_bf16 a[96:99], a[228:231], v[144:147], a[96:99]\n\t"
  "ds_read_b128 a[196:199], v216 offset:4096\n\t"
  "v_mfma_f32_16x16x32_bf16 a[100:103], a[232:235], v[112:115], a[100:103]\n\t"
  "v_exp_f32 v170, v170\n\t"
  "v_mfma_f32_16x16x32_bf16 a[104:107], a[232:235], v[120:123], a[104:107]\n\t"
  "v_cvt_pk_bf16_f32 v168, v168, v169\n\t"
  "ds_read_b128 a[200:203], v217 offset:4096\n\t"
  "v_mfma_f32_16x16x32_bf16 a[108:111], a[232:235], v[128:131], a[108:111]\n\t"
  "v_exp_f32 v171, v171\n\t"
  "v_mfma_f32_16x16x32_bf16 a[112:115], a[232:235], v[136:139], a[112:115]\n\t"
  "ds_read_b128 a[204:207], v218 offset:4096\n\t"
  "v_mfma_f32_16x16x32_bf16 a[116:119], a[232:235], v[144:147], a[116:119]\n\t"
  "v_exp_f32 v176, v176\n\t"
  "v_mfma_f32_16x16x32_bf16 a[120:123], a[236:239], v[112:115], a[120:123]\n\t"
  "v_cvt_pk_bf16_f32 v169, v170, v171\n\t"
  "ds_read_b128 a[208:211], v219 offset:4096\n\t"
  "v_mfma_f32_16x16x32_bf16 a[124:127], a[236:239], v[120:123], a[124:127]\n\t"
  "v_exp_f32 v177, v177\n\t"
  "v_mfma_f32_16x16x32_bf16 a[128:131], a[236:239], v[128:131], a[128:131]\n\t"
  "v_exp_f32 v178, v178\n\t"
  "v_mfma_f32_16x16x32_bf16 a[132:135], a[236:239], v[136:139], a[132:135]\n\t"
  "v_cvt_pk_bf16_f32 v176, v176, v177\n\t"
  "v_mfma_f32_16x16x32_bf16 a[136:139], a[236:239], v[144:147], a[136:139]\n\t"
  "v_exp_f32 v179, v179\n\t"
  "v_mfma_f32_16x16x32_bf16 a[140:143], a[240:243], v[112:115], a[140:143]\n\t"
  "s_waitcnt vmcnt(4)\n\t"
  "buffer_load_dwordx4 v213, s[28:31], s52 offen offset:1024 nt lds\n\t"
  "v_mfma_f32_16x16x32_bf16 a[144:147], a[240:243], v[120:123], a[144:147]\n\t"
  "v_mfma_f32_16x16x32_bf16 a[148:151], a[240:243], v[128:131], a[148:151]\n\t"
  "v_exp_f32 v184, v184\n\t"
  "v_mfma_f32_16x16x32_bf16 a[152:155], a[240:243], v[136:139], a[152:155]\n\t"
  "v_cvt_pk_bf16_f32 v177, v178, v179\n\t"
  "v_mfma_f32_16x16x32_bf16 a[156:159], a[240:243], v[144:147], a[156:159]\n\t"
  "v_exp_f32 v185, v185\n\t"
  "v_mfma_f32_16x16x32_bf16 a[160:163], a[244:247], v[112:115], a[160:163]\n\t"
  "v_exp_f32 v186, v186\n\t"
  "v_mfma_f32_16x16x32_bf16 a[164:167], a[244:247], v[120:123], a[164:167]\n\t"
  "v_cvt_pk_bf16_f32 v184, v184, v185\n\t"
  "v_mfma_f32_16x16x32_bf16 a[168:171], a[244:247], v[128:131], a[168:171]\n\t"
  "v_exp_f32 v187, v187\n\t"
  "v_mfma_f32_16x16x32_bf16 a[172:175], a[244:247], v[136:139], a[172:175]\n\t"
  "s_nop 0\n\t"
  "v_cvt_pk_bf16_f32 v185, v186, v187\n\t"
  "v_mfma_f32_16x16x32_bf16 a[176:179], a[244:247], v[144:147], a[176:179]\n\t"
  "s_waitcnt lgkmcnt(0)\n\t"
  "v_mfma_f32_16x16x32_bf16 v[112:115], a[180:183], v[32:35], v[192:195]\n\t"
  "v_exp_f32 v156, v156\n\t"
  "v_mfma_f32_16x16x32_bf16 v[120:123], a[180:183], v[48:51], v[196:199]\n\t"
  "v_add_u32 v220, 0xfffe0000, v220\n\t"
  "v_add_u32 v221, 0xfffe0000, v221\n\t"
  "v_mfma_f32_16x16x32_bf16 v[128:131], a[180:183], v[64:67], v[200:203]\n\t"
  "v_exp_f32 v157, v157\n\t"
  "v_mfma_f32_16x16x32_bf16 v[136:139], a[180:183], v[80:83], v[204:207]\n\t"
  "v_exp_f32 v158, v158\n\t"
  "s_waitcnt vmcnt(4)\n\t"
  "buffer_load_dwordx4 v214, s[28:31], s52 offen offset:2048 nt lds\n\t"
  "v_mfma_f32_16x16x32_bf16 v[144:147], a[180:183], v[96:99], v[208:211]\n\t"
  "v_mfma_f32_16x16x32_bf16 v[112:115], a[184:187], v[36:39], v[112:115]\n\t"
  "v_mfma_f32_16x16x32_bf16 v[120:123], a[184:187], v[52:55], v[120:123]\n\t"
  "ds_read_b64_tr_b16 a[212:213], v220 offset:0\n\t"
  "v_cvt_pk_bf16_f32 v154, v156, v157\n\t"
  "v_mfma_f32_16x16x32_bf16 v[128:131], a[184:187], v[68:71], v[128:131]\n\t"
  "ds_read_b64_tr_b16 a[214:215], v220 offset:4096\n\t"
  "v_mfma_f32_16x16x32_bf16 v[136:139], a[184:187], v[84:87], v[136:139]\n\t"
  "v_exp_f32 v159, v159\n\t"
  "v_mfma_f32_16x16x32_bf16 v[144:147], a[184:187], v[100:103], v[144:147]\n\t"
  "ds_read_b64_tr_b16 a[216:217], v221 offset:0\n\t"
  "v_mfma_f32_16x16x32_bf16 v[112:115], a[188:191], v[40:43], v[112:115]\n\t"
  "v_exp_f32 v164, v164\n\t"
  "v_mfma_f32_16x16x32_bf16 v[120:123], a[188:191], v[56:59], v[120:123]\n\t"
  "v_cvt_pk_bf16_f32 v155, v158, v159\n\t"
  "ds_read_b64_tr_b16 a[218:219], v221 offset:4096\n\t"
  "v_mfma_f32_16x16x32_bf16 v[128:131], a[188:191], v[72:75], v[128:131]\n\t"
  "v_exp_f32 v165, v165\n\t"
  "v_mfma_f32_16x16x32_bf16 v[136:139], a[188:191], v[88:91], v[136:139]\n\t"
  "ds_read_b64_tr_b16 a[220:221], v220 offset:512\n\t"
  "v_mfma_f32_16x16x32_bf16 v[144:147], a[188:191], v[104:107], v[144:147]\n\t"
  "v_exp_f32 v166, v166\n\t"
  "v_mfma_f32_16x16x32_bf16 v[112:115], a[192:195], v[44:47], v[112:115]\n\t"
  "ds_read_b64_tr_b16 a[222:223], v220 offset:4608\n\t"
  "v_cvt_pk_bf16_f32 v162, v164, v165\n\t"
  "s_waitcnt vmcnt(4)\n\t"
  "buffer_load_dwordx4 v215, s[28:31], s52 offen offset:3072 nt lds\n\t"
  "v_mfma_f32_16x16x32_bf16 v[120:123], a[192:195], v[60:63], v[120:123]\n\t"
  "v_mfma_f32_16x16x32_bf16 v[128:131], a[192:195], v[76:79], v[128:131]\n\t"
  "v_mfma_f32_16x16x32_bf16 v[136:139], a[192:195], v[92:95], v[136:139]\n\t"
  "v_exp_f32 v167, v167\n\t"
  "v_mfma_f32_16x16x32_bf16 v[144:147], a[192:195], v[108:111], v[144:147]\n\t"
  "ds_read_b64_tr_b16 a[224:225], v221 offset:512\n\t"
  "v_mfma_f32_16x16x32_bf16 v[116:119], a[196:199], v[32:35], v[192:195]\n\t"
  "v_exp_f32 v172, v172\n\t"
  "v_mfma_f32_16x16x32_bf16 v[124:127], a[196:199], v[48:51], v[196:199]\n\t"
  "ds_read_b64_tr_b16 a[226:227], v221 offset:4608\n\t"
  "v_cvt_pk_bf16_f32 v163, v166, v167\n\t"
  "v_mfma_f32_16x16x32_bf16 v[132:135], a[196:199], v[64:67], v[200:203]\n\t"
  "v_exp_f32 v173, v173\n\t"
  "v_mfma_f32_16x16x32_bf16 v[140:143], a[196:199], v[80:83], v[204:207]\n\t"
  "ds_read_b64_tr_b16 a[228:229], v220 offset:1024\n\t"
  "v_mfma_f32_16x16x32_bf16 v[148:151], a[196:199], v[96:99], v[208:211]\n\t"
  "v_exp_f32 v174, v174\n\t"
  "v_mfma_f32_16x16x32_bf16 v[116:119], a[200:203], v[36:39], v[116:119]\n\t"
  "ds_read_b64_tr_b16 a[230:231], v220 offset:5120\n\t"
  "v_cvt_pk_bf16_f32 v170, v172, v173\n\t"
  "v_mfma_f32_16x16x32_bf16 v[124:127], a[200:203], v[52:55], v[124:127]\n\t"
  "ds_read_b64_tr_b16 a[232:233], v221 offset:1024\n\t"
  "v_mfma_f32_16x16x32_bf16 v[132:135], a[200:203], v[68:71], v[132:135]\n\t"
  "v_exp_f32 v175, v175\n\t"
  "v_mfma_f32_16x16x32_bf16 v[140:143], a[200:203], v[84:87], v[140:143]\n\t"
  "v_exp_f32 v180, v180\n\t"
  "v_mfma_f32_16x16x32_bf16 v[148:151], a[200:203], v[100:103], v[148:151]\n\t"
  "ds_read_b64_tr_b16 a[234:235], v221 offset:5120\n\t"
  "s_waitcnt vmcnt(4)\n\t"
  "s_add_u32 m0, s51, 98304\n\t"
  "s_mov_b32 s52, 98304\n\t"
  "buffer_load_dwordx4 v212, s[28:31], s52 offen offset:0 nt lds\n\t"
  "v_mfma_f32_16x16x32_bf16 v[116:119], a[204:207], v[40:43], v[116:119]\n\t"
  "v_mfma_f32_16x16x32_bf16 v[124:127], a[204:207], v[56:59], v[124:127]\n\t"
  "v_mfma_f32_16x16x32_bf16 v[132:135], a[204:207], v[72:75], v[132:135]\n\t"
  "v_cvt_pk_bf16_f32 v171, v174, v175\n\t"
  "v_mfma_f32_16x16x32_bf16 v[140:143], a[204:207], v[88:91], v[140:143]\n\t"
  "ds_read_b64_tr_b16 a[236:237], v220 offset:1536\n\t"
  "v_mfma_f32_16x16x32_bf16 v[148:151], a[204:207], v[104:107], v[148:151]\n\t"
  "v_exp_f32 v181, v181\n\t"
  "v_mfma_f32_16x16x32_bf16 v[116:119], a[208:211], v[44:47], v[116:119]\n\t"
  "ds_read_b64_tr_b16 a[238:239], v220 offset:5632\n\t"
  "v_mfma_f32_16x16x32_bf16 v[124:127], a[208:211], v[60:63], v[124:127]\n\t"
  "v_exp_f32 v182, v182\n\t"
  "v_mfma_f32_16x16x32_bf16 v[132:135], a[208:211], v[76:79], v[132:135]\n\t"
  "ds_read_b64_tr_b16 a[240:241], v221 offset:1536\n\t"
  "v_cvt_pk_bf16_f32 v178, v180, v181\n\t"
  "v_mfma_f32_16x16x32_bf16 v[140:143], a[208:211], v[92:95], v[140:143]\n\t"
  "v_exp_f32 v183, v183\n\t"
  "ds_read_b64_tr_b16 a[242:243], v221 offset:5632\n\t"
  "v_exp_f32 v188, v188\n\t"
  "v_cvt_pk_bf16_f32 v179, v182, v183\n\t"
  "v_exp_f32 v189, v189\n\t"
  "v_exp_f32 v190, v190\n\t"
  "v_cvt_pk_bf16_f32 v186, v188, v189\n\t"
  "v_exp_f32 v191, v191\n\t"
  "s_nop 0\n\t"
  "v_cvt_pk_bf16_f32 v187, v190, v191\n\t"
  "v_mfma_f32_16x16x32_bf16 v[148:151], a[208:211], v[108:111], v[148:151]\n\t"
  "s_waitcnt vmcnt(9) lgkmcnt(0)\n\t"
  "s_barrier\n\t"
  "v_mfma_f32_16x16x32_bf16 a[0:3], a[212:215], v[152:155], a[0:3]\n\t"
  "v_exp_f32 v112, v112\n\t"
  "v_mfma_f32_16x16x32_bf16 a[4:7], a[212:215], v[160:163], a[4:7]\n\t"
  "ds_read_b128 a[180:183], v216 offset:32768\n\t"
  "s_waitcnt vmcnt(4)\n\t"
  "buffer_load_dwordx4 v213, s[28:31], s52 offen offset:1024 nt lds\n\t"
  "v_mfma_f32_16x16x32_bf16 a[8:11], a[212:215], v[168:171], a[8:11]\n\t"
  "v_mfma_f32_16x16x32_bf16 a[12:15], a[212:215], v[176:179], a[12:15]\n\t"
  "v_mfma_f32_16x16x32_bf16 a[16:19], a[212:215], v[184:187], a[16:19]\n\t"
  "v_exp_f32 v113, v113\n\t"
  "v_mfma_f32_16x16x32_bf16 a[20:23], a[216:219], v[152:155], a[20:23]\n\t"
  "v_exp_f32 v114, v114\n\t"
  "v_mfma_f32_16x16x32_bf16 a[24:27], a[216:219], v[160:163], a[24:27]\n\t"
  "ds_read_b128 a[184:187], v217 offset:32768\n\t"
  "v_cvt_pk_bf16_f32 v112, v112, v113\n\t"
  "v_mfma_f32_16x16x32_bf16 a[28:31], a[216:219], v[168:171], a[28:31]\n\t"
  "v_exp_f32 v115, v115\n\t"
  "v_mfma_f32_16x16x32_bf16 a[32:35], a[216:219], v[176:179], a[32:35]\n\t"
  "ds_read_b128 a[188:191], v218 offset:32768\n\t"
  "v_mfma_f32_16x16x32_bf16 a[36:39], a[216:219], v[184:187], a[36:39]\n\t"
  "v_exp_f32 v120, v120\n\t"
  "v_mfma_f32_16x16x32_bf16 a[40:43], a[220:223], v[152:155], a[40:43]\n\t"
  "v_cvt_pk_bf16_f32 v113, v114, v115\n\t"
  "v_mfma_f32_16x16x32_bf16 a[44:47], a[220:223], v[160:163], a[44:47]\n\t"
  "v_exp_f32 v121, v121\n\t"
  "v_mfma_f32_16x16x32_bf16 a[48:51], a[220:223], v[168:171], a[48:51]\n\t"
  "s_waitcnt vmcnt(4)\n\t"
  "buffer_load_dwordx4 v214, s[28:31], s52 offen offset:2048 nt lds\n\t"
  "v_mfma_f32_16x16x32_bf16 a[52:55], a[220:223], v[176:179], a[52:55]\n\t"
  "v_mfma_f32_16x16x32_bf16 a[56:59], a[220:223], v[184:187], a[56:59]\n\t"
  "ds_read_b128 a[192:195], v219 offset:32768\n\t"
  "v_mfma_f32_16x16x32_bf16 a[60:63], a[224:227], v[152:155], a[60:63]\n\t"
  "v_exp_f32 v122, v122\n\t"
  "v_mfma_f32_16x16x32_bf16 a[64:67], a[224:227], v[160:163], a[64:67]\n\t"
  "v_cvt_pk_bf16_f32 v120, v120, v121\n\t"
  "v_mfma_f32_16x16x32_bf16 a[68:71], a[224:227], v[168:171], a[68:71]\n\t"
  "v_exp_f32 v123, v123\n\t"
  "v_mfma_f32_16x16x32_bf16 a[72:75], a[224:227], v[176:179], a[72:75]\n\t"
  "ds_read_b128 a[196:199], v216 offset:36864\n\t"
  "v_mfma_f32_16x16x32_bf16 a[76:79], a[224:227], v[184:187], a[76:79]\n\t"
  "v_exp_f32 v128, v128\n\t"
  "v_mfma_f32_16x16x32_bf16 a[80:83], a[228:231], v[152:155], a[80:83]\n\t"
  "v_cvt_pk_bf16_f32 v121, v122, v123\n\t"
  "ds_read_b128 a[200:203], v217 offset:36864\n\t"
  "v_mfma_f32_16x16x32_bf16 a[84:87], a[228:231], v[160:163], a[84:87]\n\t"
  "v_exp_f32 v129, v129\n\t"
  "v_mfma_f32_16x16x32_bf16 a[88:91], a[228:231], v[168:171], a[88:91]\n\t"
  "s_waitcnt vmcnt(4)\n\t"
  "buffer_load_dwordx4 v215, s[28:31], s52 offen offset:3072 nt lds\n\t"
  "v_mfma_f32_16x16x32_bf16 a[92:95], a[228:231], v[176:179], a[92:95]\n\t"
  "v_mfma_f32_16x16x32_bf16 a[96:99], a[228:231], v[184:187], a[96:99]\n\t"
  "v_exp_f32 v130, v130\n\t"
  "v_mfma_f32_16x16x32_bf16 a[100:103], a[232:235], v[152:155], a[100:103]\n\t"
  "v_cvt_pk_bf16_f32 v128, v128, v129\n\t"
  "ds_read_b128 a[204:207], v218 offset:36864\n\t"
  "v_mfma_f32_16x16x32_bf16 a[104:107], a[232:235], v[160:163], a[104:107]\n\t"
  "v_exp_f32 v131, v131\n\t"
  "v_mfma_f32_16x16x32_bf16 a[108:111], a[232:235], v[168:171], a[108:111]\n\t"
  "v_exp_f32 v136, v136\n\t"
  "v_mfma_f32_16x16x32_bf16 a[112:115], a[232:235], v[176:179], a[112:115]\n\t"
  "ds_read_b128 a[208:211], v219 offset:36864\n\t"
  "v_cvt_pk_bf16_f32 v129, v130, v131\n\t"
  "v_mfma_f32_16x16x32_bf16 a[116:119], a[232:235], v[184:187], a[116:119]\n\t"
  "v_exp_f32 v137, v137\n\t"
  "v_mfma_f32_16x16x32_bf16 a[120:123], a[236:239], v[152:155], a[120:123]\n\t"
  "v_exp_f32 v138, v138\n\t"
  "v_mfma_f32_16x16x32_bf16 a[124:127], a[236:239], v[160:163], a[124:127]\n\t"
  "s_waitcnt vmcnt(4)\n\t"
  "s_add_u32 m0, s51, 102400\n\t"
  "s_mov_b32 s52, 102400\n\t"
  "buffer_load_dwordx4 v212, s[28:31], s52 offen offset:0 nt lds\n\t"
  "v_mfma_f32_16x16x32_bf16 a[128:131], a[236:239], v[168:171], a[128:131]\n\t"
  "v_mfma_f32_16x16x32_bf16 a[132:135], a[236:239], v[176:179], a[132:135]\n\t"
  "v_mfma_f32_16x16x32_bf16 a[136:139], a[236:239], v[184:187], a[136:139]\n\t"
  "v_cvt_pk_bf16_f32 v136, v136, v137\n\t"
  "v_mfma_f32_16x16x32_bf16 a[140:143], a[240:243], v[152:155], a[140:143]\n\t"
  "v_exp_f32 v139, v139\n\t"
  "v_mfma_f32_16x16x32_bf16 a[144:147], a[240:243], v[160:163], a[144:147]\n\t"
  "v_exp_f32 v144, v144\n\t"
  "v_mfma_f32_16x16x32_bf16 a[148:151], a[240:243], v[168:171], a[148:151]\n\t"
  "v_cvt_pk_bf16_f32 v137, v138, v139\n\t"
  "v_mfma_f32_16x16x32_bf16 a[152:155], a[240:243], v[176:179], a[152:155]\n\t"
  "v_exp_f32 v145, v145\n\t"
  "v_mfma_f32_16x16x32_bf16 a[156:159], a[240:243], v[184:187], a[156:159]\n\t"
  "v_exp_f32 v146, v146\n\t"
  "v_mfma_f32_16x16x32_bf16 a[160:163], a[244:247], v[152:155], a[160:163]\n\t"
  "v_cvt_pk_bf16_f32 v144, v144, v145\n\t"
  "v_mfma_f32_16x16x32_bf16 a[164:167], a[244:247], v[160:163], a[164:167]\n\t"
  "v_exp_f32 v147, v147\n\t"
  "v_mfma_f32_16x16x32_bf16 a[168:171], a[244:247], v[168:171], a[168:171]\n\t"
  "v_mfma_f32_16x16x32_bf16 a[172:175], a[244:247], v[176:179], a[172:175]\n\t"
  "s_nop 0\n\t"
  "v_cvt_pk_bf16_f32 v145, v146, v147\n\t"
  "v_mfma_f32_16x16x32_bf16 a[176:179], a[244:247], v[184:187], a[176:179]\n\t"
  "s_waitcnt lgkmcnt(0)\n\t"
  "v_mfma_f32_16x16x32_bf16 v[152:155], a[180:183], v[32:35], v[192:195]\n\t"
  "v_exp_f32 v116, v116\n\t"
  "v_mfma_f32_16x16x32_bf16 v[160:163], a[180:183], v[48:51], v[196:199]\n\t"
  "ds_read_b64_tr_b16 a[212:213], v220 offset:32768\n\t"
  "v_mfma_f32_16x16x32_bf16 v[168:171], a[180:183], v[64:67], v[200:203]\n\t"
  "v_exp_f32 v117, v117\n\t"
  "v_mfma_f32_16x16x32_bf16 v[176:179], a[180:183], v[80:83], v[204:207]\n\t"
  "ds_read_b64_tr_b16 a[214:215], v220 offset:36864\n\t"
  "v_mfma_f32_16x16x32_bf16 v[184:187], a[180:183], v[96:99], v[208:211]\n\t"
  "v_exp_f32 v118, v118\n\t"
  "s_waitcnt vmcnt(4)\n\t"
  "buffer_load_dwordx4 v213, s[28:31], s52 offen offset:1024 nt lds\n\t"
  "v_mfma_f32_16x16x32_bf16 v[152:155], a[184:187], v[36:39], v[152:155]\n\t"
  "v_mfma_f32_16x16x32_bf16 v[160:163], a[184:187], v[52:55], v[160:163]\n\t"
  "v_mfma_f32_16x16x32_bf16 v[168:171], a[184:187], v[68:71], v[168:171]\n\t"
  "ds_read_b64_tr_b16 a[216:217], v221 offset:32768\n\t"
  "v_cvt_pk_bf16_f32 v114, v116, v117\n\t"
  "v_mfma_f32_16x16x32_bf16 v[176:179], a[184:187], v[84:87], v[176:179]\n\t"
  "v_exp_f32 v119, v119\n\t"
  "v_mfma_f32_16x16x32_bf16 v[184:187], a[184:187], v[100:103], v[184:187]\n\t"
  "ds_read_b64_tr_b16 a[218:219], v221 offset:36864\n\t"
  "v_mfma_f32_16x16x32_bf16 v[152:155], a[188:191], v[40:43], v[152:155]\n\t"
  "v_exp_f32 v124, v124\n\t"
  "v_mfma_f32_16x16x32_bf16 v[160:163], a[188:191], v[56:59], v[160:163]\n\t"
  "ds_read_b64_tr_b16 a[220:221], v220 offset:33280\n\t"
  "v_cvt_pk_bf16_f32 v115, v118, v119\n\t"
  "v_mfma_f32_16x16x32_bf16 v[168:171], a[188:191], v[72:75], v[168:171]\n\t"
  "v_exp_f32 v125, v125\n\t"
  "v_mfma_f32_16x16x32_bf16 v[176:179], a[188:191], v[88:91], v[176:179]\n\t"
  "ds_read_b64_tr_b16 a[222:223], v220 offset:37376\n\t"
  "v_mfma_f32_16x16x32_bf16 v[184:187], a[188:191], v[104:107], v[184:187]\n\t"
  "v_exp_f32 v126, v126\n\t"
  "v_mfma_f32_16x16x32_bf16 v[152:155], a[192:195], v[44:47], v[152:155]\n\t"
  "ds_read_b64_tr_b16 a[224:225], v221 offset:33280\n\t"
  "v_cvt_pk_bf16_f32 v122, v124, v125\n\t"
  "v_mfma_f32_16x16x32_bf16 v[160:163], a[192:195], v[60:63], v[160:163]\n\t"
  "v_exp_f32 v127, v127\n\t"
  "v_mfma_f32_16x16x32_bf16 v[168:171], a[192:195], v[76:79], v[168:171]\n\t"
  "ds_read_b64_tr_b16 a[226:227], v221 offset:37376\n\t"
  "s_waitcnt vmcnt(4)\n\t"
  "buffer_load_dwordx4 v214, s[28:31], s52 offen offset:2048 nt lds\n\t"
  "v_mfma_f32_16x16x32_bf16 v[176:179], a[192:195], v[92:95], v[176:179]\n\t"
  "v_mfma_f32_16x16x32_bf16 v[184:187], a[192:195], v[108:111], v[184:187]\n\t"
  "v_mfma_f32_16x16x32_bf16 v[156:159], a[196:199], v[32:35], v[192:195]\n\t"
  "v_exp_f32 v132, v132\n\t"
  "v_mfma_f32_16x16x32_bf16 v[164:167], a[196:199], v[48:51], v[196:199]\n\t"
  "ds_read_b64_tr_b16 a[228:229], v220 offset:33792\n\t"
  "v_cvt_pk_bf16_f32 v123, v126, v127\n\t"
  "v_mfma_f32_16x16x32_bf16 v[172:175], a[196:199], v[64:67], v[200:203]\n\t"
  "v_exp_f32 v133, v133\n\t"
  "v_mfma_f32_16x16x32_bf16 v[180:183], a[196:199], v[80:83], v[204:207]\n\t"
  "ds_read_b64_tr_b16 a[230:231], v220 offset:37888\n\t"
  "v_mfma_f32_16x16x32_bf16 v[188:191], a[196:199], v[96:99], v[208:211]\n\t"
  "v_exp_f32 v134, v134\n\t"
  "v_mfma_f32_16x16x32_bf16 v[156:159], a[200:203], v[36:39], v[156:159]\n\t"
  "ds_read_b64_tr_b16 a[232:233], v221 offset:33792\n\t"
  "v_cvt_pk_bf16_f32 v130, v132, v133\n\t"
  "v_mfma_f32_16x16x32_bf16 v[164:167], a[200:203], v[52:55], v[164:167]\n\t"
  "v_exp_f32 v135, v135\n\t"
  "v_mfma_f32_16x16x32_bf16 v[172:175], a[200:203], v[68:71], v[172:175]\n\t"
  "ds_read_b64_tr_b16 a[234:235], v221 offset:37888\n\t"
  "v_mfma_f32_16x16x32_bf16 v[180:183], a[200:203], v[84:87], v[180:183]\n\t"
  "v_exp_f32 v140, v140\n\t"
  "v_mfma_f32_16x16x32_bf16 v[188:191], a[200:203], v[100:103], v[188:191]\n\t"
  "ds_read_b64_tr_b16 a[236:237], v220 offset:34304\n\t"
  "v_cvt_pk_bf16_f32 v131, v134, v135\n\t"
  "v_mfma_f32_16x16x32_bf16 v[156:159], a[204:207], v[40:43], v[156:159]\n\t"
  "v_exp_f32 v141, v141\n\t"
  "v_mfma_f32_16x16x32_bf16 v[164:167], a[204:207], v[56:59], v[164:167]\n\t"
  "ds_read_b64_tr_b16 a[238:239], v220 offset:38400\n\t"
  "s_waitcnt vmcnt(4)\n\t"
  "buffer_load_dwordx4 v215, s[28:31], s52 offen offset:3072 nt lds\n\t"
  "v_mfma_f32_16x16x32_bf16 v[172:175], a[204:207], v[72:75], v[172:175]\n\t"
  "v_mfma_f32_16x16x32_bf16 v[180:183], a[204:207], v[88:91], v[180:183]\n\t"
  "v_mfma_f32_16x16x32_bf16 v[188:191], a[204:207], v[104:107], v[188:191]\n\t"
  "v_exp_f32 v142, v142\n\t"
  "v_mfma_f32_16x16x32_bf16 v[156:159], a[208:211], v[44:47], v[156:159]\n\t"
  "ds_read_b64_tr_b16 a[240:241], v221 offset:34304\n\t"
  "v_cvt_pk_bf16_f32 v138, v140, v141\n\t"
  "v_mfma_f32_16x16x32_bf16 v[164:167], a[208:211], v[60:63], v[164:167]\n\t"
  "v_exp_f32 v143, v143\n\t"
  "v_mfma_f32_16x16x32_bf16 v[172:175], a[208:211], v[76:79], v[172:175]\n\t"
  "ds_read_b64_tr_b16 a[242:243], v221 offset:38400\n\t"
  "v_mfma_f32_16x16x32_bf16 v[180:183], a[208:211], v[92:95], v[180:183]\n\t"
  "v_exp_f32 v148, v148\n\t"
  "v_cvt_pk_bf16_f32 v139, v142, v143\n\t"
  "v_exp_f32 v149, v149\n\t"
  "v_exp_f32 v150, v150\n\t"
  "v_cvt_pk_bf16_f32 v146, v148, v149\n\t"
  "v_exp_f32 v151, v151\n\t"
  "s_nop 0\n\t"
  "v_cvt_pk_bf16_f32 v147, v150, v151\n\t"
  "v_mfma_f32_16x16x32_bf16 v[188:191], a[208:211], v[108:111], v[188:191]\n\t"
  "s_waitcnt vmcnt(8) lgkmcnt(0)\n\t"
  "s_barrier\n\t"
  "v_mfma_f32_16x16x32_bf16 a[0:3], a[212:215], v[112:115], a[0:3]\n\t"
  "v_exp_f32 v152, v152\n\t"
  "v_mfma_f32_16x16x32_bf16 a[4:7], a[212:215], v[120:123], a[4:7]\n\t"
  "v_add_u32 v216, 65536, v216\n\t"
  "v_add_u32 v217, 65536, v217\n\t"
  "v_add_u32 v218, 65536, v218\n\t"
  "v_add_u32 v219, 65536, v219\n\t"
  "v_mfma_f32_16x16x32_bf16 a[8:11], a[212:215], v[128:131], a[8:11]\n\t"
  "v_mfma_f32_16x16x32_bf16 a[12:15], a[212:215], v[136:139], a[12:15]\n\t"
  "s_waitcnt vmcnt(4)\n\t"
  "s_add_u32 m0, s51, 131072\n\t"
  "s_mov_b32 s52, 106496\n\t"
  "buffer_load_dwordx4 v212, s[28:31], s52 offen offset:0 nt lds\n\t"
  "v_mfma_f32_16x16x32_bf16 a[16:19], a[212:215], v[144:147], a[16:19]\n\t"
  "v_mfma_f32_16x16x32_bf16 a[20:23], a[216:219], v[112:115], a[20:23]\n\t"
  "v_mfma_f32_16x16x32_bf16 a[24:27], a[216:219], v[120:123], a[24:27]\n\t"
  "v_exp_f32 v153, v153\n\t"
  "v_mfma_f32_16x16x32_bf16 a[28:31], a[216:219], v[128:131], a[28:31]\n\t"
  "v_exp_f32 v154, v154\n\t"
  "v_mfma_f32_16x16x32_bf16 a[32:35], a[216:219], v[136:139], a[32:35]\n\t"
  "v_cvt_pk_bf16_f32 v152, v152, v153\n\t"
  "v_mfma_f32_16x16x32_bf16 a[36:39], a[216:219], v[144:147], a[36:39]\n\t"
  "v_exp_f32 v155, v155\n\t"
  "v_mfma_f32_16x16x32_bf16 a[40:43], a[220:223], v[112:115], a[40:43]\n\t"
  "v_exp_f32 v160, v160\n\t"
  "v_mfma_f32_16x16x32_bf16 a[44:47], a[220:223], v[120:123], a[44:47]\n\t"
  "v_cvt_pk_bf16_f32 v153, v154, v155\n\t"
  "ds_read_b128 a[180:183], v216 offset:0\n\t"
  "v_mfma_f32_16x16x32_bf16 a[48:51], a[220:223], v[128:131], a[48:51]\n\t"
  "v_exp_f32 v161, v161\n\t"
  "v_mfma_f32_16x16x32_bf16 a[52:55], a[220:223], v[136:139], a[52:55]\n\t"
  "v_exp_f32 v162, v162\n\t"
  "v_mfma_f32_16x16x32_bf16 a[56:59], a[220:223], v[144:147], a[56:59]\n\t"
  "ds_read_b128 a[184:187], v217 offset:0\n\t"
  "v_cvt_pk_bf16_f32 v160, v160, v161\n\t"
  "v_mfma_f32_16x16x32_bf16 a[60:63], a[224:227], v[112:115], a[60:63]\n\t"
  "s_waitcnt vmcnt(4)\n\t"
  "buffer_load_dwordx4 v213, s[28:31], s52 offen offset:1024 nt lds\n\t"
  "v_mfma_f32_16x16x32_bf16 a[64:67], a[224:227], v[120:123], a[64:67]\n\t"
  "v_mfma_f32_16x16x32_bf16 a[68:71], a[224:227], v[128:131], a[68:71]\n\t"
  "v_exp_f32 v163, v163\n\t"
  "v_mfma_f32_16x16x32_bf16 a[72:75], a[224:227], v[136:139], a[72:75]\n\t"
  "ds_read_b128 a[188:191], v218 offset:0\n\t"
  "v_mfma_f32_16x16x32_bf16 a[76:79], a[224:227], v[144:147], a[76:79]\n\t"
  "v_exp_f32 v168, v168\n\t"
  "v_mfma_f32_16x16x32_bf16 a[80:83], a[228:231], v[112:115], a[80:83]\n\t"
  "ds_read_b128 a[192:195], v219 offset:0\n\t"
  "v_cvt_pk_bf16_f32 v161, v162, v163\n\t"
  "v_mfma_f32_16x16x32_bf16 a[84:87], a[228:231], v[120:123], a[84:87]\n\t"
  "v_exp_f32 v169, v169\n\t"
  "v_mfma_f32_16x16x32_bf16 a[88:91], a[228:231], v[128:131], a[88:91]\n\t"
  "ds_read_b128 a[196:199], v216 offset:4096\n\t"
  "v_mfma_f32_16x16x32_bf16 a[92:95], a[228:231], v[136:139], a[92:95]\n\t"
  "v_exp_f32 v170, v170\n\t"
  "v_mfma_f32_16x16x32_bf16 a[96:99], a[228:231], v[144:147], a[96:99]\n\t"
  "v_cvt_pk_bf16_f32 v168, v168, v169\n\t"
  "ds_read_b128 a[200:203], v217 offset:4096\n\t"
  "v_mfma_f32_16x16x32_bf16 a[100:103], a[232:235], v[112:115], a[100:103]\n\t"
  "v_exp_f32 v171, v171\n\t"
  "v_mfma_f32_16x16x32_bf16 a[104:107], a[232:235], v[120:123], a[104:107]\n\t"
  "s_waitcnt vmcnt(4)\n\t"
  "buffer_load_dwordx4 v214, s[28:31], s52 offen offset:2048 nt lds\n\t"
  "v_mfma_f32_16x16x32_bf16 a[108:111], a[232:235], v[128:131], a[108:111]\n\t"
  "v_mfma_f32_16x16x32_bf16 a[112:115], a[232:235], v[136:139], a[112:115]\n\t"
  "ds_read_b128 a[204:207], v218 offset:4096\n\t"
  "v_mfma_f32_16x16x32_bf16 a[116:119], a[232:235], v[144:147], a[116:119]\n\t"
  "v_exp_f32 v176, v176\n\t"
  "v_mfma_f32_16x16x32_bf16 a[120:123], a[236:239], v[112:115], a[120:123]\n\t"
  "v_cvt_pk_bf16_f32 v169, v170, v171\n\t"
  "ds_read_b128 a[208:211], v219 offset:4096\n\t"
  "v_mfma_f32_16x16x32_bf16 a[124:127], a[236:239], v[120:123], a[124:127]\n\t"
  "v_exp_f32 v177, v177\n\t"
  "v_mfma_f32_16x16x32_bf16 a[128:131], a[236:239], v[128:131], a[128:131]\n\t"
  "v_exp_f32 v178, v178\n\t"
  "v_mfma_f32_16x16x32_bf16 a[132:135], a[236:239], v[136:139], a[132:135]\n\t"
  "v_cvt_pk_bf16_f32 v176, v176, v177\n\t"
  "v_mfma_f32_16x16x32_bf16 a[136:139], a[236:239], v[144:147], a[136:139]\n\t"
  "v_exp_f32 v179, v179\n\t"
  "v_mfma_f32_16x16x32_bf16 a[140:143], a[240:243], v[112:115], a[140:143]\n\t"
  "s_waitcnt vmcnt(4)\n\t"
  "buffer_load_dwordx4 v215, s[28:31], s52 offen offset:3072 nt lds\n\t"
  "v_mfma_f32_16x16x32_bf16 a[144:147], a[240:243], v[120:123], a[144:147]\n\t"
  "v_mfma_f32_16x16x32_bf16 a[148:151], a[240:243], v[128:131], a[148:151]\n\t"
  "v_exp_f32 v184, v184\n\t"
  "v_mfma_f32_16x16x32_bf16 a[152:155], a[240:243], v[136:139], a[152:155]\n\t"
  "v_cvt_pk_bf16_f32 v177, v178, v179\n\t"
  "v_mfma_f32_16x16x32_bf16 a[156:159], a[240:243], v[144:147], a[156:159]\n\t"
  "v_exp_f32 v185, v185\n\t"
  "v_mfma_f32_16x16x32_bf16 a[160:163], a[244:247], v[112:115], a[160:163]\n\t"
  "v_exp_f32 v186, v186\n\t"
  "v_mfma_f32_16x16x32_bf16 a[164:167], a[244:247], v[120:123], a[164:167]\n\t"
  "v_cvt_pk_bf16_f32 v184, v184, v185\n\t"
  "v_mfma_f32_16x16x32_bf16 a[168:171], a[244:247], v[128:131], a[168:171]\n\t"
  "v_exp_f32 v187, v187\n\t"
  "v_mfma_f32_16x16x32_bf16 a[172:175], a[244:247], v[136:139], a[172:175]\n\t"
  "s_nop 0\n\t"
  "v_cvt_pk_bf16_f32 v185, v186, v187\n\t"
  "v_mfma_f32_16x16x32_bf16 a[176:179], a[244:247], v[144:147], a[176:179]\n\t"
  "s_waitcnt lgkmcnt(0)\n\t"
  "v_mfma_f32_16x16x32_bf16 v[112:115], a[180:183], v[32:35], v[192:195]\n\t"
  "v_exp_f32 v156, v156\n\t"
  "v_mfma_f32_16x16x32_bf16 v[120:123], a[180:183], v[48:51], v[196:199]\n\t"
  "v_add_u32 v220, 65536, v220\n\t"
  "v_add_u32 v221, 65536, v221\n\t"
  "v_mfma_f32_16x16x32_bf16 v[128:131], a[180:183], v[64:67], v[200:203]\n\t"
  "v_exp_f32 v157, v157\n\t"
  "v_mfma_f32_16x16x32_bf16 v[136:139], a[180:183], v[80:83], v[204:207]\n\t"
  "v_exp_f32 v158, v158\n\t"
  "s_waitcnt vmcnt(4)\n\t"
  "s_add_u32 m0, s51, 135168\n\t"
  "s_mov_b32 s52, 110592\n\t"
  "buffer_load_dwordx4 v212, s[28:31], s52 offen offset:0 nt lds\n\t"
  "v_mfma_f32_16x16x32_bf16 v[144:147], a[180:183], v[96:99], v[208:211]\n\t"
  "v_mfma_f32_16x16x32_bf16 v[112:115], a[184:187], v[36:39], v[112:115]\n\t"
  "v_mfma_f32_16x16x32_bf16 v[120:123], a[184:187], v[52:55], v[120:123]\n\t"
  "v_mfma_f32_16x16x32_bf16 v[128:131], a[184:187], v[68:71], v[128:131]\n\t"
  "ds_read_b64_tr_b16 a[212:213], v220 offset:0\n\t"
  "v_cvt_pk_bf16_f32 v154, v156, v157\n\t"
  "v_mfma_f32_16x16x32_bf16 v[136:139], a[184:187], v[84:87], v[136:139]\n\t"
  "ds_read_b64_tr_b16 a[214:215], v220 offset:4096\n\t"
  "v_mfma_f32_16x16x32_bf16 v[144:147], a[184:187], v[100:103], v[144:147]\n\t"
  "v_exp_f32 v159, v159\n\t"
  "v_mfma_f32_16x16x32_bf16 v[112:115], a[188:191], v[40:43], v[112:115]\n\t"
  "ds_read_b64_tr_b16 a[216:217], v221 offset:0\n\t"
  "v_mfma_f32_16x16x32_bf16 v[120:123], a[188:191], v[56:59], v[120:123]\n\t"
  "v_exp_f32 v164, v164\n\t"
  "v_mfma_f32_16x16x32_bf16 v[128:131], a[188:191], v[72:75], v[128:131]\n\t"
  "v_cvt_pk_bf16_f32 v155, v158, v159\n\t"
  "ds_read_b64_tr_b16 a[218:219], v221 offset:4096\n\t"
  "v_mfma_f32_16x16x32_bf16 v[136:139], a[188:191], v[88:91], v[136:139]\n\t"
  "v_exp_f32 v165, v165\n\t"
  "v_mfma_f32_16x16x32_bf16 v[144:147], a[188:191], v[104:107], v[144:147]\n\t"
  "ds_read_b64_tr_b16 a[220:221], v220 offset:512\n\t"
  "v_mfma_f32_16x16x32_bf16 v[112:115], a[192:195], v[44:47], v[112:115]\n\t"
  "v_exp_f32 v166, v166\n\t"
  "v_mfma_f32_16x16x32_bf16 v[120:123], a[192:195], v[60:63], v[120:123]\n\t"
  "ds_read_b64_tr_b16 a[222:223], v220 offset:4608\n\t"
  "v_cvt_pk_bf16_f32 v162, v164, v165\n\t"
  "v_mfma_f32_16x16x32_bf16 v[128:131], a[192:195], v[76:79], v[128:131]\n\t"
  "v_exp_f32 v167, v167\n\t"
  "v_mfma_f32_16x16x32_bf16 v[136:139], a[192:195], v[92:95], v[136:139]\n\t"
  "ds_read_b64_tr_b16 a[224:225], v221 offset:512\n\t"
  "v_mfma_f32_16x16x32_bf16 v[144:147], a[192:195], v[108:111], v[144:147]\n\t"
  "v_exp_f32 v172, v172\n\t"
  "v_mfma_f32_16x16x32_bf16 v[116:119], a[196:199], v[32:35], v[192:195]\n\t"
  "ds_read_b64_tr_b16 a[226:227], v221 offset:4608\n\t"
  "v_cvt_pk_bf16_f32 v163, v166, v167\n\t"
  "v_mfma_f32_16x16x32_bf16 v[124:127], a[196:199], v[48:51], v[196:199]\n\t"
  "v_exp_f32 v173, v173\n\t"
  "v_mfma_f32_16x16x32_bf16 v[132:135], a[196:199], v[64:67], v[200:203]\n\t"
  "ds_read_b64_tr_b16 a[228:229], v220 offset:1024\n\t"
  "s_waitcnt vmcnt(4)\n\t"
  "buffer_load_dwordx4 v213, s[28:31], s52 offen offset:1024 nt lds\n\t"
  "v_mfma_f32_16x16x32_bf16 v[140:143], a[196:199], v[80:83], v[204:207]\n\t"
  "v_mfma_f32_16x16x32_bf16 v[148:151], a[196:199], v[96:99], v[208:211]\n\t"
  "v_mfma_f32_16x16x32_bf16 v[116:119], a[200:203], v[36:39], v[116:119]\n\t"
  "v_exp_f32 v174, v174\n\t"
  "v_mfma_f32_16x16x32_bf16 v[124:127], a[200:203], v[52:55], v[124:127]\n\t"
  "ds_read_b64_tr_b16 a[230:231], v220 offset:5120\n\t"
  "v_cvt_pk_bf16_f32 v170, v172, v173\n\t"
  "v_mfma_f32_16x16x32_bf16 v[132:135], a[200:203], v[68:71], v[132:135]\n\t"
  "ds_read_b64_tr_b16 a[232:233], v221 offset:1024\n\t"
  "v_mfma_f32_16x16x32_bf16 v[140:143], a[200:203], v[84:87], v[140:143]\n\t"
  "v_exp_f32 v175, v175\n\t"
  "v_mfma_f32_16x16x32_bf16 v[148:151], a[200:203], v[100:103], v[148:151]\n\t"
  "v_exp_f32 v180, v180\n\t"
  "v_mfma_f32_16x16x32_bf16 v[116:119], a[204:207], v[40:43], v[116:119]\n\t"
  "ds_read_b64_tr_b16 a[234:235], v221 offset:5120\n\t"
  "v_cvt_pk_bf16_f32 v171, v174, v175\n\t"
  "v_mfma_f32_16x16x32_bf16 v[124:127], a[204:207], v[56:59], v[124:127]\n\t"
  "ds_read_b64_tr_b16 a[236:237], v220 offset:1536\n\t"
  "v_mfma_f32_16x16x32_bf16 v[132:135], a[204:207], v[72:75], v[132:135]\n\t"
  "v_exp_f32 v181, v181\n\t"
  "v_mfma_f32_16x16x32_bf16 v[140:143], a[204:207], v[88:91], v[140:143]\n\t"
  "ds_read_b64_tr_b16 a[238:239], v220 offset:5632\n\t"
  "v_mfma_f32_16x16x32_bf16 v[148:151], a[204:207], v[104:107], v[148:151]\n\t"
  "v_exp_f32 v182, v182\n\t"
  "s_waitcnt vmcnt(4)\n\t"
  "buffer_load_dwordx4 v214, s[28:31], s52 offen offset:2048 nt lds\n\t"
  "v_mfma_f32_16x16x32_bf16 v[116:119], a[208:211], v[44:47], v[116:119]\n\t"
  "v_mfma_f32_16x16x32_bf16 v[124:127], a[208:211], v[60:63], v[124:127]\n\t"
  "v_mfma_f32_16x16x32_bf16 v[132:135], a[208:211], v[76:79], v[132:135]\n\t"
  "ds_read_b64_tr_b16 a[240:241], v221 offset:1536\n\t"
  "v_cvt_pk_bf16_f32 v178, v180, v181\n\t"
  "v_mfma_f32_16x16x32_bf16 v[140:143], a[208:211], v[92:95], v[140:143]\n\t"
  "v_exp_f32 v183, v183\n\t"
  "ds_read_b64_tr_b16 a[242:243], v221 offset:5632\n\t"
  "v_exp_f32 v188, v188\n\t"
  "v_cvt_pk_bf16_f32 v179, v182, v183\n\t"
  "v_exp_f32 v189, v189\n\t"
  "v_exp_f32 v190, v190\n\t"
  "v_cvt_pk_bf16_f32 v186, v188, v189\n\t"
  "v_exp_f32 v191, v191\n\t"
  "s_nop 0\n\t"
  "v_cvt_pk_bf16_f32 v187, v190, v191\n\t"
  "v_mfma_f32_16x16x32_bf16 v[148:151], a[208:211], v[108:111], v[148:151]\n\t"
  "s_waitcnt vmcnt(7) lgkmcnt(0)\n\t"
  "s_barrier\n\t"
  "v_mfma_f32_16x16x32_bf16 a[0:3], a[212:215], v[152:155], a[0:3]\n\t"
  "v_exp_f32 v112, v112\n\t"
  "v_mfma_f32_16x16x32_bf16 a[4:7], a[212:215], v[160:163], a[4:7]\n\t"
  "ds_read_b128 a[180:183], v216 offset:32768\n\t"
  "s_waitcnt vmcnt(4)\n\t"
  "buffer_load_dwordx4 v215, s[28:31], s52 offen offset:3072 nt lds\n\t"
  "v_mfma_f32_16x16x32_bf16 a[8:11], a[212:215], v[168:171], a[8:11]\n\t"
  "v_mfma_f32_16x16x32_bf16 a[12:15], a[212:215], v[176:179], a[12:15]\n\t"
  "v_mfma_f32_16x16x32_bf16 a[16:19], a[212:215], v[184:187], a[16:19]\n\t"
  "v_exp_f32 v113, v113\n\t"
  "v_mfma_f32_16x16x32_bf16 a[20:23], a[216:219], v[152:155], a[20:23]\n\t"
  "v_exp_f32 v114, v114\n\t"
  "v_mfma_f32_16x16x32_bf16 a[24:27], a[216:219], v[160:163], a[24:27]\n\t"
  "ds_read_b128 a[184:187], v217 offset:32768\n\t"
  "v_cvt_pk_bf16_f32 v112, v112, v113\n\t"
  "v_mfma_f32_16x16x32_bf16 a[28:31], a[216:219], v[168:171], a[28:31]\n\t"
  "v_exp_f32 v115, v115\n\t"
  "v_mfma_f32_16x16x32_bf16 a[32:35], a[216:219], v[176:179], a[32:35]\n\t"
  "ds_read_b128 a[188:191], v218 offset:32768\n\t"
  "v_mfma_f32_16x16x32_bf16 a[36:39], a[216:219], v[184:187], a[36:39]\n\t"
  "v_exp_f32 v120, v120\n\t"
  "v_mfma_f32_16x16x32_bf16 a[40:43], a[220:223], v[152:155], a[40:43]\n\t"
  "v_cvt_pk_bf16_f32 v113, v114, v115\n\t"
  "v_mfma_f32_16x16x32_bf16 a[44:47], a[220:223], v[160:163], a[44:47]\n\t"
  "v_exp_f32 v121, v121\n\t"
  "v_mfma_f32_16x16x32_bf16 a[48:51], a[220:223], v[168:171], a[48:51]\n\t"
  "s_waitcnt vmcnt(4)\n\t"
  "s_add_u32 m0, s51, 0\n\t"
  "s_mov_b32 s52, 114688\n\t"
  "buffer_load_dwordx4 v212, s[28:31], s52 offen offset:0 nt lds\n\t"
  "v_mfma_f32_16x16x32_bf16 a[52:55], a[220:223], v[176:179], a[52:55]\n\t"
  "v_mfma_f32_16x16x32_bf16 a[56:59], a[220:223], v[184:187], a[56:59]\n\t"
  "v_mfma_f32_16x16x32_bf16 a[60:63], a[224:227], v[152:155], a[60:63]\n\t"
  "ds_read_b128 a[192:195], v219 offset:32768\n\t"
  "v_mfma_f32_16x16x32_bf16 a[64:67], a[224:227], v[160:163], a[64:67]\n\t"
  "v_exp_f32 v122, v122\n\t"
  "v_mfma_f32_16x16x32_bf16 a[68:71], a[224:227], v[168:171], a[68:71]\n\t"
  "v_cvt_pk_bf16_f32 v120, v120, v121\n\t"
  "v_mfma_f32_16x16x32_bf16 a[72:75], a[224:227], v[176:179], a[72:75]\n\t"
  "v_exp_f32 v123, v123\n\t"
  "v_mfma_f32_16x16x32_bf16 a[76:79], a[224:227], v[184:187], a[76:79]\n\t"
  "ds_read_b128 a[196:199], v216 offset:36864\n\t"
  "v_mfma_f32_16x16x32_bf16 a[80:83], a[228:231], v[152:155], a[80:83]\n\t"
  "v_exp_f32 v128, v128\n\t"
  "v_mfma_f32_16x16x32_bf16 a[84:87], a[228:231], v[160:163], a[84:87]\n\t"
  "v_cvt_pk_bf16_f32 v121, v122, v123\n\t"
  "ds_read_b128 a[200:203], v217 offset:36864\n\t"
  "v_mfma_f32_16x16x32_bf16 a[88:91], a[228:231], v[168:171], a[88:91]\n\t"
  "v_exp_f32 v129, v129\n\t"
  "v_mfma_f32_16x16x32_bf16 a[92:95], a[228:231], v[176:179], a[92:95]\n\t"
  "v_exp_f32 v130, v130\n\t"
  "v_mfma_f32_16x16x32_bf16 a[96:99], a[228:231], v[184:187], a[96:99]\n\t"
  "v_cvt_pk_bf16_f32 v128, v128, v129\n\t"
  "ds_read_b128 a[204:207], v218 offset:36864\n\t"
  "v_mfma_f32_16x16x32_bf16 a[100:103], a[232:235], v[152:155], a[100:103]\n\t"
  "v_exp_f32 v131, v131\n\t"
  "v_mfma_f32_16x16x32_bf16 a[104:107], a[232:235], v[160:163], a[104:107]\n\t"
  "s_waitcnt vmcnt(4)\n\t"
  "buffer_load_dwordx4 v213, s[28:31], s52 offen offset:1024 nt lds\n\t"
  "v_mfma_f32_16x16x32_bf16 a[108:111], a[232:235], v[168:171], a[108:111]\n\t"
  "v_mfma_f32_16x16x32_bf16 a[112:115], a[232:235], v[176:179], a[112:115]\n\t"
  "v_exp_f32 v136, v136\n\t"
  "v_mfma_f32_16x16x32_bf16 a[116:119], a[232:235], v[184:187], a[116:119]\n\t"
  "ds_read_b128 a[208:211], v219 offset:36864\n\t"
  "v_cvt_pk_bf16_f32 v129, v130, v131\n\t"
  "v_mfma_f32_16x16x32_bf16 a[120:123], a[236:239], v[152:155], a[120:123]\n\t"
  "v_exp_f32 v137, v137\n\t"
  "v_mfma_f32_16x16x32_bf16 a[124:127], a[236:239], v[160:163], a[124:127]\n\t"
  "v_exp_f32 v138, v138\n\t"
  "v_mfma_f32_16x16x32_bf16 a[128:131], a[236:239], v[168:171], a[128:131]\n\t"
  "v_cvt_pk_bf16_f32 v136, v136, v137\n\t"
  "v_mfma_f32_16x16x32_bf16 a[132:135], a[236:239], v[176:179], a[132:135]\n\t"
  "v_exp_f32 v139, v139\n\t"
  "v_mfma_f32_16x16x32_bf16 a[136:139], a[236:239], v[184:187], a[136:139]\n\t"
  "s_waitcnt vmcnt(4)\n\t"
  "buffer_load_dwordx4 v214, s[28:31], s52 offen offset:2048 nt lds\n\t"
  "v_mfma_f32_16x16x32_bf16 a[140:143], a[240:243], v[152:155], a[140:143]\n\t"
  "v_mfma_f32_16x16x32_bf16 a[144:147], a[240:243], v[160:163], a[144:147]\n\t"
  "v_exp_f32 v144, v144\n\t"
  "v_mfma_f32_16x16x32_bf16 a[148:151], a[240:243], v[168:171], a[148:151]\n\t"
  "v_cvt_pk_bf16_f32 v137, v138, v139\n\t"
  "v_mfma_f32_16x16x32_bf16 a[152:155], a[240:243], v[176:179], a[152:155]\n\t"
  "v_exp_f32 v145, v145\n\t"
  "v_mfma_f32_16x16x32_bf16 a[156:159], a[240:243], v[184:187], a[156:159]\n\t"
  "v_exp_f32 v146, v146\n\t"
  "v_mfma_f32_16x16x32_bf16 a[160:163], a[244:247], v[152:155], a[160:163]\n\t"
  "v_cvt_pk_bf16_f32 v144, v144, v145\n\t"
  "v_mfma_f32_16x16x32_bf16 a[164:167], a[244:247], v[160:163], a[164:167]\n\t"
  "v_exp_f32 v147, v147\n\t"
  "v_mfma_f32_16x16x32_bf16 a[168:171], a[244:247], v[168:171], a[168:171]\n\t"
  "v_mfma_f32_16x16x32_bf16 a[172:175], a[244:247], v[176:179], a[172:175]\n\t"
  "s_nop 0\n\t"
  "v_cvt_pk_bf16_f32 v145, v146, v147\n\t"
  "v_mfma_f32_16x16x32_bf16 a[176:179], a[244:247], v[184:187], a[176:179]\n\t"
  "s_waitcnt lgkmcnt(0)\n\t"
  "v_mfma_f32_16x16x32_bf16 v[152:155], a[180:183], v[32:35], v[192:195]\n\t"
  "v_exp_f32 v116, v116\n\t"
  "v_mfma_f32_16x16x32_bf16 v[160:163], a[180:183], v[48:51], v[196:199]\n\t"
  "ds_read_b64_tr_b16 a[212:213], v220 offset:32768\n\t"
  "v_mfma_f32_16x16x32_bf16 v[168:171], a[180:183], v[64:67], v[200:203]\n\t"
  "v_exp_f32 v117, v117\n\t"
  "v_mfma_f32_16x16x32_bf16 v[176:179], a[180:183], v[80:83], v[204:207]\n\t"
  "ds_read_b64_tr_b16 a[214:215], v220 offset:36864\n\t"
  "v_mfma_f32_16x16x32_bf16 v[184:187], a[180:183], v[96:99], v[208:211]\n\t"
  "v_exp_f32 v118, v118\n\t"
  "s_waitcnt vmcnt(4)\n\t"
  "buffer_load_dwordx4 v215, s[28:31], s52 offen offset:3072 nt lds\n\t"
  "v_mfma_f32_16x16x32_bf16 v[152:155], a[184:187], v[36:39], v[152:155]\n\t"
  "v_mfma_f32_16x16x32_bf16 v[160:163], a[184:187], v[52:55], v[160:163]\n\t"
  "v_mfma_f32_16x16x32_bf16 v[168:171], a[184:187], v[68:71], v[168:171]\n\t"
  "ds_read_b64_tr_b16 a[216:217], v221 offset:32768\n\t"
  "v_cvt_pk_bf16_f32 v114, v116, v117\n\t"
  "v_mfma_f32_16x16x32_bf16 v[176:179], a[184:187], v[84:87], v[176:179]\n\t"
  "v_exp_f32 v119, v119\n\t"
  "v_mfma_f32_16x16x32_bf16 v[184:187], a[184:187], v[100:103], v[184:187]\n\t"
  "ds_read_b64_tr_b16 a[218:219], v221 offset:36864\n\t"
  "v_mfma_f32_16x16x32_bf16 v[152:155], a[188:191], v[40:43], v[152:155]\n\t"
  "v_exp_f32 v124, v124\n\t"
  "v_mfma_f32_16x16x32_bf16 v[160:163], a[188:191], v[56:59], v[160:163]\n\t"
  "ds_read_b64_tr_b16 a[220:221], v220 offset:33280\n\t"
  "v_cvt_pk_bf16_f32 v115, v118, v119\n\t"
  "v_mfma_f32_16x16x32_bf16 v[168:171], a[188:191], v[72:75], v[168:171]\n\t"
  "v_exp_f32 v125, v125\n\t"
  "v_mfma_f32_16x16x32_bf16 v[176:179], a[188:191], v[88:91], v[176:179]\n\t"
  "ds_read_b64_tr_b16 a[222:223], v220 offset:37376\n\t"
  "v_mfma_f32_16x16x32_bf16 v[184:187], a[188:191], v[104:107], v[184:187]\n\t"
  "v_exp_f32 v126, v126\n\t"
  "v_mfma_f32_16x16x32_bf16 v[152:155], a[192:195], v[44:47], v[152:155]\n\t"
  "ds_read_b64_tr_b16 a[224:225], v221 offset:33280\n\t"
  "v_cvt_pk_bf16_f32 v122, v124, v125\n\t"
  "s_waitcnt vmcnt(4)\n\t"
  "s_add_u32 m0, s51, 4096\n\t"
  "s_mov_b32 s52, 118784\n\t"
  "buffer_load_dwordx4 v212, s[28:31], s52 offen offset:0 nt lds\n\t"
  "v_mfma_f32_16x16x32_bf16 v[160:163], a[192:195], v[60:63], v[160:163]\n\t"
  "v_mfma_f32_16x16x32_bf16 v[168:171], a[192:195], v[76:79], v[168:171]\n\t"
  "v_mfma_f32_16x16x32_bf16 v[176:179], a[192:195], v[92:95], v[176:179]\n\t"
  "v_mfma_f32_16x16x32_bf16 v[184:187], a[192:195], v[108:111], v[184:187]\n\t"
  "v_exp_f32 v127, v127\n\t"
  "v_mfma_f32_16x16x32_bf16 v[156:159], a[196:199], v[32:35], v[192:195]\n\t"
  "ds_read_b64_tr_b16 a[226:227], v221 offset:37376\n\t"
  "v_mfma_f32_16x16x32_bf16 v[164:167], a[196:199], v[48:51], v[196:199]\n\t"
  "v_exp_f32 v132, v132\n\t"
  "v_mfma_f32_16x16x32_bf16 v[172:175], a[196:199], v[64:67], v[200:203]\n\t"
  "ds_read_b64_tr_b16 a[228:229], v220 offset:33792\n\t"
  "v_cvt_pk_bf16_f32 v123, v126, v127\n\t"
  "v_mfma_f32_16x16x32_bf16 v[180:183], a[196:199], v[80:83], v[204:207]\n\t"
  "v_exp_f32 v133, v133\n\t"
  "v_mfma_f32_16x16x32_bf16 v[188:191], a[196:199], v[96:99], v[208:211]\n\t"
  "ds_read_b64_tr_b16 a[230:231], v220 offset:37888\n\t"
  "v_mfma_f32_16x16x32_bf16 v[156:159], a[200:203], v[36:39], v[156:159]\n\t"
  "v_exp_f32 v134, v134\n\t"
  "v_mfma_f32_16x16x32_bf16 v[164:167], a[200:203], v[52:55], v[164:167]\n\t"
  "ds_read_b64_tr_b16 a[232:233], v221 offset:33792\n\t"
  "v_cvt_pk_bf16_f32 v130, v132, v133\n\t"
  "v_mfma_f32_16x16x32_bf16 v[172:175], a[200:203], v[68:71], v[172:175]\n\t"
  "v_exp_f32 v135, v135\n\t"
  "v_mfma_f32_16x16x32_bf16 v[180:183], a[200:203], v[84:87], v[180:183]\n\t"
  "ds_read_b64_tr_b16 a[234:235], v221 offset:37888\n\t"
  "v_mfma_f32_16x16x32_bf16 v[188:191], a[200:203], v[100:103], v[188:191]\n\t"
  "v_exp_f32 v140, v140\n\t"
  "v_mfma_f32_16x16x32_bf16 v[156:159], a[204:207], v[40:43], v[156:159]\n\t"
  "ds_read_b64_tr_b16 a[236:237], v220 offset:34304\n\t"
  "v_cvt_pk_bf16_f32 v131, v134, v135\n\t"
  "v_mfma_f32_16x16x32_bf16 v[164:167], a[204:207], v[56:59], v[164:167]\n\t"
  "v_exp_f32 v141, v141\n\t"
  "v_mfma_f32_16x16x32_bf16 v[172:175], a[204:207], v[72:75], v[172:175]\n\t"
  "ds_read_b64_tr_b16 a[238:239], v220 offset:38400\n\t"
  "v_mfma_f32_16x16x32_bf16 v[180:183], a[204:207], v[88:91], v[180:183]\n\t"
  "v_exp_f32 v142, v142\n\t"
  "v_mfma_f32_16x16x32_bf16 v[188:191], a[204:207], v[104:107], v[188:191]\n\t"
  "ds_read_b64_tr_b16 a[240:241], v221 offset:34304\n\t"
  "s_waitcnt vmcnt(4)\n\t"
  "buffer_load_dwordx4 v213, s[28:31], s52 offen offset:1024 nt lds\n\t"
  "v_mfma_f32_16x16x32_bf16 v[156:159], a[208:211], v[44:47], v[156:159]\n\t"
  "v_mfma_f32_16x16x32_bf16 v[164:167], a[208:211], v[60:63], v[164:167]\n\t"
  "v_cvt_pk_bf16_f32 v138, v140, v141\n\t"
  "v_mfma_f32_16x16x32_bf16 v[172:175], a[208:211], v[76:79], v[172:175]\n\t"
  "v_exp_f32 v143, v143\n\t"
  "v_mfma_f32_16x16x32_bf16 v[180:183], a[208:211], v[92:95], v[180:183]\n\t"
  "ds_read_b64_tr_b16 a[242:243], v221 offset:38400\n\t"
  "v_exp_f32 v148, v148\n\t"
  "v_cvt_pk_bf16_f32 v139, v142, v143\n\t"
  "v_exp_f32 v149, v149\n\t"
  "v_exp_f32 v150, v150\n\t"
  "v_cvt_pk_bf16_f32 v146, v148, v149\n\t"
  "v_exp_f32 v151, v151\n\t"
  "s_nop 0\n\t"
  "v_cvt_pk_bf16_f32 v147, v150, v151\n\t"
  "v_mfma_f32_16x16x32_bf16 v[188:191], a[208:211], v[108:111], v[188:191]\n\t"
  "s_waitcnt vmcnt(6) lgkmcnt(0)\n\t"
  "s_barrier\n\t"
  "v_mfma_f32_16x16x32_bf16 a[0:3], a[212:215], v[112:115], a[0:3]\n\t"
  "v_exp_f32 v152, v152\n\t"
  "v_mfma_f32_16x16x32_bf16 a[4:7], a[212:215], v[120:123], a[4:7]\n\t"
  "v_add_u32 v216, 65536, v216\n\t"
  "v_add_u32 v217, 65536, v217\n\t"
  "v_add_u32 v218, 65536, v218\n\t"
  "v_add_u32 v219, 65536, v219\n\t"
  "v_mfma_f32_16x16x32_bf16 a[8:11], a[212:215], v[128:131], a[8:11]\n\t"
  "v_mfma_f32_16x16x32_bf16 a[12:15], a[212:215], v[136:139], a[12:15]\n\t"
  "s_waitcnt vmcnt(4)\n\t"
  "buffer_load_dwordx4 v214, s[28:31], s52 offen offset:2048 nt lds\n\t"
  "v_mfma_f32_16x16x32_bf16 a[16:19], a[212:215], v[144:147], a[16:19]\n\t"
  "v_mfma_f32_16x16x32_bf16 a[20:23], a[216:219], v[112:115], a[20:23]\n\t"
  "v_exp_f32 v153, v153\n\t"
  "v_mfma_f32_16x16x32_bf16 a[24:27], a[216:219], v[120:123], a[24:27]\n\t"
  "v_exp_f32 v154, v154\n\t"
  "v_mfma_f32_16x16x32_bf16 a[28:31], a[216:219], v[128:131], a[28:31]\n\t"
  "v_cvt_pk_bf16_f32 v152, v152, v153\n\t"
  "v_mfma_f32_16x16x32_bf16 a[32:35], a[216:219], v[136:139], a[32:35]\n\t"
  "v_exp_f32 v155, v155\n\t"
  "v_mfma_f32_16x16x32_bf16 a[36:39], a[216:219], v[144:147], a[36:39]\n\t"
  "v_exp_f32 v160, v160\n\t"
  "v_mfma_f32_16x16x32_bf16 a[40:43], a[220:223], v[112:115], a[40:43]\n\t"
  "v_cvt_pk_bf16_f32 v153, v154, v155\n\t"
  "ds_read_b128 a[180:183], v216 offset:0\n\t"
  "v_mfma_f32_16x16x32_bf16 a[44:47], a[220:223], v[120:123], a[44:47]\n\t"
  "v_exp_f32 v161, v161\n\t"
  "v_mfma_f32_16x16x32_bf16 a[48:51], a[220:223], v[128:131], a[48:51]\n\t"
  "s_waitcnt vmcnt(4)\n\t"
  "buffer_load_dwordx4 v215, s[28:31], s52 offen offset:3072 nt lds\n\t"
  "v_mfma_f32_16x16x32_bf16 a[52:55], a[220:223], v[136:139], a[52:55]\n\t"
  "v_mfma_f32_16x16x32_bf16 a[56:59], a[220:223], v[144:147], a[56:59]\n\t"
  "v_exp_f32 v162, v162\n\t"
  "v_mfma_f32_16x16x32_bf16 a[60:63], a[224:227], v[112:115], a[60:63]\n\t"
  "ds_read_b128 a[184:187], v217 offset:0\n\t"
  "v_cvt_pk_bf16_f32 v160, v160, v161\n\t"
  "v_mfma_f32_16x16x32_bf16 a[64:67], a[224:227], v[120:123], a[64:67]\n\t"
  "v_exp_f32 v163, v163\n\t"
  "v_mfma_f32_16x16x32_bf16 a[68:71], a[224:227], v[128:131], a[68:71]\n\t"
  "ds_read_b128 a[188:191], v218 offset:0\n\t"
  "v_mfma_f32_16x16x32_bf16 a[72:75], a[224:227], v[136:139], a[72:75]\n\t"
  "v_exp_f32 v168, v168\n\t"
  "v_mfma_f32_16x16x32_bf16 a[76:79], a[224:227], v[144:147], a[76:79]\n\t"
  "ds_read_b128 a[192:195], v219 offset:0\n\t"
  "v_cvt_pk_bf16_f32 v161, v162, v163\n\t"
  "v_mfma_f32_16x16x32_bf16 a[80:83], a[228:231], v[112:115], a[80:83]\n\t"
  "v_exp_f32 v169, v169\n\t"
  "v_mfma_f32_16x16x32_bf16 a[84:87], a[228:231], v[120:123], a[84:87]\n\t"
  "s_waitcnt vmcnt(4)\n\t"
  "s_add_u32 m0, s51, 32768\n\t"
  "s_mov_b32 s52, 122880\n\t"
  "buffer_load_dwordx4 v212, s[28:31], s52 offen offset:0 nt lds\n\t"
  "v_mfma_f32_16x16x32_bf16 a[88:91], a[228:231], v[128:131], a[88:91]\n\t"
  "v_mfma_f32_16x16x32_bf16 a[92:95], a[228:231], v[136:139], a[92:95]\n\t"
  "v_mfma_f32_16x16x32_bf16 a[96:99], a[228:231], v[144:147], a[96:99]\n\t"
  "ds_read_b128 a[196:199], v216 offset:4096\n\t"
  "v_mfma_f32_16x16x32_bf16 a[100:103], a[232:235], v[112:115], a[100:103]\n\t"
  "v_exp_f32 v170, v170\n\t"
  "v_mfma_f32_16x16x32_bf16 a[104:107], a[232:235], v[120:123], a[104:107]\n\t"
  "v_cvt_pk_bf16_f32 v168, v168, v169\n\t"
  "ds_read_b128 a[200:203], v217 offset:4096\n\t"
  "v_mfma_f32_16x16x32_bf16 a[108:111], a[232:235], v[128:131], a[108:111]\n\t"
  "v_exp_f32 v171, v171\n\t"
  "v_mfma_f32_16x16x32_bf16 a[112:115], a[232:235], v[136:139], a[112:115]\n\t"
  "ds_read_b128 a[204:207], v218 offset:4096\n\t"
  "v_mfma_f32_16x16x32_bf16 a[116:119], a[232:235], v[144:147], a[116:119]\n\t"
  "v_exp_f32 v176, v176\n\t"
  "v_mfma_f32_16x16x32_bf16 a[120:123], a[236:239], v[112:115], a[120:123]\n\t"
  "v_cvt_pk_bf16_f32 v169, v170, v171\n\t"
  "ds_read_b128 a[208:211], v219 offset:4096\n\t"
  "v_mfma_f32_16x16x32_bf16 a[124:127], a[236:239], v[120:123], a[124:127]\n\t"
  "v_exp_f32 v177, v177\n\t"
  "v_mfma_f32_16x16x32_bf16 a[128:131], a[236:239], v[128:131], a[128:131]\n\t"
  "v_exp_f32 v178, v178\n\t"
  "v_mfma_f32_16x16x32_bf16 a[132:135], a[236:239], v[136:139], a[132:135]\n\t"
  "v_cvt_pk_bf16_f32 v176, v176, v177\n\t"
  "v_mfma_f32_16x16x32_bf16 a[136:139], a[236:239], v[144:147], a[136:139]\n\t"
  "v_exp_f32 v179, v179\n\t"
  "v_mfma_f32_16x16x32_bf16 a[140:143], a[240:243], v[112:115], a[140:143]\n\t"
  "s_waitcnt vmcnt(4)\n\t"
  "buffer_load_dwordx4 v213, s[28:31], s52 offen offset:1024 nt lds\n\t"
  "v_mfma_f32_16x16x32_bf16 a[144:147], a[240:243], v[120:123], a[144:147]\n\t"
  "v_mfma_f32_16x16x32_bf16 a[148:151], a[240:243], v[128:131], a[148:151]\n\t"
  "v_exp_f32 v184, v184\n\t"
  "v_mfma_f32_16x16x32_bf16 a[152:155], a[240:243], v[136:139], a[152:155]\n\t"
  "v_cvt_pk_bf16_f32 v177, v178, v179\n\t"
  "v_mfma_f32_16x16x32_bf16 a[156:159], a[240:243], v[144:147], a[156:159]\n\t"
  "v_exp_f32 v185, v185\n\t"
  "v_mfma_f32_16x16x32_bf16 a[160:163], a[244:247], v[112:115], a[160:163]\n\t"
  "v_exp_f32 v186, v186\n\t"
  "v_mfma_f32_16x16x32_bf16 a[164:167], a[244:247], v[120:123], a[164:167]\n\t"
  "v_cvt_pk_bf16_f32 v184, v184, v185\n\t"
  "v_mfma_f32_16x16x32_bf16 a[168:171], a[244:247], v[128:131], a[168:171]\n\t"
  "v_exp_f32 v187, v187\n\t"
  "v_mfma_f32_16x16x32_bf16 a[172:175], a[244:247], v[136:139], a[172:175]\n\t"
  "s_nop 0\n\t"
  "v_cvt_pk_bf16_f32 v185, v186, v187\n\t"
  "v_mfma_f32_16x16x32_bf16 a[176:179], a[244:247], v[144:147], a[176:179]\n\t"
  "s_waitcnt lgkmcnt(0)\n\t"
  "v_mfma_f32_16x16x32_bf16 v[112:115], a[180:183], v[32:35], v[192:195]\n\t"
  "v_exp_f32 v156, v156\n\t"
  "v_mfma_f32_16x16x32_bf16 v[120:123], a[180:183], v[48:51], v[196:199]\n\t"
  "v_add_u32 v220, 65536, v220\n\t"
  "v_add_u32 v221, 65536, v221\n\t"
  "v_mfma_f32_16x16x32_bf16 v[128:131], a[180:183], v[64:67], v[200:203]\n\t"
  "v_exp_f32 v157, v157\n\t"
  "v_mfma_f32_16x16x32_bf16 v[136:139], a[180:183], v[80:83], v[204:207]\n\t"
  "v_exp_f32 v158, v158\n\t"
  "s_waitcnt vmcnt(4)\n\t"
  "buffer_load_dwordx4 v214, s[28:31], s52 offen offset:2048 nt lds\n\t"
  "v_mfma_f32_16x16x32_bf16 v[144:147], a[180:183], v[96:99], v[208:211]\n\t"
  "v_mfma_f32_16x16x32_bf16 v[112:115], a[184:187], v[36:39], v[112:115]\n\t"
  "v_mfma_f32_16x16x32_bf16 v[120:123], a[184:187], v[52:55], v[120:123]\n\t"
  "ds_read_b64_tr_b16 a[212:213], v220 offset:0\n\t"
  "v_cvt_pk_bf16_f32 v154, v156, v157\n\t"
  "v_mfma_f32_16x16x32_bf16 v[128:131], a[184:187], v[68:71], v[128:131]\n\t"
  "ds_read_b64_tr_b16 a[214:215], v220 offset:4096\n\t"
  "v_mfma_f32_16x16x32_bf16 v[136:139], a[184:187], v[84:87], v[136:139]\n\t"
  "v_exp_f32 v159, v159\n\t"
  "v_mfma_f32_16x16x32_bf16 v[144:147], a[184:187], v[100:103], v[144:147]\n\t"
  "ds_read_b64_tr_b16 a[216:217], v221 offset:0\n\t"
  "v_mfma_f32_16x16x32_bf16 v[112:115], a[188:191], v[40:43], v[112:115]\n\t"
  "v_exp_f32 v164, v164\n\t"
  "v_mfma_f32_16x16x32_bf16 v[120:123], a[188:191], v[56:59], v[120:123]\n\t"
  "v_cvt_pk_bf16_f32 v155, v158, v159\n\t"
  "ds_read_b64_tr_b16 a[218:219], v221 offset:4096\n\t"
  "v_mfma_f32_16x16x32_bf16 v[128:131], a[188:191], v[72:75], v[128:131]\n\t"
  "v_exp_f32 v165, v165\n\t"
  "v_mfma_f32_16x16x32_bf16 v[136:139], a[188:191], v[88:91], v[136:139]\n\t"
  "ds_read_b64_tr_b16 a[220:221], v220 offset:512\n\t"
  "v_mfma_f32_16x16x32_bf16 v[144:147], a[188:191], v[104:107], v[144:147]\n\t"
  "v_exp_f32 v166, v166\n\t"
  "v_mfma_f32_16x16x32_bf16 v[112:115], a[192:195], v[44:47], v[112:115]\n\t"
  "ds_read_b64_tr_b16 a[222:223], v220 offset:4608\n\t"
  "v_cvt_pk_bf16_f32 v162, v164, v165\n\t"
  "s_waitcnt vmcnt(4)\n\t"
  "buffer_load_dwordx4 v215, s[28:31], s52 offen offset:3072 nt lds\n\t"
  "v_mfma_f32_16x16x32_bf16 v[120:123], a[192:195], v[60:63], v[120:123]\n\t"
  "v_mfma_f32_16x16x32_bf16 v[128:131], a[192:195], v[76:79], v[128:131]\n\t"
  "v_mfma_f32_16x16x32_bf16 v[136:139], a[192:195], v[92:95], v[136:139]\n\t"
  "v_exp_f32 v167, v167\n\t"
  "v_mfma_f32_16x16x32_bf16 v[144:147], a[192:195], v[108:111], v[144:147]\n\t"
  "ds_read_b64_tr_b16 a[224:225], v221 offset:512\n\t"
  "v_mfma_f32_16x16x32_bf16 v[116:119], a[196:199], v[32:35], v[192:195]\n\t"
  "v_exp_f32 v172, v172\n\t"
  "v_mfma_f32_16x16x32_bf16 v[124:127], a[196:199], v[48:51], v[196:199]\n\t"
  "ds_read_b64_tr_b16 a[226:227], v221 offset:4608\n\t"
  "v_cvt_pk_bf16_f32 v163, v166, v167\n\t"
  "v_mfma_f32_16x16x32_bf16 v[132:135], a[196:199], v[64:67], v[200:203]\n\t"
  "v_exp_f32 v173, v173\n\t"
  "v_mfma_f32_16x16x32_bf16 v[140:143], a[196:199], v[80:83], v[204:207]\n\t"
  "ds_read_b64_tr_b16 a[228:229], v220 offset:1024\n\t"
  "v_mfma_f32_16x16x32_bf16 v[148:151], a[196:199], v[96:99], v[208:211]\n\t"
  "v_exp_f32 v174, v174\n\t"
  "v_mfma_f32_16x16x32_bf16 v[116:119], a[200:203], v[36:39], v[116:119]\n\t"
  "ds_read_b64_tr_b16 a[230:231], v220 offset:5120\n\t"
  "v_cvt_pk_bf16_f32 v170, v172, v173\n\t"
  "v_mfma_f32_16x16x32_bf16 v[124:127], a[200:203], v[52:55], v[124:127]\n\t"
  "ds_read_b64_tr_b16 a[232:233], v221 offset:1024\n\t"
  "v_mfma_f32_16x16x32_bf16 v[132:135], a[200:203], v[68:71], v[132:135]\n\t"
  "v_exp_f32 v175, v175\n\t"
  "v_mfma_f32_16x16x32_bf16 v[140:143], a[200:203], v[84:87], v[140:143]\n\t"
  "v_exp_f32 v180, v180\n\t"
  "v_mfma_f32_16x16x32_bf16 v[148:151], a[200:203], v[100:103], v[148:151]\n\t"
  "ds_read_b64_tr_b16 a[234:235], v221 offset:5120\n\t"
  "s_waitcnt vmcnt(4)\n\t"
  "s_add_u32 m0, s51, 36864\n\t"
  "s_mov_b32 s52, 126976\n\t"
  "buffer_load_dwordx4 v212, s[28:31], s52 offen offset:0 nt lds\n\t"
  "v_mfma_f32_16x16x32_bf16 v[116:119], a[204:207], v[40:43], v[116:119]\n\t"
  "v_mfma_f32_16x16x32_bf16 v[124:127], a[204:207], v[56:59], v[124:127]\n\t"
  "v_mfma_f32_16x16x32_bf16 v[132:135], a[204:207], v[72:75], v[132:135]\n\t"
  "v_cvt_pk_bf16_f32 v171, v174, v175\n\t"
  "v_mfma_f32_16x16x32_bf16 v[140:143], a[204:207], v[88:91], v[140:143]\n\t"
  "ds_read_b64_tr_b16 a[236:237], v220 offset:1536\n\t"
  "v_mfma_f32_16x16x32_bf16 v[148:151], a[204:207], v[104:107], v[148:151]\n\t"
  "v_exp_f32 v181, v181\n\t"
  "v_mfma_f32_16x16x32_bf16 v[116:119], a[208:211], v[44:47], v[116:119]\n\t"
  "ds_read_b64_tr_b16 a[238:239], v220 offset:5632\n\t"
  "v_mfma_f32_16x16x32_bf16 v[124:127], a[208:211], v[60:63], v[124:127]\n\t"
  "v_exp_f32 v182, v182\n\t"
  "v_mfma_f32_16x16x32_bf16 v[132:135], a[208:211], v[76:79], v[132:135]\n\t"
  "ds_read_b64_tr_b16 a[240:241], v221 offset:1536\n\t"
  "v_cvt_pk_bf16_f32 v178, v180, v181\n\t"
  "v_mfma_f32_16x16x32_bf16 v[140:143], a[208:211], v[92:95], v[140:143]\n\t"
  "v_exp_f32 v183, v183\n\t"
  "ds_read_b64_tr_b16 a[242:243], v221 offset:5632\n\t"
  "v_exp_f32 v188, v188\n\t"
  "v_cvt_pk_bf16_f32 v179, v182, v183\n\t"
  "v_exp_f32 v189, v189\n\t"
  "v_exp_f32 v190, v190\n\t"
  "v_cvt_pk_bf16_f32 v186, v188, v189\n\t"
  "v_exp_f32 v191, v191\n\t"
  "s_nop 0\n\t"
  "v_cvt_pk_bf16_f32 v187, v190, v191\n\t"
  "v_mfma_f32_16x16x32_bf16 v[148:151], a[208:211], v[108:111], v[148:151]\n\t"
  "s_waitcnt vmcnt(5) lgkmcnt(0)\n\t"
  "s_barrier\n\t"
  "v_mfma_f32_16x16x32_bf16 a[0:3], a[212:215], v[152:155], a[0:3]\n\t"
  "v_exp_f32 v112, v112\n\t"
  "v_add_u32 v216, 0xfffe0000, v216\n\t"
  "v_add_u32 v217, 0xfffe0000, v217\n\t"
  "v_add_u32 v218, 0xfffe0000, v218\n\t"
  "v_add_u32 v219, 0xfffe0000, v219\n\t"
  "v_mfma_f32_16x16x32_bf16 a[4:7], a[212:215], v[160:163], a[4:7]\n\t"
  "v_mfma_f32_16x16x32_bf16 a[8:11], a[212:215], v[168:171], a[8:11]\n\t"
  "s_waitcnt vmcnt(4)\n\t"
  "buffer_load_dwordx4 v213, s[28:31], s52 offen offset:1024 nt lds\n\t"
  "v_mfma_f32_16x16x32_bf16 a[12:15], a[212:215], v[176:179], a[12:15]\n\t"
  "v_mfma_f32_16x16x32_bf16 a[16:19], a[212:215], v[184:187], a[16:19]\n\t"
  "v_mfma_f32_16x16x32_bf16 a[20:23], a[216:219], v[152:155], a[20:23]\n\t"
  "v_exp_f32 v113, v113\n\t"
  "v_mfma_f32_16x16x32_bf16 a[24:27], a[216:219], v[160:163], a[24:27]\n\t"
  "v_exp_f32 v114, v114\n\t"
  "v_mfma_f32_16x16x32_bf16 a[28:31], a[216:219], v[168:171], a[28:31]\n\t"
  "v_cvt_pk_bf16_f32 v112, v112, v113\n\t"
  "s_waitcnt vmcnt(4)\n\t"
  "buffer_load_dwordx4 v214, s[28:31], s52 offen offset:2048 nt lds\n\t"
  "v_mfma_f32_16x16x32_bf16 a[32:35], a[216:219], v[176:179], a[32:35]\n\t"
  "v_mfma_f32_16x16x32_bf16 a[36:39], a[216:219], v[184:187], a[36:39]\n\t"
  "v_exp_f32 v115, v115\n\t"
  "v_mfma_f32_16x16x32_bf16 a[40:43], a[220:223], v[152:155], a[40:43]\n\t"
  "v_exp_f32 v120, v120\n\t"
  "v_mfma_f32_16x16x32_bf16 a[44:47], a[220:223], v[160:163], a[44:47]\n\t"
  "v_cvt_pk_bf16_f32 v113, v114, v115\n\t"
  "ds_read_b128 a[180:183], v216 offset:0\n\t"
  "s_waitcnt vmcnt(4)\n\t"
  "buffer_load_dwordx4 v215, s[28:31], s52 offen offset:3072 nt lds\n\t"
  "v_mfma_f32_16x16x32_bf16 a[48:51], a[220:223], v[168:171], a[48:51]\n\t"
  "v_mfma_f32_16x16x32_bf16 a[52:55], a[220:223], v[176:179], a[52:55]\n\t"
  "v_mfma_f32_16x16x32_bf16 a[56:59], a[220:223], v[184:187], a[56:59]\n\t"
  "v_exp_f32 v121, v121\n\t"
  "v_mfma_f32_16x16x32_bf16 a[60:63], a[224:227], v[152:155], a[60:63]\n\t"
  "v_exp_f32 v122, v122\n\t"
  "v_mfma_f32_16x16x32_bf16 a[64:67], a[224:227], v[160:163], a[64:67]\n\t"
  "ds_read_b128 a[184:187], v217 offset:0\n\t"
  "v_cvt_pk_bf16_f32 v120, v120, v121\n\t"
  "s_add_u32 m0, s55, 65536\n\t"
  "s_add_u32 s53, s56, 0\n\t"
  "buffer_load_dwordx4 v16, s[20:23], s53 offen offset:0 lds\n\t"
  "v_mfma_f32_16x16x32_bf16 a[68:71], a[224:227], v[168:171], a[68:71]\n\t"
  "v_mfma_f32_16x16x32_bf16 a[72:75], a[224:227], v[176:179], a[72:75]\n\t"
  "v_mfma_f32_16x16x32_bf16 a[76:79], a[224:227], v[184:187], a[76:79]\n\t"
  "v_exp_f32 v123, v123\n\t"
  "v_mfma_f32_16x16x32_bf16 a[80:83], a[228:231], v[152:155], a[80:83]\n\t"
  "ds_read_b128 a[188:191], v218 offset:0\n\t"
  "v_mfma_f32_16x16x32_bf16 a[84:87], a[228:231], v[160:163], a[84:87]\n\t"
  "v_exp_f32 v128, v128\n\t"
  "v_mfma_f32_16x16x32_bf16 a[88:91], a[228:231], v[168:171], a[88:91]\n\t"
  "ds_read_b128 a[192:195], v219 offset:0\n\t"
  "v_cvt_pk_bf16_f32 v121, v122, v123\n\t"
  "v_mfma_f32_16x16x32_bf16 a[92:95], a[228:231], v[176:179], a[92:95]\n\t"
  "v_exp_f32 v129, v129\n\t"
  "v_mfma_f32_16x16x32_bf16 a[96:99], a[228:231], v[184:187], a[96:99]\n\t"
  "ds_read_b128 a[196:199], v216 offset:4096\n\t"
  "buffer_load_dwordx4 v17, s[20:23], s53 offen offset:1024 lds\n\t"
  "v_mfma_f32_16x16x32_bf16 a[100:103], a[232:235], v[152:155], a[100:103]\n\t"
  "v_mfma_f32_16x16x32_bf16 a[104:107], a[232:235], v[160:163], a[104:107]\n\t"
  "v_exp_f32 v130, v130\n\t"
  "v_mfma_f32_16x16x32_bf16 a[108:111], a[232:235], v[168:171], a[108:111]\n\t"
  "v_cvt_pk_bf16_f32 v128, v128, v129\n\t"
  "ds_read_b128 a[200:203], v217 offset:4096\n\t"
  "v_mfma_f32_16x16x32_bf16 a[112:115], a[232:235], v[176:179], a[112:115]\n\t"
  "v_exp_f32 v131, v131\n\t"
  "buffer_load_dwordx4 v18, s[20:23], s53 offen offset:2048 lds\n\t"
  "v_mfma_f32_16x16x32_bf16 a[116:119], a[232:235], v[184:187], a[116:119]\n\t"
  "v_mfma_f32_16x16x32_bf16 a[120:123], a[236:239], v[152:155], a[120:123]\n\t"
  "ds_read_b128 a[204:207], v218 offset:4096\n\t"
  "v_mfma_f32_16x16x32_bf16 a[124:127], a[236:239], v[160:163], a[124:127]\n\t"
  "v_exp_f32 v136, v136\n\t"
  "v_mfma_f32_16x16x32_bf16 a[128:131], a[236:239], v[168:171], a[128:131]\n\t"
  "v_cvt_pk_bf16_f32 v129, v130, v131\n\t"
  "s_add_u32 m0, s55, 77824\n\t"
  "s_add_u32 s53, s56, 40960\n\t"
  "buffer_load_dwordx4 v16, s[20:23], s53 offen offset:0 lds\n\t"
  "v_mfma_f32_16x16x32_bf16 a[132:135], a[236:239], v[176:179], a[132:135]\n\t"
  "v_mfma_f32_16x16x32_bf16 a[136:139], a[236:239], v[184:187], a[136:139]\n\t"
  "ds_read_b128 a[208:211], v219 offset:4096\n\t"
  "v_mfma_f32_16x16x32_bf16 a[140:143], a[240:243], v[152:155], a[140:143]\n\t"
  "v_exp_f32 v137, v137\n\t"
  "v_mfma_f32_16x16x32_bf16 a[144:147], a[240:243], v[160:163], a[144:147]\n\t"
  "v_exp_f32 v138, v138\n\t"
  "v_mfma_f32_16x16x32_bf16 a[148:151], a[240:243], v[168:171], a[148:151]\n\t"
  "v_cvt_pk_bf16_f32 v136, v136, v137\n\t"
  "v_mfma_f32_16x16x32_bf16 a[152:155], a[240:243], v[176:179], a[152:155]\n\t"
  "v_exp_f32 v139, v139\n\t"
  "buffer_load_dwordx4 v17, s[20:23], s53 offen offset:1024 lds\n\t"
  "v_mfma_f32_16x16x32_bf16 a[156:159], a[240:243], v[184:187], a[156:159]\n\t"
  "v_mfma_f32_16x16x32_bf16 a[160:163], a[244:247], v[152:155], a[160:163]\n\t"
  "v_exp_f32 v144, v144\n\t"
  "v_mfma_f32_16x16x32_bf16 a[164:167], a[244:247], v[160:163], a[164:167]\n\t"
  "v_cvt_pk_bf16_f32 v137, v138, v139\n\t"
  "buffer_load_dwordx4 v18, s[20:23], s53 offen offset:2048 lds\n\t"
  "v_mfma_f32_16x16x32_bf16 a[168:171], a[244:247], v[168:171], a[168:171]\n\t"
  "v_mfma_f32_16x16x32_bf16 a[172:175], a[244:247], v[176:179], a[172:175]\n\t"
  "v_exp_f32 v145, v145\n\t"
  "v_exp_f32 v146, v146\n\t"
  "global_load_dword %2, v9, s[70:71] sc1\n\t"
  "v_cvt_pk_bf16_f32 v144, v144, v145\n\t"
  "v_exp_f32 v147, v147\n\t"
  "s_nop 0\n\t"
  "v_cvt_pk_bf16_f32 v145, v146, v147\n\t"
  "v_mfma_f32_16x16x32_bf16 a[176:179], a[244:247], v[184:187], a[176:179]\n\t"
  "s_waitcnt lgkmcnt(0)\n\t"
  "v_mfma_f32_16x16x32_bf16 v[152:155], a[180:183], v[32:35], v[192:195]\n\t"
  "v_exp_f32 v116, v116\n\t"
  "v_mfma_f32_16x16x32_bf16 v[160:163], a[180:183], v[48:51], v[196:199]\n\t"
  "v_add_u32 v220, 0xfffe0000, v220\n\t"
  "v_add_u32 v221, 0xfffe0000, v221\n\t"
  "v_mfma_f32_16x16x32_bf16 v[168:171], a[180:183], v[64:67], v[200:203]\n\t"
  "v_exp_f32 v117, v117\n\t"
  "v_mfma_f32_16x16x32_bf16 v[176:179], a[180:183], v[80:83], v[204:207]\n\t"
  "v_exp_f32 v118, v118\n\t"
  "s_add_u32 m0, s55, 90112\n\t"
  "s_add_u32 s53, s56, 81920\n\t"
  "buffer_load_dwordx4 v16, s[20:23], s53 offen offset:0 lds\n\t"
  "v_mfma_f32_16x16x32_bf16 v[184:187], a[180:183], v[96:99], v[208:211]\n\t"
  "v_mfma_f32_16x16x32_bf16 v[152:155], a[184:187], v[36:39], v[152:155]\n\t"
  "v_mfma_f32_16x16x32_bf16 v[160:163], a[184:187], v[52:55], v[160:163]\n\t"
  "ds_read_b64_tr_b16 a[212:213], v220 offset:0\n\t"
  "v_cvt_pk_bf16_f32 v114, v116, v117\n\t"
  "v_mfma_f32_16x16x32_bf16 v[168:171], a[184:187], v[68:71], v[168:171]\n\t"
  "ds_read_b64_tr_b16 a[214:215], v220 offset:4096\n\t"
  "v_mfma_f32_16x16x32_bf16 v[176:179], a[184:187], v[84:87], v[176:179]\n\t"
  "v_exp_f32 v119, v119\n\t"
  "v_mfma_f32_16x16x32_bf16 v[184:187], a[184:187], v[100:103], v[184:187]\n\t"
  "ds_read_b64_tr_b16 a[216:217], v221 offset:0\n\t"
  "v_mfma_f32_16x16x32_bf16 v[152:155], a[188:191], v[40:43], v[152:155]\n\t"
  "v_exp_f32 v124, v124\n\t"
  "v_mfma_f32_16x16x32_bf16 v[160:163], a[188:191], v[56:59], v[160:163]\n\t"
  "v_cvt_pk_bf16_f32 v115, v118, v119\n\t"
  "ds_read_b64_tr_b16 a[218:219], v221 offset:4096\n\t"
  "v_mfma_f32_16x16x32_bf16 v[168:171], a[188:191], v[72:75], v[168:171]\n\t"
  "v_exp_f32 v125, v125\n\t"
  "v_mfma_f32_16x16x32_bf16 v[176:179], a[188:191], v[88:91], v[176:179]\n\t"
  "ds_read_b64_tr_b16 a[220:221], v220 offset:512\n\t"
  "buffer_load_dwordx4 v17, s[20:23], s53 offen offset:1024 lds\n\t"
  "v_mfma_f32_16x16x32_bf16 v[184:187], a[188:191], v[104:107], v[184:187]\n\t"
  "v_mfma_f32_16x16x32_bf16 v[152:155], a[192:195], v[44:47], v[152:155]\n\t"
  "v_exp_f32 v126, v126\n\t"
  "v_mfma_f32_16x16x32_bf16 v[160:163], a[192:195], v[60:63], v[160:163]\n\t"
  "ds_read_b64_tr_b16 a[222:223], v220 offset:4608\n\t"
  "v_cvt_pk_bf16_f32 v122, v124, v125\n\t"
  "v_mfma_f32_16x16x32_bf16 v[168:171], a[192:195], v[76:79], v[168:171]\n\t"
  "v_exp_f32 v127, v127\n\t"
  "v_mfma_f32_16x16x32_bf16 v[176:179], a[192:195], v[92:95], v[176:179]\n\t"
  "ds_read_b64_tr_b16 a[224:225], v221 offset:512\n\t"
  "v_mfma_f32_16x16x32_bf16 v[184:187], a[192:195], v[108:111], v[184:187]\n\t"
  "v_exp_f32 v132, v132\n\t"
  "buffer_load_dwordx4 v18, s[20:23], s53 offen offset:2048 lds\n\t"
  "v_mfma_f32_16x16x32_bf16 v[156:159], a[196:199], v[32:35], v[192:195]\n\t"
  "v_mfma_f32_16x16x32_bf16 v[164:167], a[196:199], v[48:51], v[196:199]\n\t"
  "ds_read_b64_tr_b16 a[226:227], v221 offset:4608\n\t"
  "v_cvt_pk_bf16_f32 v123, v126, v127\n\t"
  "v_mfma_f32_16x16x32_bf16 v[172:175], a[196:199], v[64:67], v[200:203]\n\t"
  "v_exp_f32 v133, v133\n\t"
  "v_mfma_f32_16x16x32_bf16 v[180:183], a[196:199], v[80:83], v[204:207]\n\t"
  "ds_read_b64_tr_b16 a[228:229], v220 offset:1024\n\t"
  "v_mfma_f32_16x16x32_bf16 v[188:191], a[196:199], v[96:99], v[208:211]\n\t"
  "v_exp_f32 v134, v134\n\t"
  "v_mfma_f32_16x16x32_bf16 v[156:159], a[200:203], v[36:39], v[156:159]\n\t"
  "ds_read_b64_tr_b16 a[230:231], v220 offset:5120\n\t"
  "v_cvt_pk_bf16_f32 v130, v132, v133\n\t"
  "s_add_u32 m0, s55, 102400\n\t"
  "s_add_u32 s53, s56, 122880\n\t"
  "buffer_load_dwordx4 v16, s[20:23], s53 offen offset:0 lds\n\t"
  "v_mfma_f32_16x16x32_bf16 v[164:167], a[200:203], v[52:55], v[164:167]\n\t"
  "v_mfma_f32_16x16x32_bf16 v[172:175], a[200:203], v[68:71], v[172:175]\n\t"
  "v_mfma_f32_16x16x32_bf16 v[180:183], a[200:203], v[84:87], v[180:183]\n\t"
  "ds_read_b64_tr_b16 a[232:233], v221 offset:1024\n\t"
  "v_mfma_f32_16x16x32_bf16 v[188:191], a[200:203], v[100:103], v[188:191]\n\t"
  "v_exp_f32 v135, v135\n\t"
  "v_mfma_f32_16x16x32_bf16 v[156:159], a[204:207], v[40:43], v[156:159]\n\t"
  "v_exp_f32 v140, v140\n\t"
  "v_mfma_f32_16x16x32_bf16 v[164:167], a[204:207], v[56:59], v[164:167]\n\t"
  "ds_read_b64_tr_b16 a[234:235], v221 offset:5120\n\t"
  "v_cvt_pk_bf16_f32 v131, v134, v135\n\t"
  "v_mfma_f32_16x16x32_bf16 v[172:175], a[204:207], v[72:75], v[172:175]\n\t"
  "ds_read_b64_tr_b16 a[236:237], v220 offset:1536\n\t"
  "v_mfma_f32_16x16x32_bf16 v[180:183], a[204:207], v[88:91], v[180:183]\n\t"
  "v_exp_f32 v141, v141\n\t"
  "v_mfma_f32_16x16x32_bf16 v[188:191], a[204:207], v[104:107], v[188:191]\n\t"
  "ds_read_b64_tr_b16 a[238:239], v220 offset:5632\n\t"
  "v_mfma_f32_16x16x32_bf16 v[156:159], a[208:211], v[44:47], v[156:159]\n\t"
  "v_exp_f32 v142, v142\n\t"
  "v_mfma_f32_16x16x32_bf16 v[164:167], a[208:211], v[60:63], v[164:167]\n\t"
  "ds_read_b64_tr_b16 a[240:241], v221 offset:1536\n\t"
  "buffer_load_dwordx4 v17, s[20:23], s53 offen offset:1024 lds\n\t"
  "v_mfma_f32_16x16x32_bf16 v[172:175], a[208:211], v[76:79], v[172:175]\n\t"
  "v_mfma_f32_16x16x32_bf16 v[180:183], a[208:211], v[92:95], v[180:183]\n\t"
  "v_cvt_pk_bf16_f32 v138, v140, v141\n\t"
  "v_exp_f32 v143, v143\n\t"
  "ds_read_b64_tr_b16 a[242:243], v221 offset:5632\n\t"
  "v_exp_f32 v148, v148\n\t"
  "v_cvt_pk_bf16_f32 v139, v142, v143\n\t"
  "v_exp_f32 v149, v149\n\t"
  "buffer_load_dwordx4 v18, s[20:23], s53 offen offset:2048 lds\n\t"
  "v_exp_f32 v150, v150\n\t"
  "v_cvt_pk_bf16_f32 v146, v148, v149\n\t"
  "v_exp_f32 v151, v151\n\t"
  "s_nop 0\n\t"
  "v_cvt_pk_bf16_f32 v147, v150, v151\n\t"
  "v_mfma_f32_16x16x32_bf16 v[188:191], a[208:211], v[108:111], v[188:191]\n\t"
  "s_waitcnt lgkmcnt(0)\n\t"
  "s_barrier\n\t"
  "v_mfma_f32_16x16x32_bf16 a[0:3], a[212:215], v[112:115], a[0:3]\n\t"
  "v_exp_f32 v152, v152\n\t"
  "v_mfma_f32_16x16x32_bf16 a[4:7], a[212:215], v[120:123], a[4:7]\n\t"
  "v_exp_f32 v153, v153\n\t"
  "v_mfma_f32_16x16x32_bf16 a[8:11], a[212:215], v[128:131], a[8:11]\n\t"
  "v_exp_f32 v154, v154\n\t"
  "v_cvt_pk_bf16_f32 v152, v152, v153\n\t"
  "v_mfma_f32_16x16x32_bf16 a[12:15], a[212:215], v[136:139], a[12:15]\n\t"
  "v_exp_f32 v155, v155\n\t"
  "v_mfma_f32_16x16x32_bf16 a[16:19], a[212:215], v[144:147], a[16:19]\n\t"
  "v_exp_f32 v160, v160\n\t"
  "v_mfma_f32_16x16x32_bf16 a[20:23], a[216:219], v[112:115], a[20:23]\n\t"
  "v_cvt_pk_bf16_f32 v153, v154, v155\n\t"
  "v_exp_f32 v161, v161\n\t"
  "v_mfma_f32_16x16x32_bf16 a[24:27], a[216:219], v[120:123], a[24:27]\n\t"
  "v_exp_f32 v162, v162\n\t"
  "v_mfma_f32_16x16x32_bf16 a[28:31], a[216:219], v[128:131], a[28:31]\n\t"
  "v_cvt_pk_bf16_f32 v160, v160, v161\n\t"
  "v_mfma_f32_16x16x32_bf16 a[32:35], a[216:219], v[136:139], a[32:35]\n\t"
  "v_exp_f32 v163, v163\n\t"
  "v_exp_f32 v168, v168\n\t"
  "v_mfma_f32_16x16x32_bf16 a[36:39], a[216:219], v[144:147], a[36:39]\n\t"
  "v_cvt_pk_bf16_f32 v161, v162, v163\n\t"
  "v_mfma_f32_16x16x32_bf16 a[40:43], a[220:223], v[112:115], a[40:43]\n\t"
  "v_exp_f32 v169, v169\n\t"
  "v_mfma_f32_16x16x32_bf16 a[44:47], a[220:223], v[120:123], a[44:47]\n\t"
  "v_exp_f32 v170, v170\n\t"
  "v_cvt_pk_bf16_f32 v168, v168, v169\n\t"
  "v_mfma_f32_16x16x32_bf16 a[48:51], a[220:223], v[128:131], a[48:51]\n\t"
  "v_exp_f32 v171, v171\n\t"
  "v_mfma_f32_16x16x32_bf16 a[52:55], a[220:223], v[136:139], a[52:55]\n\t"
  "v_exp_f32 v176, v176\n\t"
  "v_mfma_f32_16x16x32_bf16 a[56:59], a[220:223], v[144:147], a[56:59]\n\t"
  "v_cvt_pk_bf16_f32 v169, v170, v171\n\t"
  "v_exp_f32 v177, v177\n\t"
  "v_mfma_f32_16x16x32_bf16 a[60:63], a[224:227], v[112:115], a[60:63]\n\t"
  "v_exp_f32 v178, v178\n\t"
  "v_mfma_f32_16x16x32_bf16 a[64:67], a[224:227], v[120:123], a[64:67]\n\t"
  "v_cvt_pk_bf16_f32 v176, v176, v177\n\t"
  "v_mfma_f32_16x16x32_bf16 a[68:71], a[224:227], v[128:131], a[68:71]\n\t"
  "v_exp_f32 v179, v179\n\t"
  "v_exp_f32 v184, v184\n\t"
  "v_mfma_f32_16x16x32_bf16 a[72:75], a[224:227], v[136:139], a[72:75]\n\t"
  "v_cvt_pk_bf16_f32 v177, v178, v179\n\t"
  "v_mfma_f32_16x16x32_bf16 a[76:79], a[224:227], v[144:147], a[76:79]\n\t"
  "v_exp_f32 v185, v185\n\t"
  "v_mfma_f32_16x16x32_bf16 a[80:83], a[228:231], v[112:115], a[80:83]\n\t"
  "v_exp_f32 v186, v186\n\t"
  "v_cvt_pk_bf16_f32 v184, v184, v185\n\t"
  "v_mfma_f32_16x16x32_bf16 a[84:87], a[228:231], v[120:123], a[84:87]\n\t"
  "v_exp_f32 v187, v187\n\t"
  "v_mfma_f32_16x16x32_bf16 a[88:91], a[228:231], v[128:131], a[88:91]\n\t"
  "s_nop 0\n\t"
  "v_cvt_pk_bf16_f32 v185, v186, v187\n\t"
  "v_mfma_f32_16x16x32_bf16 a[92:95], a[228:231], v[136:139], a[92:95]\n\t"
  "v_exp_f32 v156, v156\n\t"
  "v_exp_f32 v157, v157\n\t"
  "v_mfma_f32_16x16x32_bf16 a[96:99], a[228:231], v[144:147], a[96:99]\n\t"
  "v_exp_f32 v158, v158\n\t"
  "v_mfma_f32_16x16x32_bf16 a[100:103], a[232:235], v[112:115], a[100:103]\n\t"
  "v_cvt_pk_bf16_f32 v154, v156, v157\n\t"
  "v_mfma_f32_16x16x32_bf16 a[104:107], a[232:235], v[120:123], a[104:107]\n\t"
  "v_exp_f32 v159, v159\n\t"
  "v_exp_f32 v164, v164\n\t"
  "v_mfma_f32_16x16x32_bf16 a[108:111], a[232:235], v[128:131], a[108:111]\n\t"
  "v_cvt_pk_bf16_f32 v155, v158, v159\n\t"
  "v_mfma_f32_16x16x32_bf16 a[112:115], a[232:235], v[136:139], a[112:115]\n\t"
  "v_exp_f32 v165, v165\n\t"
  "v_mfma_f32_16x16x32_bf16 a[116:119], a[232:235], v[144:147], a[116:119]\n\t"
  "v_exp_f32 v166, v166\n\t"
  "v_cvt_pk_bf16_f32 v162, v164, v165\n\t"
  "v_mfma_f32_16x16x32_bf16 a[120:123], a[236:239], v[112:115], a[120:123]\n\t"
  "v_exp_f32 v167, v167\n\t"
  "v_mfma_f32_16x16x32_bf16 a[124:127], a[236:239], v[120:123], a[124:127]\n\t"
  "v_exp_f32 v172, v172\n\t"
  "v_mfma_f32_16x16x32_bf16 a[128:131], a[236:239], v[128:131], a[128:131]\n\t"
  "v_cvt_pk_bf16_f32 v163, v166, v167\n\t"
  "v_exp_f32 v173, v173\n\t"
  "v_mfma_f32_16x16x32_bf16 a[132:135], a[236:239], v[136:139], a[132:135]\n\t"
  "v_exp_f32 v174, v174\n\t"
  "v_mfma_f32_16x16x32_bf16 a[136:139], a[236:239], v[144:147], a[136:139]\n\t"
  "v_cvt_pk_bf16_f32 v170, v172, v173\n\t"
  "v_mfma_f32_16x16x32_bf16 a[140:143], a[240:243], v[112:115], a[140:143]\n\t"
  "v_exp_f32 v175, v175\n\t"
  "v_exp_f32 v180, v180\n\t"
  "v_mfma_f32_16x16x32_bf16 a[144:147], a[240:243], v[120:123], a[144:147]\n\t"
  "v_cvt_pk_bf16_f32 v171, v174, v175\n\t"
  "v_mfma_f32_16x16x32_bf16 a[148:151], a[240:243], v[128:131], a[148:151]\n\t"
  "v_exp_f32 v181, v181\n\t"
  "v_mfma_f32_16x16x32_bf16 a[152:155], a[240:243], v[136:139], a[152:155]\n\t"
  "v_exp_f32 v182, v182\n\t"
  "v_cvt_pk_bf16_f32 v178, v180, v181\n\t"
  "v_mfma_f32_16x16x32_bf16 a[156:159], a[240:243], v[144:147], a[156:159]\n\t"
  "v_exp_f32 v183, v183\n\t"
  "v_mfma_f32_16x16x32_bf16 a[160:163], a[244:247], v[112:115], a[160:163]\n\t"
  "v_exp_f32 v188, v188\n\t"
  "v_mfma_f32_16x16x32_bf16 a[164:167], a[244:247], v[120:123], a[164:167]\n\t"
  "v_cvt_pk_bf16_f32 v179, v182, v183\n\t"
  "v_exp_f32 v189, v189\n\t"
  "v_mfma_f32_16x16x32_bf16 a[168:171], a[244:247], v[128:131], a[168:171]\n\t"
  "v_exp_f32 v190, v190\n\t"
  "v_mfma_f32_16x16x32_bf16 a[172:175], a[244:247], v[136:139], a[172:175]\n\t"
  "v_cvt_pk_bf16_f32 v186, v188, v189\n\t"
  "v_mfma_f32_16x16x32_bf16 a[176:179], a[244:247], v[144:147], a[176:179]\n\t"
  "v_exp_f32 v191, v191\n\t"
  "s_nop 0\n\t"
  "v_cvt_pk_bf16_f32 v187, v190, v191\n\t"
  "s_waitcnt vmcnt(13)\n\t"
  "s_barrier\n\t"
  "ds_read_b64_tr_b16 a[212:213], v220 offset:32768\n\t"
  "ds_read_b64_tr_b16 a[214:215], v220 offset:36864\n\t"
  "ds_read_b64_tr_b16 a[216:217], v221 offset:32768\n\t"
  "ds_read_b64_tr_b16 a[218:219], v221 offset:36864\n\t"
  "ds_read_b64_tr_b16 a[220:221], v220 offset:33280\n\t"
  "ds_read_b64_tr_b16 a[222:223], v220 offset:37376\n\t"
  "ds_read_b64_tr_b16 a[224:225], v221 offset:33280\n\t"
  "ds_read_b64_tr_b16 a[226:227], v221 offset:37376\n\t"
  "ds_read_b64_tr_b16 a[228:229], v220 offset:33792\n\t"
  "ds_read_b64_tr_b16 a[230:231], v220 offset:37888\n\t"
  "ds_read_b64_tr_b16 a[232:233], v221 offset:33792\n\t"
  "ds_read_b64_tr_b16 a[234:235], v221 offset:37888\n\t"
  "ds_read_b64_tr_b16 a[236:237], v220 offset:34304\n\t"
  "ds_read_b64_tr_b16 a[238:239], v220 offset:38400\n\t"
  "ds_read_b64_tr_b16 a[240:241], v221 offset:34304\n\t"
  "ds_read_b64_tr_b16 a[242:243], v221 offset:38400\n\t"
  "s_add_u32 m0, s55, 114688\n\t"
  "s_add_u32 s53, s56, 163840\n\t"
  "buffer_load_dwordx4 v16, s[20:23], s53 offen offset:0 lds\n\t"
  "buffer_load_dwordx4 v17, s[20:23], s53 offen offset:1024 lds\n\t"
  "buffer_load_dwordx4 v18, s[20:23], s53 offen offset:2048 lds\n\t"
  "s_add_u32 m0, s55, 126976\n\t"
  "s_add_u32 s53, s56, 204800\n\t"
  "buffer_load_dwordx4 v16, s[20:23], s53 offen offset:0 lds\n\t"
  "buffer_load_dwordx4 v17, s[20:23], s53 offen offset:1024 lds\n\t"
  "buffer_load_dwordx4 v18, s[20:23], s53 offen offset:2048 lds\n\t"
  "s_waitcnt lgkmcnt(0)\n\t"
  "s_barrier\n\t"
  "s_mov_b64 s[60:61], 0\n\t"
  "v_mov_b32 v27, 0x71800000\n\t"
  "v_mov_b32 v28, 0\n\t"
  "v_mov_b32 v29, 0\n\t"
  "v_mov_b32 v30, 0\n\t"
  "v_mov_b32 v31, 0\n\t"
  "v_mfma_f32_16x16x32_bf16 a[160:163], a[244:247], v[152:155], a[160:163]\n\t"
  "v_mfma_f32_16x16x32_bf16 a[0:3], a[212:215], v[152:155], a[0:3]\n\t"
  "v_mfma_f32_16x16x32_bf16 a[20:23], a[216:219], v[152:155], a[20:23]\n\t"
  "v_mfma_f32_16x16x32_bf16 a[40:43], a[220:223], v[152:155], a[40:43]\n\t"
  "v_mfma_f32_16x16x32_bf16 a[60:63], a[224:227], v[152:155], a[60:63]\n\t"
  "v_mfma_f32_16x16x32_bf16 a[80:83], a[228:231], v[152:155], a[80:83]\n\t"
  "v_mfma_f32_16x16x32_bf16 a[100:103], a[232:235], v[152:155], a[100:103]\n\t"
  "v_mfma_f32_16x16x32_bf16 a[120:123], a[236:239], v[152:155], a[120:123]\n\t"
  "v_mfma_f32_16x16x32_bf16 a[140:143], a[240:243], v[152:155], a[140:143]\n\t"
  "v_mfma_f32_16x16x32_bf16 a[164:167], a[244:247], v[160:163], a[164:167]\n\t"
  "v_mfma_f32_16x16x32_bf16 a[4:7], a[212:215], v[160:163], a[4:7]\n\t"
  "v_accvgpr_read_b32 v25, a160\n\t"
  "v_cmp_nlt_f32 vcc, v25, v27\n\t"
  "v_mfma_f32_16x16x32_bf16 a[24:27], a[216:219], v[160:163], a[24:27]\n\t"
  "v_rcp_f32 v25, v25\n\t"
  "s_or_b64 s[60:61], s[60:61], vcc\n\t"
  "v_mfma_f32_16x16x32_bf16 a[44:47], a[220:223], v[160:163], a[44:47]\n\t"
  "v_accvgpr_read_b32 v112, a0\n\t"
  "v_accvgpr_read_b32 v113, a1\n\t"
  "v_mfma_f32_16x16x32_bf16 a[64:67], a[224:227], v[160:163], a[64:67]\n\t"
  "v_accvgpr_read_b32 v114, a2\n\t"
  "v_accvgpr_read_b32 v115, a3\n\t"
  "v_mfma_f32_16x16x32_bf16 a[84:87], a[228:231], v[160:163], a[84:87]\n\t"
  "v_accvgpr_read_b32 v116, a20\n\t"
  "v_accvgpr_read_b32 v117, a21\n\t"
  "v_mfma_f32_16x16x32_bf16 a[104:107], a[232:235], v[160:163], a[104:107]\n\t"
  "v_accvgpr_read_b32 v118, a22\n\t"
  "v_accvgpr_read_b32 v119, a23\n\t"
  "v_mfma_f32_16x16x32_bf16 a[124:127], a[236:239], v[160:163], a[124:127]\n\t"
  "v_mul_f32 v112, v112, v25\n\t"
  "v_mul_f32 v113, v113, v25\n\t"
  "v_mfma_f32_16x16x32_bf16 a[144:147], a[240:243], v[160:163], a[144:147]\n\t"
  "v_mul_f32 v114, v114, v25\n\t"
  "v_mul_f32 v115, v115, v25\n\t"
  "v_mul_f32 v116, v116, v25\n\t"
  "v_mul_f32 v117, v117, v25\n\t"
  "v_mul_f32 v118, v118, v25\n\t"
  "v_mul_f32 v119, v119, v25\n\t"
  "v_cvt_pk_bf16_f32 v112, v112, v113\n\t"
  "v_cvt_pk_bf16_f32 v113, v114, v115\n\t"
  "v_cvt_pk_bf16_f32 v114, v116, v117\n\t"
  "v_cvt_pk_bf16_f32 v115, v118, v119\n\t"
  "s_mov_b32 s48, 0\n\t"
  "buffer_store_dwordx4 v[112:115], v222, s[36:39], s48 offen sc1\n\t"
  "v_mfma_f32_16x16x32_bf16 v[28:31], a[248:251], v[112:115], v[28:31]\n\t"
  "ds_write_b128 v23, v[112:115] offset:0\n\t"
  "v_accvgpr_read_b32 v120, a40\n\t"
  "v_accvgpr_read_b32 v121, a41\n\t"
  "v_accvgpr_read_b32 v122, a42\n\t"
  "v_accvgpr_read_b32 v123, a43\n\t"
  "v_accvgpr_read_b32 v124, a60\n\t"
  "v_accvgpr_read_b32 v125, a61\n\t"
  "v_accvgpr_read_b32 v126, a62\n\t"
  "v_accvgpr_read_b32 v127, a63\n\t"
  "v_mul_f32 v120, v120, v25\n\t"
  "v_mul_f32 v121, v121, v25\n\t"
  "v_mul_f32 v122, v122, v25\n\t"
  "v_mul_f32 v123, v123, v25\n\t"
  "v_mul_f32 v124, v124, v25\n\t"
  "v_mul_f32 v125, v125, v25\n\t"
  "v_mul_f32 v126, v126, v25\n\t"
  "v_mul_f32 v127, v127, v25\n\t"
  "v_cvt_pk_bf16_f32 v120, v120, v121\n\t"
  "v_cvt_pk_bf16_f32 v121, v122, v123\n\t"
  "v_cvt_pk_bf16_f32 v122, v124, v125\n\t"
  "v_cvt_pk_bf16_f32 v123, v126, v127\n\t"
  "s_mov_b32 s48, 5120\n\t"
  "buffer_store_dwordx4 v[120:123], v222, s[36:39], s48 offen sc1\n\t"
  "v_mfma_f32_16x16x32_bf16 v[28:31], a[248:251], v[120:123], v[28:31]\n\t"
  "ds_write_b128 v23, v[120:123] offset:5120\n\t"
  "v_accvgpr_read_b32 v112, a80\n\t"
  "v_accvgpr_read_b32 v113, a81\n\t"
  "v_accvgpr_read_b32 v114, a82\n\t"
  "v_accvgpr_read_b32 v115, a83\n\t"
  "v_accvgpr_read_b32 v116, a100\n\t"
  "v_accvgpr_read_b32 v117, a101\n\t"
  "v_accvgpr_read_b32 v118, a102\n\t"
  "v_accvgpr_read_b32 v119, a103\n\t"
  "v_mul_f32 v112, v112, v25\n\t"
  "v_mul_f32 v113, v113, v25\n\t"
  "v_mul_f32 v114, v114, v25\n\t"
  "v_mul_f32 v115, v115, v25\n\t"
  "v_mul_f32 v116, v116, v25\n\t"
  "v_mul_f32 v117, v117, v25\n\t"
  "v_mul_f32 v118, v118, v25\n\t"
  "v_mul_f32 v119, v119, v25\n\t"
  "v_cvt_pk_bf16_f32 v112, v112, v113\n\t"
  "v_cvt_pk_bf16_f32 v113, v114, v115\n\t"
  "v_cvt_pk_bf16_f32 v114, v116, v117\n\t"
  "v_cvt_pk_bf16_f32 v115, v118, v119\n\t"
  "s_mov_b32 s48, 10240\n\t"
  "buffer_store_dwordx4 v[112:115], v222, s[36:39], s48 offen sc1\n\t"
  "v_mfma_f32_16x16x32_bf16 v[28:31], a[248:251], v[112:115], v[28:31]\n\t"
  "ds_write_b128 v24, v[112:115] offset:0\n\t"
  "v_accvgpr_read_b32 v120, a120\n\t"
  "v_accvgpr_read_b32 v121, a121\n\t"
  "v_accvgpr_read_b32 v122, a122\n\t"
  "v_accvgpr_read_b32 v123, a123\n\t"
  "v_accvgpr_read_b32 v124, a140\n\t"
  "v_accvgpr_read_b32 v125, a141\n\t"
  "v_accvgpr_read_b32 v126, a142\n\t"
  "v_accvgpr_read_b32 v127, a143\n\t"
  "v_mul_f32 v120, v120, v25\n\t"
  "v_mul_f32 v121, v121, v25\n\t"
  "v_mul_f32 v122, v122, v25\n\t"
  "v_mul_f32 v123, v123, v25\n\t"
  "v_mul_f32 v124, v124, v25\n\t"
  "v_mul_f32 v125, v125, v25\n\t"
  "v_mul_f32 v126, v126, v25\n\t"
  "v_mul_f32 v127, v127, v25\n\t"
  "v_cvt_pk_bf16_f32 v120, v120, v121\n\t"
  "v_cvt_pk_bf16_f32 v121, v122, v123\n\t"
  "v_cvt_pk_bf16_f32 v122, v124, v125\n\t"
  "v_cvt_pk_bf16_f32 v123, v126, v127\n\t"
  "s_mov_b32 s48, 15360\n\t"
  "buffer_store_dwordx4 v[120:123], v222, s[36:39], s48 offen sc1\n\t"
  "v_mfma_f32_16x16x32_bf16 v[28:31], a[248:251], v[120:123], v[28:31]\n\t"
  "ds_write_b128 v24, v[120:123] offset:5120\n\t"
  "v_mfma_f32_16x16x32_bf16 a[168:171], a[244:247], v[168:171], a[168:171]\n\t"
  "v_mfma_f32_16x16x32_bf16 a[8:11], a[212:215], v[168:171], a[8:11]\n\t"
  "v_accvgpr_read_b32 v26, a164\n\t"
  "v_cmp_nlt_f32 vcc, v26, v27\n\t"
  "v_mfma_f32_16x16x32_bf16 a[28:31], a[216:219], v[168:171], a[28:31]\n\t"
  "v_rcp_f32 v26, v26\n\t"
  "s_or_b64 s[60:61], s[60:61], vcc\n\t"
  "v_mfma_f32_16x16x32_bf16 a[48:51], a[220:223], v[168:171], a[48:51]\n\t"
  "v_accvgpr_read_b32 v128, a4\n\t"
  "v_accvgpr_read_b32 v129, a5\n\t"
  "v_mfma_f32_16x16x32_bf16 a[68:71], a[224:227], v[168:171], a[68:71]\n\t"
  "v_accvgpr_read_b32 v130, a6\n\t"
  "v_accvgpr_read_b32 v131, a7\n\t"
  "v_mfma_f32_16x16x32_bf16 a[88:91], a[228:231], v[168:171], a[88:91]\n\t"
  "v_accvgpr_read_b32 v132, a24\n\t"
  "v_accvgpr_read_b32 v133, a25\n\t"
  "v_mfma_f32_16x16x32_bf16 a[108:111], a[232:235], v[168:171], a[108:111]\n\t"
  "v_accvgpr_read_b32 v134, a26\n\t"
  "v_accvgpr_read_b32 v135, a27\n\t"
  "v_mfma_f32_16x16x32_bf16 a[128:131], a[236:239], v[168:171], a[128:131]\n\t"
  "v_mul_f32 v128, v128, v26\n\t"
  "v_mul_f32 v129, v129, v26\n\t"
  "v_mfma_f32_16x16x32_bf16 a[148:151], a[240:243], v[168:171], a[148:151]\n\t"
  "v_mul_f32 v130, v130, v26\n\t"
  "v_mul_f32 v131, v131, v26\n\t"
  "v_mul_f32 v132, v132, v26\n\t"
  "v_mul_f32 v133, v133, v26\n\t"
  "v_mul_f32 v134, v134, v26\n\t"
  "v_mul_f32 v135, v135, v26\n\t"
  "v_cvt_pk_bf16_f32 v128, v128, v129\n\t"
  "v_cvt_pk_bf16_f32 v129, v130, v131\n\t"
  "v_cvt_pk_bf16_f32 v130, v132, v133\n\t"
  "v_cvt_pk_bf16_f32 v131, v134, v135\n\t"
  "s_mov_b32 s48, 1024\n\t"
  "buffer_store_dwordx4 v[128:131], v222, s[36:39], s48 offen sc1\n\t"
  "v_mfma_f32_16x16x32_bf16 v[28:31], a[248:251], v[128:131], v[28:31]\n\t"
  "ds_write_b128 v23, v[128:131] offset:1024\n\t"
  "v_accvgpr_read_b32 v136, a44\n\t"
  "v_accvgpr_read_b32 v137, a45\n\t"
  "v_accvgpr_read_b32 v138, a46\n\t"
  "v_accvgpr_read_b32 v139, a47\n\t"
  "v_accvgpr_read_b32 v140, a64\n\t"
  "v_accvgpr_read_b32 v141, a65\n\t"
  "v_accvgpr_read_b32 v142, a66\n\t"
  "v_accvgpr_read_b32 v143, a67\n\t"
  "v_mul_f32 v136, v136, v26\n\t"
  "v_mul_f32 v137, v137, v26\n\t"
  "v_mul_f32 v138, v138, v26\n\t"
  "v_mul_f32 v139, v139, v26\n\t"
  "v_mul_f32 v140, v140, v26\n\t"
  "v_mul_f32 v141, v141, v26\n\t"
  "v_mul_f32 v142, v142, v26\n\t"
  "v_mul_f32 v143, v143, v26\n\t"
  "v_cvt_pk_bf16_f32 v136, v136, v137\n\t"
  "v_cvt_pk_bf16_f32 v137, v138, v139\n\t"
  "v_cvt_pk_bf16_f32 v138, v140, v141\n\t"
  "v_cvt_pk_bf16_f32 v139, v142, v143\n\t"
  "s_mov_b32 s48, 6144\n\t"
  "buffer_store_dwordx4 v[136:139], v222, s[36:39], s48 offen sc1\n\t"
  "v_mfma_f32_16x16x32_bf16 v[28:31], a[248:251], v[136:139], v[28:31]\n\t"
  "ds_write_b128 v23, v[136:139] offset:6144\n\t"
  "v_accvgpr_read_b32 v128, a84\n\t"
  "v_accvgpr_read_b32 v129, a85\n\t"
  "v_accvgpr_read_b32 v130, a86\n\t"
  "v_accvgpr_read_b32 v131, a87\n\t"
  "v_accvgpr_read_b32 v132, a104\n\t"
  "v_accvgpr_read_b32 v133, a105\n\t"
  "v_accvgpr_read_b32 v134, a106\n\t"
  "v_accvgpr_read_b32 v135, a107\n\t"
  "v_mul_f32 v128, v128, v26\n\t"
  "v_mul_f32 v129, v129, v26\n\t"
  "v_mul_f32 v130, v130, v26\n\t"
  "v_mul_f32 v131, v131, v26\n\t"
  "v_mul_f32 v132, v132, v26\n\t"
  "v_mul_f32 v133, v133, v26\n\t"
  "v_mul_f32 v134, v134, v26\n\t"
  "v_mul_f32 v135, v135, v26\n\t"
  "v_cvt_pk_bf16_f32 v128, v128, v129\n\t"
  "v_cvt_pk_bf16_f32 v129, v130, v131\n\t"
  "v_cvt_pk_bf16_f32 v130, v132, v133\n\t"
  "v_cvt_pk_bf16_f32 v131, v134, v135\n\t"
  "s_mov_b32 s48, 11264\n\t"
  "buffer_store_dwordx4 v[128:131], v222, s[36:39], s48 offen sc1\n\t"
  "v_mfma_f32_16x16x32_bf16 v[28:31], a[248:251], v[128:131], v[28:31]\n\t"
  "ds_write_b128 v24, v[128:131] offset:1024\n\t"
  "v_accvgpr_read_b32 v136, a124\n\t"
  "v_accvgpr_read_b32 v137, a125\n\t"
  "v_accvgpr_read_b32 v138, a126\n\t"
  "v_accvgpr_read_b32 v139, a127\n\t"
  "v_accvgpr_read_b32 v140, a144\n\t"
  "v_accvgpr_read_b32 v141, a145\n\t"
  "v_accvgpr_read_b32 v142, a146\n\t"
  "v_accvgpr_read_b32 v143, a147\n\t"
  "v_mul_f32 v136, v136, v26\n\t"
  "v_mul_f32 v137, v137, v26\n\t"
  "v_mul_f32 v138, v138, v26\n\t"
  "v_mul_f32 v139, v139, v26\n\t"
  "v_mul_f32 v140, v140, v26\n\t"
  "v_mul_f32 v141, v141, v26\n\t"
  "v_mul_f32 v142, v142, v26\n\t"
  "v_mul_f32 v143, v143, v26\n\t"
  "v_cvt_pk_bf16_f32 v136, v136, v137\n\t"
  "v_cvt_pk_bf16_f32 v137, v138, v139\n\t"
  "v_cvt_pk_bf16_f32 v138, v140, v141\n\t"
  "v_cvt_pk_bf16_f32 v139, v142, v143\n\t"
  "s_mov_b32 s48, 16384\n\t"
  "buffer_store_dwordx4 v[136:139], v222, s[36:39], s48 offen sc1\n\t"
  "v_mfma_f32_16x16x32_bf16 v[28:31], a[248:251], v[136:139], v[28:31]\n\t"
  "ds_write_b128 v24, v[136:139] offset:6144\n\t"
  "v_mfma_f32_16x16x32_bf16 a[172:175], a[244:247], v[176:179], a[172:175]\n\t"
  "v_mfma_f32_16x16x32_bf16 a[12:15], a[212:215], v[176:179], a[12:15]\n\t"
  "v_accvgpr_read_b32 v25, a168\n\t"
  "v_cmp_nlt_f32 vcc, v25, v27\n\t"
  "v_mfma_f32_16x16x32_bf16 a[32:35], a[216:219], v[176:179], a[32:35]\n\t"
  "v_rcp_f32 v25, v25\n\t"
  "s_or_b64 s[60:61], s[60:61], vcc\n\t"
  "v_mfma_f32_16x16x32_bf16 a[52:55], a[220:223], v[176:179], a[52:55]\n\t"
  "v_accvgpr_read_b32 v112, a8\n\t"
  "v_accvgpr_read_b32 v113, a9\n\t"
  "v_mfma_f32_16x16x32_bf16 a[72:75], a[224:227], v[176:179], a[72:75]\n\t"
  "v_accvgpr_read_b32 v114, a10\n\t"
  "v_accvgpr_read_b32 v115, a11\n\t"
  "v_mfma_f32_16x16x32_bf16 a[92:95], a[228:231], v[176:179], a[92:95]\n\t"
  "v_accvgpr_read_b32 v116, a28\n\t"
  "v_accvgpr_read_b32 v117, a29\n\t"
  "v_mfma_f32_16x16x32_bf16 a[112:115], a[232:235], v[176:179], a[112:115]\n\t"
  "v_accvgpr_read_b32 v118, a30\n\t"
  "v_accvgpr_read_b32 v119, a31\n\t"
  "v_mfma_f32_16x16x32_bf16 a[132:135], a[236:239], v[176:179], a[132:135]\n\t"
  "v_mul_f32 v112, v112, v25\n\t"
  "v_mul_f32 v113, v113, v25\n\t"
  "v_mfma_f32_16x16x32_bf16 a[152:155], a[240:243], v[176:179], a[152:155]\n\t"
  "v_mul_f32 v114, v114, v25\n\t"
  "v_mul_f32 v115, v115, v25\n\t"
  "v_mul_f32 v116, v116, v25\n\t"
  "v_mul_f32 v117, v117, v25\n\t"
  "v_mul_f32 v118, v118, v25\n\t"
  "v_mul_f32 v119, v119, v25\n\t"
  "v_cvt_pk_bf16_f32 v112, v112, v113\n\t"
  "v_cvt_pk_bf16_f32 v113, v114, v115\n\t"
  "v_cvt_pk_bf16_f32 v114, v116, v117\n\t"
  "v_cvt_pk_bf16_f32 v115, v118, v119\n\t"
  "s_mov_b32 s48, 2048\n\t"
  "buffer_store_dwordx4 v[112:115], v222, s[36:39], s48 offen sc1\n\t"
  "v_mfma_f32_16x16x32_bf16 v[28:31], a[248:251], v[112:115], v[28:31]\n\t"
  "ds_write_b128 v23, v[112:115] offset:2048\n\t"
  "v_accvgpr_read_b32 v120, a48\n\t"
  "v_accvgpr_read_b32 v121, a49\n\t"
  "v_accvgpr_read_b32 v122, a50\n\t"
  "v_accvgpr_read_b32 v123, a51\n\t"
  "v_accvgpr_read_b32 v124, a68\n\t"
  "v_accvgpr_read_b32 v125, a69\n\t"
  "v_accvgpr_read_b32 v126, a70\n\t"
  "v_accvgpr_read_b32 v127, a71\n\t"
  "v_mul_f32 v120, v120, v25\n\t"
  "v_mul_f32 v121, v121, v25\n\t"
  "v_mul_f32 v122, v122, v25\n\t"
  "v_mul_f32 v123, v123, v25\n\t"
  "v_mul_f32 v124, v124, v25\n\t"
  "v_mul_f32 v125, v125, v25\n\t"
  "v_mul_f32 v126, v126, v25\n\t"
  "v_mul_f32 v127, v127, v25\n\t"
  "v_cvt_pk_bf16_f32 v120, v120, v121\n\t"
  "v_cvt_pk_bf16_f32 v121, v122, v123\n\t"
  "v_cvt_pk_bf16_f32 v122, v124, v125\n\t"
  "v_cvt_pk_bf16_f32 v123, v126, v127\n\t"
  "s_mov_b32 s48, 7168\n\t"
  "buffer_store_dwordx4 v[120:123], v222, s[36:39], s48 offen sc1\n\t"
  "v_mfma_f32_16x16x32_bf16 v[28:31], a[248:251], v[120:123], v[28:31]\n\t"
  "ds_write_b128 v23, v[120:123] offset:7168\n\t"
  "v_accvgpr_read_b32 v112, a88\n\t"
  "v_accvgpr_read_b32 v113, a89\n\t"
  "v_accvgpr_read_b32 v114, a90\n\t"
  "v_accvgpr_read_b32 v115, a91\n\t"
  "v_accvgpr_read_b32 v116, a108\n\t"
  "v_accvgpr_read_b32 v117, a109\n\t"
  "v_accvgpr_read_b32 v118, a110\n\t"
  "v_accvgpr_read_b32 v119, a111\n\t"
  "v_mul_f32 v112, v112, v25\n\t"
  "v_mul_f32 v113, v113, v25\n\t"
  "v_mul_f32 v114, v114, v25\n\t"
  "v_mul_f32 v115, v115, v25\n\t"
  "v_mul_f32 v116, v116, v25\n\t"
  "v_mul_f32 v117, v117, v25\n\t"
  "v_mul_f32 v118, v118, v25\n\t"
  "v_mul_f32 v119, v119, v25\n\t"
  "v_cvt_pk_bf16_f32 v112, v112, v113\n\t"
  "v_cvt_pk_bf16_f32 v113, v114, v115\n\t"
  "v_cvt_pk_bf16_f32 v114, v116, v117\n\t"
  "v_cvt_pk_bf16_f32 v115, v118, v119\n\t"
  "s_mov_b32 s48, 12288\n\t"
  "buffer_store_dwordx4 v[112:115], v222, s[36:39], s48 offen sc1\n\t"
  "v_mfma_f32_16x16x32_bf16 v[28:31], a[248:251], v[112:115], v[28:31]\n\t"
  "ds_write_b128 v24, v[112:115] offset:2048\n\t"
  "v_accvgpr_read_b32 v120, a128\n\t"
  "v_accvgpr_read_b32 v121, a129\n\t"
  "v_accvgpr_read_b32 v122, a130\n\t"
  "v_accvgpr_read_b32 v123, a131\n\t"
  "v_accvgpr_read_b32 v124, a148\n\t"
  "v_accvgpr_read_b32 v125, a149\n\t"
  "v_accvgpr_read_b32 v126, a150\n\t"
  "v_accvgpr_read_b32 v127, a151\n\t"
  "v_mul_f32 v120, v120, v25\n\t"
  "v_mul_f32 v121, v121, v25\n\t"
  "v_mul_f32 v122, v122, v25\n\t"
  "v_mul_f32 v123, v123, v25\n\t"
  "v_mul_f32 v124, v124, v25\n\t"
  "v_mul_f32 v125, v125, v25\n\t"
  "v_mul_f32 v126, v126, v25\n\t"
  "v_mul_f32 v127, v127, v25\n\t"
  "v_cvt_pk_bf16_f32 v120, v120, v121\n\t"
  "v_cvt_pk_bf16_f32 v121, v122, v123\n\t"
  "v_cvt_pk_bf16_f32 v122, v124, v125\n\t"
  "v_cvt_pk_bf16_f32 v123, v126, v127\n\t"
  "s_mov_b32 s48, 17408\n\t"
  "buffer_store_dwordx4 v[120:123], v222, s[36:39], s48 offen sc1\n\t"
  "v_mfma_f32_16x16x32_bf16 v[28:31], a[248:251], v[120:123], v[28:31]\n\t"
  "ds_write_b128 v24, v[120:123] offset:7168\n\t"
  "v_mfma_f32_16x16x32_bf16 a[176:179], a[244:247], v[184:187], a[176:179]\n\t"
  "v_mfma_f32_16x16x32_bf16 a[16:19], a[212:215], v[184:187], a[16:19]\n\t"
  "v_accvgpr_read_b32 v26, a172\n\t"
  "v_cmp_nlt_f32 vcc, v26, v27\n\t"
  "v_mfma_f32_16x16x32_bf16 a[36:39], a[216:219], v[184:187], a[36:39]\n\t"
  "v_rcp_f32 v26, v26\n\t"
  "s_or_b64 s[60:61], s[60:61], vcc\n\t"
  "v_mfma_f32_16x16x32_bf16 a[56:59], a[220:223], v[184:187], a[56:59]\n\t"
  "v_accvgpr_read_b32 v128, a12\n\t"
  "v_accvgpr_read_b32 v129, a13\n\t"
  "v_mfma_f32_16x16x32_bf16 a[76:79], a[224:227], v[184:187], a[76:79]\n\t"
  "v_accvgpr_read_b32 v130, a14\n\t"
  "v_accvgpr_read_b32 v131, a15\n\t"
  "v_mfma_f32_16x16x32_bf16 a[96:99], a[228:231], v[184:187], a[96:99]\n\t"
  "v_accvgpr_read_b32 v132, a32\n\t"
  "v_accvgpr_read_b32 v133, a33\n\t"
  "v_mfma_f32_16x16x32_bf16 a[116:119], a[232:235], v[184:187], a[116:119]\n\t"
  "v_accvgpr_read_b32 v134, a34\n\t"
  "v_accvgpr_read_b32 v135, a35\n\t"
  "v_mfma_f32_16x16x32_bf16 a[136:139], a[236:239], v[184:187], a[136:139]\n\t"
  "v_mul_f32 v128, v128, v26\n\t"
  "v_mul_f32 v129, v129, v26\n\t"
  "v_mfma_f32_16x16x32_bf16 a[156:159], a[240:243], v[184:187], a[156:159]\n\t"
  "v_mul_f32 v130, v130, v26\n\t"
  "v_mul_f32 v131, v131, v26\n\t"
  "v_mul_f32 v132, v132, v26\n\t"
  "v_mul_f32 v133, v133, v26\n\t"
  "v_mul_f32 v134, v134, v26\n\t"
  "v_mul_f32 v135, v135, v26\n\t"
  "v_cvt_pk_bf16_f32 v128, v128, v129\n\t"
  "v_cvt_pk_bf16_f32 v129, v130, v131\n\t"
  "v_cvt_pk_bf16_f32 v130, v132, v133\n\t"
  "v_cvt_pk_bf16_f32 v131, v134, v135\n\t"
  "s_mov_b32 s48, 3072\n\t"
  "buffer_store_dwordx4 v[128:131], v222, s[36:39], s48 offen sc1\n\t"
  "v_mfma_f32_16x16x32_bf16 v[28:31], a[248:251], v[128:131], v[28:31]\n\t"
  "ds_write_b128 v23, v[128:131] offset:3072\n\t"
  "v_accvgpr_read_b32 v136, a52\n\t"
  "v_accvgpr_read_b32 v137, a53\n\t"
  "v_accvgpr_read_b32 v138, a54\n\t"
  "v_accvgpr_read_b32 v139, a55\n\t"
  "v_accvgpr_read_b32 v140, a72\n\t"
  "v_accvgpr_read_b32 v141, a73\n\t"
  "v_accvgpr_read_b32 v142, a74\n\t"
  "v_accvgpr_read_b32 v143, a75\n\t"
  "v_mul_f32 v136, v136, v26\n\t"
  "v_mul_f32 v137, v137, v26\n\t"
  "v_mul_f32 v138, v138, v26\n\t"
  "v_mul_f32 v139, v139, v26\n\t"
  "v_mul_f32 v140, v140, v26\n\t"
  "v_mul_f32 v141, v141, v26\n\t"
  "v_mul_f32 v142, v142, v26\n\t"
  "v_mul_f32 v143, v143, v26\n\t"
  "v_cvt_pk_bf16_f32 v136, v136, v137\n\t"
  "v_cvt_pk_bf16_f32 v137, v138, v139\n\t"
  "v_cvt_pk_bf16_f32 v138, v140, v141\n\t"
  "v_cvt_pk_bf16_f32 v139, v142, v143\n\t"
  "s_mov_b32 s48, 8192\n\t"
  "buffer_store_dwordx4 v[136:139], v222, s[36:39], s48 offen sc1\n\t"
  "v_mfma_f32_16x16x32_bf16 v[28:31], a[248:251], v[136:139], v[28:31]\n\t"
  "ds_write_b128 v23, v[136:139] offset:8192\n\t"
  "v_accvgpr_read_b32 v128, a92\n\t"
  "v_accvgpr_read_b32 v129, a93\n\t"
  "v_accvgpr_read_b32 v130, a94\n\t"
  "v_accvgpr_read_b32 v131, a95\n\t"
  "v_accvgpr_read_b32 v132, a112\n\t"
  "v_accvgpr_read_b32 v133, a113\n\t"
  "v_accvgpr_read_b32 v134, a114\n\t"
  "v_accvgpr_read_b32 v135, a115\n\t"
  "v_mul_f32 v128, v128, v26\n\t"
  "v_mul_f32 v129, v129, v26\n\t"
  "v_mul_f32 v130, v130, v26\n\t"
  "v_mul_f32 v131, v131, v26\n\t"
  "v_mul_f32 v132, v132, v26\n\t"
  "v_mul_f32 v133, v133, v26\n\t"
  "v_mul_f32 v134, v134, v26\n\t"
  "v_mul_f32 v135, v135, v26\n\t"
  "v_cvt_pk_bf16_f32 v128, v128, v129\n\t"
  "v_cvt_pk_bf16_f32 v129, v130, v131\n\t"
  "v_cvt_pk_bf16_f32 v130, v132, v133\n\t"
  "v_cvt_pk_bf16_f32 v131, v134, v135\n\t"
  "s_mov_b32 s48, 13312\n\t"
  "buffer_store_dwordx4 v[128:131], v222, s[36:39], s48 offen sc1\n\t"
  "v_mfma_f32_16x16x32_bf16 v[28:31], a[248:251], v[128:131], v[28:31]\n\t"
  "ds_write_b128 v24, v[128:131] offset:3072\n\t"
  "v_accvgpr_read_b32 v136, a132\n\t"
  "v_accvgpr_read_b32 v137, a133\n\t"
  "v_accvgpr_read_b32 v138, a134\n\t"
  "v_accvgpr_read_b32 v139, a135\n\t"
  "v_accvgpr_read_b32 v140, a152\n\t"
  "v_accvgpr_read_b32 v141, a153\n\t"
  "v_accvgpr_read_b32 v142, a154\n\t"
  "v_accvgpr_read_b32 v143, a155\n\t"
  "v_mul_f32 v136, v136, v26\n\t"
  "v_mul_f32 v137, v137, v26\n\t"
  "v_mul_f32 v138, v138, v26\n\t"
  "v_mul_f32 v139, v139, v26\n\t"
  "v_mul_f32 v140, v140, v26\n\t"
  "v_mul_f32 v141, v141, v26\n\t"
  "v_mul_f32 v142, v142, v26\n\t"
  "v_mul_f32 v143, v143, v26\n\t"
  "v_cvt_pk_bf16_f32 v136, v136, v137\n\t"
  "v_cvt_pk_bf16_f32 v137, v138, v139\n\t"
  "v_cvt_pk_bf16_f32 v138, v140, v141\n\t"
  "v_cvt_pk_bf16_f32 v139, v142, v143\n\t"
  "s_mov_b32 s48, 18432\n\t"
  "buffer_store_dwordx4 v[136:139], v222, s[36:39], s48 offen sc1\n\t"
  "v_mfma_f32_16x16x32_bf16 v[28:31], a[248:251], v[136:139], v[28:31]\n\t"
  "ds_write_b128 v24, v[136:139] offset:8192\n\t"
  "s_nop 7\n\t"
  "v_accvgpr_read_b32 v25, a176\n\t"
  "v_cmp_nlt_f32 vcc, v25, v27\n\t"
  "v_rcp_f32 v25, v25\n\t"
  "s_or_b64 s[60:61], s[60:61], vcc\n\t"
  "v_accvgpr_read_b32 v112, a16\n\t"
  "v_accvgpr_read_b32 v113, a17\n\t"
  "v_accvgpr_read_b32 v114, a18\n\t"
  "v_accvgpr_read_b32 v115, a19\n\t"
  "v_accvgpr_read_b32 v116, a36\n\t"
  "v_accvgpr_read_b32 v117, a37\n\t"
  "v_accvgpr_read_b32 v118, a38\n\t"
  "v_accvgpr_read_b32 v119, a39\n\t"
  "v_mul_f32 v112, v112, v25\n\t"
  "v_mul_f32 v113, v113, v25\n\t"
  "v_mul_f32 v114, v114, v25\n\t"
  "v_mul_f32 v115, v115, v25\n\t"
  "v_mul_f32 v116, v116, v25\n\t"
  "v_mul_f32 v117, v117, v25\n\t"
  "v_mul_f32 v118, v118, v25\n\t"
  "v_mul_f32 v119, v119, v25\n\t"
  "v_cvt_pk_bf16_f32 v112, v112, v113\n\t"
  "v_cvt_pk_bf16_f32 v113, v114, v115\n\t"
  "v_cvt_pk_bf16_f32 v114, v116, v117\n\t"
  "v_cvt_pk_bf16_f32 v115, v118, v119\n\t"
  "s_mov_b32 s48, 4096\n\t"
  "buffer_store_dwordx4 v[112:115], v222, s[36:39], s48 offen sc1\n\t"
  "v_mfma_f32_16x16x32_bf16 v[28:31], a[248:251], v[112:115], v[28:31]\n\t"
  "ds_write_b128 v23, v[112:115] offset:4096\n\t"
  "v_accvgpr_read_b32 v120, a56\n\t"
  "v_accvgpr_read_b32 v121, a57\n\t"
  "v_accvgpr_read_b32 v122, a58\n\t"
  "v_accvgpr_read_b32 v123, a59\n\t"
  "v_accvgpr_read_b32 v124, a76\n\t"
  "v_accvgpr_read_b32 v125, a77\n\t"
  "v_accvgpr_read_b32 v126, a78\n\t"
  "v_accvgpr_read_b32 v127, a79\n\t"
  "v_mul_f32 v120, v120, v25\n\t"
  "v_mul_f32 v121, v121, v25\n\t"
  "v_mul_f32 v122, v122, v25\n\t"
  "v_mul_f32 v123, v123, v25\n\t"
  "v_mul_f32 v124, v124, v25\n\t"
  "v_mul_f32 v125, v125, v25\n\t"
  "v_mul_f32 v126, v126, v25\n\t"
  "v_mul_f32 v127, v127, v25\n\t"
  "v_cvt_pk_bf16_f32 v120, v120, v121\n\t"
  "v_cvt_pk_bf16_f32 v121, v122, v123\n\t"
  "v_cvt_pk_bf16_f32 v122, v124, v125\n\t"
  "v_cvt_pk_bf16_f32 v123, v126, v127\n\t"
  "s_mov_b32 s48, 9216\n\t"
  "buffer_store_dwordx4 v[120:123], v222, s[36:39], s48 offen sc1\n\t"
  "v_mfma_f32_16x16x32_bf16 v[28:31], a[248:251], v[120:123], v[28:31]\n\t"
  "ds_write_b128 v23, v[120:123] offset:9216\n\t"
  "v_accvgpr_read_b32 v112, a96\n\t"
  "v_accvgpr_read_b32 v113, a97\n\t"
  "v_accvgpr_read_b32 v114, a98\n\t"
  "v_accvgpr_read_b32 v115, a99\n\t"
  "v_accvgpr_read_b32 v116, a116\n\t"
  "v_accvgpr_read_b32 v117, a117\n\t"
  "v_accvgpr_read_b32 v118, a118\n\t"
  "v_accvgpr_read_b32 v119, a119\n\t"
  "v_mul_f32 v112, v112, v25\n\t"
  "v_mul_f32 v113, v113, v25\n\t"
  "v_mul_f32 v114, v114, v25\n\t"
  "v_mul_f32 v115, v115, v25\n\t"
  "v_mul_f32 v116, v116, v25\n\t"
  "v_mul_f32 v117, v117, v25\n\t"
  "v_mul_f32 v118, v118, v25\n\t"
  "v_mul_f32 v119, v119, v25\n\t"
  "v_cvt_pk_bf16_f32 v112, v112, v113\n\t"
  "v_cvt_pk_bf16_f32 v113, v114, v115\n\t"
  "v_cvt_pk_bf16_f32 v114, v116, v117\n\t"
  "v_cvt_pk_bf16_f32 v115, v118, v119\n\t"
  "s_mov_b32 s48, 14336\n\t"
  "buffer_store_dwordx4 v[112:115], v222, s[36:39], s48 offen sc1\n\t"
  "v_mfma_f32_16x16x32_bf16 v[28:31], a[248:251], v[112:115], v[28:31]\n\t"
  "ds_write_b128 v24, v[112:115] offset:4096\n\t"
  "v_accvgpr_read_b32 v120, a136\n\t"
  "v_accvgpr_read_b32 v121, a137\n\t"
  "v_accvgpr_read_b32 v122, a138\n\t"
  "v_accvgpr_read_b32 v123, a139\n\t"
  "v_accvgpr_read_b32 v124, a156\n\t"
  "v_accvgpr_read_b32 v125, a157\n\t"
  "v_accvgpr_read_b32 v126, a158\n\t"
  "v_accvgpr_read_b32 v127, a159\n\t"
  "v_mul_f32 v120, v120, v25\n\t"
  "v_mul_f32 v121, v121, v25\n\t"
  "v_mul_f32 v122, v122, v25\n\t"
  "v_mul_f32 v123, v123, v25\n\t"
  "v_mul_f32 v124, v124, v25\n\t"
  "v_mul_f32 v125, v125, v25\n\t"
  "v_mul_f32 v126, v126, v25\n\t"
  "v_mul_f32 v127, v127, v25\n\t"
  "v_cvt_pk_bf16_f32 v120, v120, v121\n\t"
  "v_cvt_pk_bf16_f32 v121, v122, v123\n\t"
  "v_cvt_pk_bf16_f32 v122, v124, v125\n\t"
  "v_cvt_pk_bf16_f32 v123, v126, v127\n\t"
  "s_mov_b32 s48, 19456\n\t"
  "buffer_store_dwordx4 v[120:123], v222, s[36:39], s48 offen sc1\n\t"
  "v_mfma_f32_16x16x32_bf16 v[28:31], a[248:251], v[120:123], v[28:31]\n\t"
  "ds_write_b128 v24, v[120:123] offset:9216\n\t"
  "s_nop 7\n\t"
  "v_cmp_u_f32 vcc, v28, v28\n\t"
  "s_nop 0\n\t"
  "s_or_b64 s[60:61], s[60:61], vcc\n\t"
  "s_or_b32 %0, s60, s61\n\t"
  "s_waitcnt vmcnt(32)\n\t"
  : "=&s"(flag), "=&s"(nonce), "+v"(tid), "=&s"(ob0), "=&s"(ob1), "=&s"(wb0), "=&s"(wb1), "=&s"(cb0), "=&s"(cb1), "=&s"(pb0), "=&s"(pb1), "=&s"(syn), "=&s"(w)
  : "s"(ka), "s"(blockIdx.x)
  : "v1","v2","v3","v4","v5","v6","v7","v8","v9","v10","v11","v12","v13","v14","v15","v16","v17","v18","v19","v20","v21","v22","v23","v24","v25","v26","v27","v28","v29","v30","v31","v32","v33","v34","v35","v36","v37","v38","v39","v40","v41","v42","v43","v44","v45","v46","v47","v48","v49","v50","v51","v52","v53","v54","v55","v56","v57","v58","v59","v60","v61","v62","v63","v64","v65","v66","v67","v68","v69","v70","v71","v72","v73","v74","v75","v76","v77","v78","v79","v80","v81","v82","v83","v84","v85","v86","v87","v88","v89","v90","v91","v92","v93","v94","v95","v96","v97","v98","v99","v100","v101","v102","v103","v104","v105","v106","v107","v108","v109","v110","v111","v112","v113","v114","v115","v116","v117","v118","v119","v120","v121","v122","v123","v124","v125","v126","v127","v128","v129","v130","v131","v132","v133","v134","v135","v136","v137","v138","v139","v140","v141","v142","v143","v144","v145","v146","v147","v148","v149","v150","v151","v152","v153","v154","v155","v156","v157","v158","v159","v160","v161","v162","v163","v164","v165","v166","v167","v168","v169","v170","v171","v172","v173","v174","v175","v176","v177","v178","v179","v180","v181","v182","v183","v184","v185","v186","v187","v188","v189","v190","v191","v192","v193","v194","v195","v196","v197","v198","v199","v200","v201","v202","v203","v204","v205","v206","v207","v208","v209","v210","v211","v212","v213","v214","v215","v216","v217","v218","v219","v220","v221","v222","v223","v224","v225","v226","v227","v228","v229","v230","v231","v232","v233","v234","v235","v236","v237","v238","v239","v240","v241","v242","v243","v244","v245","v246","v247","v248","v249","v250","v251","v252","v253","v254","v255","a0","a1","a2","a3","a4","a5","a6","a7","a8","a9","a10","a11","a12","a13","a14","a15","a16","a17","a18","a19","a20","a21","a22","a23","a24","a25","a26","a27","a28","a29","a30","a31","a32","a33","a34","a35","a36","a37","a38","a39","a40","a41","a42","a43","a44","a45","a46","a47","a48","a49","a50","a51","a52","a53","a54","a55","a56","a57","a58","a59","a60","a61","a62","a63","a64","a65","a66","a67","a68","a69","a70","a71","a72","a73","a74","a75","a76","a77","a78","a79","a80","a81","a82","a83","a84","a85","a86","a87","a88","a89","a90","a91","a92","a93","a94","a95","a96","a97","a98","a99","a100","a101","a102","a103","a104","a105","a106","a107","a108","a109","a110","a111","a112","a113","a114","a115","a116","a117","a118","a119","a120","a121","a122","a123","a124","a125","a126","a127","a128","a129","a130","a131","a132","a133","a134","a135","a136","a137","a138","a139","a140","a141","a142","a143","a144","a145","a146","a147","a148","a149","a150","a151","a152","a153","a154","a155","a156","a157","a158","a159","a160","a161","a162","a163","a164","a165","a166","a167","a168","a169","a170","a171","a172","a173","a174","a175","a176","a177","a178","a179","a180","a181","a182","a183","a184","a185","a186","a187","a188","a189","a190","a191","a192","a193","a194","a195","a196","a197","a198","a199","a200","a201","a202","a203","a204","a205","a206","a207","a208","a209","a210","a211","a212","a213","a214","a215","a216","a217","a218","a219","a220","a221","a222","a223","a224","a225","a226","a227","a228","a229","a230","a231","a232","a233","a234","a235","a236","a237","a238","a239","a240","a241","a242","a243","a244","a245","a246","a247","a248","a249","a250","a251","a252","a253","a254","a255","s20","s21","s22","s23","s24","s25","s26","s27","s28","s29","s30","s31","s36","s37","s38","s39","s40","s41","s42","s43","s44","s45","s46","s47","s48","s49","s50","s51","s52","s53","s54","s55","s56","s57","s58","s59","s60","s61","s62","s63","s64","s65","s66","s67","s68","s69","s70","s71","s72","s73","s74","s75","s76","s77","s78","s79","vcc","scc","memory");
  asm volatile("" : "+s"(ka));
  { int bid2=blockIdx.x; asm volatile("" : "+s"(bid2));
  const int bid=bid2, xcd=bid&7, idx=bid>>3, br=xcd>>2, kq=xcd&3, nq=idx>>3, hp=kq*4+nq, b=idx&7;
  const int lane=__builtin_amdgcn_mbcnt_hi(~0u,__builtin_amdgcn_mbcnt_lo(~0u,0u)), hh=w>>1, qh=w&1, h=2*hp+hh;
  if(__builtin_expect(flag!=0,0)){
    char* ws = (char*)ka[10];
    const u16* x = ka[br*5]; const u16* kc = ka[br*5+1]; const u16* vc = ka[br*5+2]; const u16* wq = ka[br*5+3];
    const size_t ob = (size_t)ws + WS_O + ((size_t)(br*16+2*b+qh)*128 + h*4)*5120;
    asm volatile("s_waitcnt vmcnt(0) lgkmcnt(0)":::"memory");
    float* ps=(float*)(lds+(w<2? 61440+w*2048 : 159744+(w-2)*2048));
    const u16* Kh=kc+(size_t)(b*32+h)*65536; const u16* Vh=vc+(size_t)(b*32+h)*65536; __attribute__((address_space(1))) u16* Oo=(__attribute__((address_space(1))) u16*)ob;
    for(int rr=0;rr<80;++rr){
      const u16* xr=x+(size_t)(b*160+qh*80+rr)*640; const u16* wc=wq+h*128+lane;
      float a0=0.f,a1=0.f;
      for(int c=0;c<640;++c){ float xv=bf2f(xr[c]); a0=fmaf(xv,bf2f(wc[(size_t)c*4096]),a0); a1=fmaf(xv,bf2f(wc[(size_t)c*4096+64]),a1); }
      a0=bf2f(f2bf(a0*0.12751743f)); a1=bf2f(f2bf(a1*0.12751743f));
      float m=-__builtin_inff();
      for(int i=0;i<8;++i){ const u16* kr=Kh+(size_t)(i*64+lane)*128; float s=0.f;
        for(int d=0;d<64;++d){ s=fmaf(__shfl(a0,d),bf2f(kr[d]),s); s=fmaf(__shfl(a1,d),bf2f(kr[64+d]),s); }
        ps[i*64+lane]=s; m=fmaxf(m,s); }
      for(int of=32;of>0;of>>=1) m=fmaxf(m,__shfl_xor(m,of));
      float l=0.f;
      for(int i=0;i<8;++i){ float p=__builtin_amdgcn_exp2f(ps[i*64+lane]-m); ps[i*64+lane]=p; l+=p; }
      for(int of=32;of>0;of>>=1) l+=__shfl_xor(l,of);
      __builtin_amdgcn_fence(__ATOMIC_SEQ_CST,"wavefront"); asm volatile("s_waitcnt lgkmcnt(0)":::"memory");
      float o0=0.f,o1=0.f;
      for(int k=0;k<512;++k){ float p=ps[k]; o0=fmaf(p,bf2f(Vh[(size_t)k*128+lane]),o0); o1=fmaf(p,bf2f(Vh[(size_t)k*128+64+lane]),o1); }
      const float il=1.f/l;
      { const int u=lane>>5, kk=lane&31, g=(kk&15)>>2, j=(kk&3)+((kk>>4)<<2); const int pos=(rr>>4)*512+(g*16+(rr&15))*8+j;
        const u16 r0=f2bf(o0*il), r1=f2bf(o1*il);
        __hip_atomic_store(Oo+u*2560+pos,r0,__ATOMIC_RELAXED,__HIP_MEMORY_SCOPE_AGENT); __hip_atomic_store(Oo+(u+2)*2560+pos,r1,__ATOMIC_RELAXED,__HIP_MEMORY_SCOPE_AGENT);
        u16* l0=(u16*)(lds+(hh?40960:0)+qh*10240+u*5120); u16* l1=(u16*)(lds+(hh?139264:20480)+qh*10240+u*5120);
        l0[pos]=r0; l1[pos]=r1; }
      __builtin_amdgcn_fence(__ATOMIC_SEQ_CST,"wavefront"); asm volatile("s_waitcnt lgkmcnt(0)":::"memory");
    }
  }
  asm volatile(
  "v_and_b32 v16, 63, %0\n\t"
  "v_and_b32 v17, 3, v16\n\t"
  "s_and_b32 s42, %14, 63\n\t"
  "v_lshl_add_u32 v68, v17, 6, s42\n\t"
  "v_lshlrev_b32 v68, 7, v68\n\t"
  "s_lshl_b32 s42, %9, 2\n\t"
  "s_add_u32 s42, s42, 65536\n\t"
  "v_add_u32 v69, s42, v68\n\t"
  "s_lshl_b32 s42, %14, 7\n\t"
  "s_add_u32 s43, s42, 65536\n\t"
  "v_lshl_add_u32 v70, v17, 2, s43\n\t"
  "v_add_u32 v73, 65536, v70\n\t"
  "s_andn2_b32 s42, %14, 3\n\t"
  "v_add_lshl_u32 v71, v17, s42, 7\n\t"
  "s_lshl_b32 s42, %8, 2\n\t"
  "s_add_u32 s42, s42, 131072\n\t"
  "v_add_u32 v72, s42, v71\n\t"
  "s_and_b32 s47, %7, 1\n\t"
  "s_lshr_b32 s48, %7, 1\n\t"
  "s_and_b32 s49, %9, 1\n\t"
  "s_mul_i32 s42, s47, 10240\n\t"
  "v_lshl_add_u32 v25, v16, 4, s42\n\t"
  "v_add_u32 v26, 139264, v25\n\t"
  "v_lshrrev_b32 v17, 5, v16\n\t"
  "v_mul_u32_u24 v17, 0xc00, v17\n\t"
  "v_bfe_u32 v18, v16, 2, 3\n\t"
  "v_lshl_add_u32 v17, v18, 6, v17\n\t"
  "v_and_b32 v18, 3, v16\n\t"
  "v_lshl_add_u32 v17, v18, 3, v17\n\t"
  "v_bfe_u32 v18, v16, 4, 1\n\t"
  "v_lshlrev_b32 v18, 5, v18\n\t"
  "s_mul_i32 s42, s48, 5\n\t"
  "s_add_u32 s42, s42, 0\n\t"
  "s_and_b32 s43, s42, 1\n\t"
  "s_lshr_b32 s42, s42, 1\n\t"
  "s_lshl_b32 s44, s49, 2\n\t"
  "s_add_u32 s42, s42, s44\n\t"
  "s_sub_u32 s44, s42, 5\n\t"
  "s_cmp_ge_u32 s42, 5\n\t"
  "s_cselect_b32 s42, s44, s42\n\t"
  "s_lshl_b32 s42, s42, 9\n\t"
  "s_add_u32 s42, s42, 65536\n\t"
  "s_lshl_b32 s43, s43, 5\n\t"
  "v_xor_b32 v19, s43, v18\n\t"
  "v_add3_u32 v28, v17, v19, s42\n\t"
  "s_mul_i32 s42, s48, 5\n\t"
  "s_add_u32 s42, s42, 1\n\t"
  "s_and_b32 s43, s42, 1\n\t"
  "s_lshr_b32 s42, s42, 1\n\t"
  "s_lshl_b32 s44, s49, 2\n\t"
  "s_add_u32 s42, s42, s44\n\t"
  "s_sub_u32 s44, s42, 5\n\t"
  "s_cmp_ge_u32 s42, 5\n\t"
  "s_cselect_b32 s42, s44, s42\n\t"
  "s_lshl_b32 s42, s42, 9\n\t"
  "s_add_u32 s42, s42, 65536\n\t"
  "s_lshl_b32 s43, s43, 5\n\t"
  "v_xor_b32 v19, s43, v18\n\t"
  "v_add3_u32 v29, v17, v19, s42\n\t"
  "s_mul_i32 s42, s48, 5\n\t"
  "s_add_u32 s42, s42, 2\n\t"
  "s_and_b32 s43, s42, 1\n\t"
  "s_lshr_b32 s42, s42, 1\n\t"
  "s_lshl_b32 s44, s49, 2\n\t"
  "s_add_u32 s42, s42, s44\n\t"
  "s_sub_u32 s44, s42, 5\n\t"
  "s_cmp_ge_u32 s42, 5\n\t"
  "s_cselect_b32 s42, s44, s42\n\t"
  "s_lshl_b32 s42, s42, 9\n\t"
  "s_add_u32 s42, s42, 65536\n\t"
  "s_lshl_b32 s43, s43, 5\n\t"
  "v_xor_b32 v19, s43, v18\n\t"
  "v_add3_u32 v30, v17, v19, s42\n\t"
  "s_mul_i32 s42, s48, 5\n\t"
  "s_add_u32 s42, s42, 3\n\t"
  "s_and_b32 s43, s42, 1\n\t"
  "s_lshr_b32 s42, s42, 1\n\t"
  "s_lshl_b32 s44, s49, 2\n\t"
  "s_add_u32 s42, s42, s44\n\t"
  "s_sub_u32 s44, s42, 5\n\t"
  "s_cmp_ge_u32 s42, 5\n\t"
  "s_cselect_b32 s42, s44, s42\n\t"
  "s_lshl_b32 s42, s42, 9\n\t"
  "s_add_u32 s42, s42, 65536\n\t"
  "s_lshl_b32 s43, s43, 5\n\t"
  "v_xor_b32 v19, s43, v18\n\t"
  "v_add3_u32 v31, v17, v19, s42\n\t"
  "s_mul_i32 s42, s48, 5\n\t"
  "s_add_u32 s42, s42, 4\n\t"
  "s_and_b32 s43, s42, 1\n\t"
  "s_lshr_b32 s42, s42, 1\n\t"
  "s_lshl_b32 s44, s49, 2\n\t"
  "s_add_u32 s42, s42, s44\n\t"
  "s_sub_u32 s44, s42, 5\n\t"
  "s_cmp_ge_u32 s42, 5\n\t"
  "s_cselect_b32 s42, s44, s42\n\t"
  "s_lshl_b32 s42, s42, 9\n\t"
  "s_add_u32 s42, s42, 65536\n\t"
  "s_lshl_b32 s43, s43, 5\n\t"
  "v_xor_b32 v19, s43, v18\n\t"
  "v_add3_u32 v32, v17, v19, s42\n\t"
  "s_waitcnt vmcnt(0) lgkmcnt(0)\n\t"
  "v_mov_b32 v74, %16\n\t"
  "s_barrier\n\t"
  "s_cmp_lg_u32 %7, 0\n\t"
  "s_cbranch_scc1 70f\n\t"
  "s_mov_b64 exec, 15\n\t"
  "global_store_dword v69, v74, %13 sc1\n\t"
  "s_mov_b64 exec, -1\n\t"
  "70:\n\t"
  "ds_read_b128 a[20:23], v25 offset:0\n\t"
  "ds_read_b128 a[24:27], v25 offset:1024\n\t"
  "ds_read_b128 a[28:31], v25 offset:2048\n\t"
  "ds_read_b128 a[32:35], v25 offset:3072\n\t"
  "ds_read_b128 a[36:39], v25 offset:4096\n\t"
  "ds_read_b64_tr_b16 a[0:1], v28 offset:0\n\t"
  "ds_read_b64_tr_b16 a[2:3], v28 offset:6144\n\t"
  "ds_read_b64_tr_b16 a[4:5], v29 offset:0\n\t"
  "ds_read_b64_tr_b16 a[6:7], v29 offset:6144\n\t"
  "ds_read_b64_tr_b16 a[8:9], v30 offset:0\n\t"
  "ds_read_b64_tr_b16 a[10:11], v30 offset:6144\n\t"
  "ds_read_b64_tr_b16 a[12:13], v31 offset:0\n\t"
  "ds_read_b64_tr_b16 a[14:15], v31 offset:6144\n\t"
  "ds_read_b64_tr_b16 a[16:17], v32 offset:0\n\t"
  "ds_read_b64_tr_b16 a[18:19], v32 offset:6144\n\t"
  "v_add_u32 v33, 49152, v28\n\t"
  "v_add_u32 v34, 49152, v29\n\t"
  "v_add_u32 v35, 49152, v30\n\t"
  "v_add_u32 v36, 49152, v31\n\t"
  "v_add_u32 v37, 49152, v32\n\t"
  "s_mov_b32 s20, %1\n\t"
  "s_mov_b32 s21, %2\n\t"
  "s_mov_b32 s22, 819200\n\t"
  "s_mov_b32 s23, 0x20000\n\t"
  "s_mov_b32 s24, %3\n\t"
  "s_mov_b32 s25, %4\n\t"
  "s_mov_b32 s26, 1311808\n\t"
  "s_mov_b32 s27, 0x20000\n\t"
  "s_mul_i32 s42, s47, 655360\n\t"
  "s_mul_i32 s43, s48, 5120\n\t"
  "s_add_u32 s42, s42, s43\n\t"
  "v_lshl_add_u32 v20, v16, 4, s42\n\t"
  "v_add_u32 v21, 0x1000, v20\n\t"
  "s_mul_i32 s45, s47, 10240\n\t"
  "s_add_u32 s45, s45, s43\n\t"
  "v_lshl_add_u32 v76, v16, 4, s45\n\t"
  "v_add_u32 v77, 139264, v76\n\t"
  "v_bfe_u32 v17, v16, 2, 3\n\t"
  "s_lshl_b32 s42, %7, 3\n\t"
  "v_add_u32 v17, s42, v17\n\t"
  "v_mul_u32_u24 v17, 0x500, v17\n\t"
  "v_bfe_u32 v18, v16, 4, 1\n\t"
  "v_lshlrev_b32 v18, 1, v18\n\t"
  "v_and_b32 v19, 3, v16\n\t"
  "v_xor_b32 v18, v18, v19\n\t"
  "v_lshl_add_u32 v17, v18, 4, v17\n\t"
  "v_lshrrev_b32 v18, 5, v16\n\t"
  "s_lshl_b32 s42, s49, 6\n\t"
  "v_lshl_add_u32 v19, v18, 6, v17\n\t"
  "s_add_u32 s43, s42, 2048\n\t"
  "v_add_u32 v22, s43, v19\n\t"
  "v_add_u32 v23, -896, v22\n\t"
  "s_lshl_b32 s42, s49, 8\n\t"
  "s_sub_u32 s42, 256, s42\n\t"
  "v_add_u32 v24, s42, v17\n\t"
  "v_lshl_add_u32 v24, v18, 24, v24\n\t"
  "s_mul_i32 s46, %7, 3072\n\t"
  "s_add_u32 s42, %9, 0\n\t"
  "s_and_b32 s42, s42, 3\n\t"
  "s_mul_i32 s52, s42, 327680\n\t"
  "s_mul_i32 s56, s42, 40960\n\t"
  "s_cmp_gt_u32 %9, 0\n\t"
  "s_cselect_b32 s42, 0, 1\n\t"
  "s_mul_i32 s53, s42, 327680\n\t"
  "s_mul_i32 s57, s42, 40960\n\t"
  "s_cmp_gt_u32 %9, 1\n\t"
  "s_cselect_b32 s42, 1, 2\n\t"
  "s_mul_i32 s54, s42, 327680\n\t"
  "s_mul_i32 s58, s42, 40960\n\t"
  "s_cmp_gt_u32 %9, 2\n\t"
  "s_cselect_b32 s42, 2, 3\n\t"
  "s_mul_i32 s55, s42, 327680\n\t"
  "s_mul_i32 s59, s42, 40960\n\t"
  "s_waitcnt lgkmcnt(0)\n\t"
  "v_mfma_f32_16x16x32_bf16 v[100:103], a[0:3], a[20:23], 0\n\t"
  "s_mov_b32 s28, %5\n\t"
  "ds_read_b128 a[60:63], v25 offset:5120\n\t"
  "v_mfma_f32_16x16x32_bf16 v[104:107], a[0:3], a[24:27], 0\n\t"
  "s_mov_b32 s29, %6\n\t"
  "ds_read_b128 a[64:67], v25 offset:6144\n\t"
  "v_mfma_f32_16x16x32_bf16 v[108:111], a[0:3], a[28:31], 0\n\t"
  "ds_read_b128 a[68:71], v25 offset:7168\n\t"
  "s_mov_b32 s30, 407360\n\t"
  "v_mfma_f32_16x16x32_bf16 v[112:115], a[0:3], a[32:35], 0\n\t"
  "ds_read_b128 a[72:75], v25 offset:8192\n\t"
  "ds_read_b128 a[76:79], v25 offset:9216\n\t"
  "v_mfma_f32_16x16x32_bf16 v[116:119], a[0:3], a[36:39], 0\n\t"
  "s_mov_b32 s31, 0x20000\n\t"
  "ds_read_b64_tr_b16 a[40:41], v28 offset:12288\n\t"
  "v_mfma_f32_16x16x32_bf16 v[120:123], a[4:7], a[20:23], 0\n\t"
  "s_mov_b32 s36, %10\n\t"
  "ds_read_b64_tr_b16 a[42:43], v28 offset:18432\n\t"
  "v_mfma_f32_16x16x32_bf16 v[124:127], a[4:7], a[24:27], 0\n\t"
  "ds_read_b64_tr_b16 a[44:45], v29 offset:12288\n\t"
  "s_mov_b32 s37, %11\n\t"
  "v_mfma_f32_16x16x32_bf16 v[128:131], a[4:7], a[28:31], 0\n\t"
  "ds_read_b64_tr_b16 a[46:47], v29 offset:18432\n\t"
  "s_mov_b32 s38, 327680\n\t"
  "v_mfma_f32_16x16x32_bf16 v[132:135], a[4:7], a[32:35], 0\n\t"
  "ds_read_b64_tr_b16 a[48:49], v30 offset:12288\n\t"
  "ds_read_b64_tr_b16 a[50:51], v30 offset:18432\n\t"
  "v_mfma_f32_16x16x32_bf16 v[136:139], a[4:7], a[36:39], 0\n\t"
  "s_mov_b32 s39, 0x20000\n\t"
  "ds_read_b64_tr_b16 a[52:53], v31 offset:12288\n\t"
  "v_mfma_f32_16x16x32_bf16 v[140:143], a[8:11], a[20:23], 0\n\t"
  "ds_read_b64_tr_b16 a[54:55], v31 offset:18432\n\t"
  "v_and_b32 v38, 15, v16\n\t"
  "v_mfma_f32_16x16x32_bf16 v[144:147], a[8:11], a[24:27], 0\n\t"
  "ds_read_b64_tr_b16 a[56:57], v32 offset:12288\n\t"
  "v_mul_u32_u24 v38, 0xa00, v38\n\t"
  "v_mfma_f32_16x16x32_bf16 v[148:151], a[8:11], a[28:31], 0\n\t"
  "ds_read_b64_tr_b16 a[58:59], v32 offset:18432\n\t"
  "v_lshrrev_b32 v17, 4, v16\n\t"
  "v_mfma_f32_16x16x32_bf16 v[152:155], a[8:11], a[32:35], 0\n\t"
  "v_lshl_add_u32 v38, v17, 3, v38\n\t"
  "v_mfma_f32_16x16x32_bf16 v[156:159], a[8:11], a[36:39], 0\n\t"
  "s_mul_i32 s42, s47, 204800\n\t"
  "v_mfma_f32_16x16x32_bf16 v[160:163], a[12:15], a[20:23], 0\n\t"
  "v_mfma_f32_16x16x32_bf16 v[164:167], a[12:15], a[24:27], 0\n\t"
  "s_mul_i32 s43, s48, 160\n\t"
  "v_mfma_f32_16x16x32_bf16 v[168:171], a[12:15], a[28:31], 0\n\t"
  "s_add_u32 s42, s42, s43\n\t"
  "v_mfma_f32_16x16x32_bf16 v[172:175], a[12:15], a[32:35], 0\n\t"
  "v_add_u32 v38, s42, v38\n\t"
  "v_mfma_f32_16x16x32_bf16 v[176:179], a[12:15], a[36:39], 0\n\t"
  "s_mul_i32 s42, %7, 5120\n\t"
  "v_mfma_f32_16x16x32_bf16 v[180:183], a[16:19], a[20:23], 0\n\t"
  "v_mfma_f32_16x16x32_bf16 v[184:187], a[16:19], a[24:27], 0\n\t"
  "v_lshl_add_u32 v39, v16, 4, s42\n\t"
  "v_mfma_f32_16x16x32_bf16 v[188:191], a[16:19], a[28:31], 0\n\t"
  "v_mfma_f32_16x16x32_bf16 v[192:195], a[16:19], a[32:35], 0\n\t"
  "v_mfma_f32_16x16x32_bf16 v[196:199], a[16:19], a[36:39], 0\n\t"
  "s_waitcnt vmcnt(63) lgkmcnt(0)\n\t"
  "s_barrier\n\t"
  "v_mfma_f32_16x16x32_bf16 v[100:103], a[40:43], a[60:63], v[100:103]\n\t"
  "ds_read_b128 a[20:23], v25 offset:20480\n\t"
  "v_mfma_f32_16x16x32_bf16 v[104:107], a[40:43], a[64:67], v[104:107]\n\t"
  "ds_read_b128 a[24:27], v25 offset:21504\n\t"
  "v_mfma_f32_16x16x32_bf16 v[108:111], a[40:43], a[68:71], v[108:111]\n\t"
  "ds_read_b128 a[28:31], v25 offset:22528\n\t"
  "v_mfma_f32_16x16x32_bf16 v[112:115], a[40:43], a[72:75], v[112:115]\n\t"
  "ds_read_b128 a[32:35], v25 offset:23552\n\t"
  "ds_read_b128 a[36:39], v25 offset:24576\n\t"
  "v_mfma_f32_16x16x32_bf16 v[116:119], a[40:43], a[76:79], v[116:119]\n\t"
  "ds_read_b64_tr_b16 a[0:1], v28 offset:24576\n\t"
  "v_mfma_f32_16x16x32_bf16 v[120:123], a[44:47], a[60:63], v[120:123]\n\t"
  "ds_read_b64_tr_b16 a[2:3], v28 offset:30720\n\t"
  "v_mfma_f32_16x16x32_bf16 v[124:127], a[44:47], a[64:67], v[124:127]\n\t"
  "ds_read_b64_tr_b16 a[4:5], v29 offset:24576\n\t"
  "ds_read_b64_tr_b16 a[6:7], v29 offset:30720\n\t"
  "v_mfma_f32_16x16x32_bf16 v[128:131], a[44:47], a[68:71], v[128:131]\n\t"
  "ds_read_b64_tr_b16 a[8:9], v30 offset:24576\n\t"
  "v_mfma_f32_16x16x32_bf16 v[132:135], a[44:47], a[72:75], v[132:135]\n\t"
  "ds_read_b64_tr_b16 a[10:11], v30 offset:30720\n\t"
  "v_mfma_f32_16x16x32_bf16 v[136:139], a[44:47], a[76:79], v[136:139]\n\t"
  "ds_read_b64_tr_b16 a[12:13], v31 offset:24576\n\t"
  "ds_read_b64_tr_b16 a[14:15], v31 offset:30720\n\t"
  "v_mfma_f32_16x16x32_bf16 v[140:143], a[48:51], a[60:63], v[140:143]\n\t"
  "ds_read_b64_tr_b16 a[16:17], v32 offset:24576\n\t"
  "v_mfma_f32_16x16x32_bf16 v[144:147], a[48:51], a[64:67], v[144:147]\n\t"
  "ds_read_b64_tr_b16 a[18:19], v32 offset:30720\n\t"
  "v_mfma_f32_16x16x32_bf16 v[148:151], a[48:51], a[68:71], v[148:151]\n\t"
  "v_mfma_f32_16x16x32_bf16 v[152:155], a[48:51], a[72:75], v[152:155]\n\t"
  "v_mfma_f32_16x16x32_bf16 v[156:159], a[48:51], a[76:79], v[156:159]\n\t"
  "v_mfma_f32_16x16x32_bf16 v[160:163], a[52:55], a[60:63], v[160:163]\n\t"
  "v_mfma_f32_16x16x32_bf16 v[164:167], a[52:55], a[64:67], v[164:167]\n\t"
  "v_mfma_f32_16x16x32_bf16 v[168:171], a[52:55], a[68:71], v[168:171]\n\t"
  "v_mfma_f32_16x16x32_bf16 v[172:175], a[52:55], a[72:75], v[172:175]\n\t"
  "v_mfma_f32_16x16x32_bf16 v[176:179], a[52:55], a[76:79], v[176:179]\n\t"
  "v_mfma_f32_16x16x32_bf16 v[180:183], a[56:59], a[60:63], v[180:183]\n\t"
  "v_mfma_f32_16x16x32_bf16 v[184:187], a[56:59], a[64:67], v[184:187]\n\t"
  "v_mfma_f32_16x16x32_bf16 v[188:191], a[56:59], a[68:71], v[188:191]\n\t"
  "v_mfma_f32_16x16x32_bf16 v[192:195], a[56:59], a[72:75], v[192:195]\n\t"
  "v_mfma_f32_16x16x32_bf16 v[196:199], a[56:59], a[76:79], v[196:199]\n\t"
  "s_waitcnt lgkmcnt(0)\n\t"
  "v_mfma_f32_16x16x32_bf16 v[100:103], a[0:3], a[20:23], v[100:103]\n\t"
  "global_load_dword v75, v70, %13 sc1\n\t"
  "v_mfma_f32_16x16x32_bf16 v[104:107], a[0:3], a[24:27], v[104:107]\n\t"
  "ds_read_b128 a[60:63], v25 offset:25600\n\t"
  "v_mfma_f32_16x16x32_bf16 v[108:111], a[0:3], a[28:31], v[108:111]\n\t"
  "ds_read_b128 a[64:67], v25 offset:26624\n\t"
  "ds_read_b128 a[68:71], v25 offset:27648\n\t"
  "v_mfma_f32_16x16x32_bf16 v[112:115], a[0:3], a[32:35], v[112:115]\n\t"
  "s_add_u32 m0, s46, 65536\n\t"
  "s_add_u32 s41, s52, 245760\n\t"
  "buffer_load_dwordx4 v22, s[24:27], s41 offen offset:0 lds\n\t"
  "v_mfma_f32_16x16x32_bf16 v[116:119], a[0:3], a[36:39], v[116:119]\n\t"
  "v_mfma_f32_16x16x32_bf16 v[120:123], a[4:7], a[20:23], v[120:123]\n\t"
  "v_mfma_f32_16x16x32_bf16 v[124:127], a[4:7], a[24:27], v[124:127]\n\t"
  "ds_read_b128 a[72:75], v25 offset:28672\n\t"
  "ds_read_b128 a[76:79], v25 offset:29696\n\t"
  "v_mfma_f32_16x16x32_bf16 v[128:131], a[4:7], a[28:31], v[128:131]\n\t"
  "ds_read_b64_tr_b16 a[40:41], v28 offset:36864\n\t"
  "ds_read_b64_tr_b16 a[42:43], v28 offset:43008\n\t"
  "v_mfma_f32_16x16x32_bf16 v[132:135], a[4:7], a[32:35], v[132:135]\n\t"
  "ds_read_b64_tr_b16 a[44:45], v29 offset:36864\n\t"
  "ds_read_b64_tr_b16 a[46:47], v29 offset:43008\n\t"
  "v_mfma_f32_16x16x32_bf16 v[136:139], a[4:7], a[36:39], v[136:139]\n\t"
  "buffer_load_dwordx4 v23, s[24:27], s41 offen offset:1024 lds\n\t"
  "v_mfma_f32_16x16x32_bf16 v[140:143], a[8:11], a[20:23], v[140:143]\n\t"
  "ds_read_b64_tr_b16 a[48:49], v30 offset:36864\n\t"
  "v_mfma_f32_16x16x32_bf16 v[144:147], a[8:11], a[24:27], v[144:147]\n\t"
  "ds_read_b64_tr_b16 a[50:51], v30 offset:43008\n\t"
  "ds_read_b64_tr_b16 a[52:53], v31 offset:36864\n\t"
  "v_mfma_f32_16x16x32_bf16 v[148:151], a[8:11], a[28:31], v[148:151]\n\t"
  "buffer_load_dwordx4 v24, s[24:27], s41 offen offset:2048 lds\n\t"
  "v_mfma_f32_16x16x32_bf16 v[152:155], a[8:11], a[32:35], v[152:155]\n\t"
  "v_mfma_f32_16x16x32_bf16 v[156:159], a[8:11], a[36:39], v[156:159]\n\t"
  "ds_read_b64_tr_b16 a[54:55], v31 offset:43008\n\t"
  "ds_read_b64_tr_b16 a[56:57], v32 offset:36864\n\t"
  "v_mfma_f32_16x16x32_bf16 v[160:163], a[12:15], a[20:23], v[160:163]\n\t"
  "ds_read_b64_tr_b16 a[58:59], v32 offset:43008\n\t"
  "s_add_u32 m0, s46, 77824\n\t"
  "s_add_u32 s41, s52, 286720\n\t"
  "buffer_load_dwordx4 v22, s[24:27], s41 offen offset:0 lds\n\t"
  "v_mfma_f32_16x16x32_bf16 v[164:167], a[12:15], a[24:27], v[164:167]\n\t"
  "v_mfma_f32_16x16x32_bf16 v[168:171], a[12:15], a[28:31], v[168:171]\n\t"
  "v_mfma_f32_16x16x32_bf16 v[172:175], a[12:15], a[32:35], v[172:175]\n\t"
  "buffer_load_dwordx4 v23, s[24:27], s41 offen offset:1024 lds\n\t"
  "v_mfma_f32_16x16x32_bf16 v[176:179], a[12:15], a[36:39], v[176:179]\n\t"
  "v_mfma_f32_16x16x32_bf16 v[180:183], a[16:19], a[20:23], v[180:183]\n\t"
  "buffer_load_dwordx4 v24, s[24:27], s41 offen offset:2048 lds\n\t"
  "v_mfma_f32_16x16x32_bf16 v[184:187], a[16:19], a[24:27], v[184:187]\n\t"
  "v_mfma_f32_16x16x32_bf16 v[188:191], a[16:19], a[28:31], v[188:191]\n\t"
  "v_mfma_f32_16x16x32_bf16 v[192:195], a[16:19], a[32:35], v[192:195]\n\t"
  "v_mfma_f32_16x16x32_bf16 v[196:199], a[16:19], a[36:39], v[196:199]\n\t"
  "s_waitcnt vmcnt(6) lgkmcnt(0)\n\t"
  "s_barrier\n\t"
  "s_mov_b32 s50, 0x400000\n\t"
  "s_branch 72f\n\t"
  "71:\n\t"
  "global_load_dword v75, v70, %13 sc1\n\t"
  "s_and_b32 s44, s50, 15\n\t"
  "s_cmp_lg_u32 s44, 1\n\t"
  "s_cbranch_scc1 74f\n\t"
  "global_load_dword v74, v68, %13 sc1\n\t"
  "s_waitcnt vmcnt(0)\n\t"
  "s_cmp_lg_u32 %7, 0\n\t"
  "s_cbranch_scc1 75f\n\t"
  "s_mov_b64 exec, 15\n\t"
  "global_store_dword v69, v74, %13 sc1\n\t"
  "s_mov_b64 exec, -1\n\t"
  "75:\n\t"
  "74:\n\t"
  "s_waitcnt vmcnt(0)\n\t"
  "s_sub_u32 s50, s50, 1\n\t"
  "s_cbranch_scc1 73f\n\t"
  "72:\n\t"
  "v_cmp_ne_u32 vcc, %12, v75\n\t"
  "s_cbranch_vccnz 71b\n\t"
  "73:\n\t"
  "v_mfma_f32_16x16x32_bf16 v[100:103], a[40:43], a[60:63], v[100:103]\n\t"
  "global_load_dword v74, v68, %13 sc1\n\t"
  "v_mfma_f32_16x16x32_bf16 v[104:107], a[40:43], a[64:67], v[104:107]\n\t"
  "ds_read_b128 a[20:23], v25 offset:40960\n\t"
  "ds_read_b128 a[24:27], v25 offset:41984\n\t"
  "v_mfma_f32_16x16x32_bf16 v[108:111], a[40:43], a[68:71], v[108:111]\n\t"
  "s_add_u32 m0, s45, 0\n\t"
  "s_add_u32 s40, s57, 0\n\t"
  "buffer_load_dwordx4 v20, s[20:23], s40 offen offset:0 sc1 lds\n\t"
  "v_mfma_f32_16x16x32_bf16 v[112:115], a[40:43], a[72:75], v[112:115]\n\t"
  "v_mfma_f32_16x16x32_bf16 v[116:119], a[40:43], a[76:79], v[116:119]\n\t"
  "ds_read_b128 a[28:31], v25 offset:43008\n\t"
  "ds_read_b128 a[32:35], v25 offset:44032\n\t"
  "v_mfma_f32_16x16x32_bf16 v[120:123], a[44:47], a[60:63], v[120:123]\n\t"
  "ds_read_b128 a[36:39], v25 offset:45056\n\t"
  "buffer_load_dwordx4 v20, s[20:23], s40 offen offset:1024 sc1 lds\n\t"
  "v_mfma_f32_16x16x32_bf16 v[124:127], a[44:47], a[64:67], v[124:127]\n\t"
  "v_mfma_f32_16x16x32_bf16 v[128:131], a[44:47], a[68:71], v[128:131]\n\t"
  "ds_read_b64_tr_b16 a[0:1], v28 offset:49152\n\t"
  "ds_read_b64_tr_b16 a[2:3], v28 offset:55296\n\t"
  "ds_read_b64_tr_b16 a[4:5], v29 offset:49152\n\t"
  "v_mfma_f32_16x16x32_bf16 v[132:135], a[44:47], a[72:75], v[132:135]\n\t"
  "buffer_load_dwordx4 v20, s[20:23], s40 offen offset:2048 sc1 lds\n\t"
  "v_mfma_f32_16x16x32_bf16 v[136:139], a[44:47], a[76:79], v[136:139]\n\t"
  "ds_read_b64_tr_b16 a[6:7], v29 offset:55296\n\t"
  "v_mfma_f32_16x16x32_bf16 v[140:143], a[48:51], a[60:63], v[140:143]\n\t"
  "ds_read_b64_tr_b16 a[8:9], v30 offset:49152\n\t"
  "buffer_load_dwordx4 v20, s[20:23], s40 offen offset:3072 sc1 lds\n\t"
  "v_mfma_f32_16x16x32_bf16 v[144:147], a[48:51], a[64:67], v[144:147]\n\t"
  "ds_read_b64_tr_b16 a[10:11], v30 offset:55296\n\t"
  "v_mfma_f32_16x16x32_bf16 v[148:151], a[48:51], a[68:71], v[148:151]\n\t"
  "ds_read_b64_tr_b16 a[12:13], v31 offset:49152\n\t"
  "s_add_u32 m0, s45, 4096\n\t"
  "s_nop 0\n\t"
  "buffer_load_dwordx4 v21, s[20:23], s40 offen offset:0 sc1 lds\n\t"
  "v_mfma_f32_16x16x32_bf16 v[152:155], a[48:51], a[72:75], v[152:155]\n\t"
  "v_mfma_f32_16x16x32_bf16 v[156:159], a[48:51], a[76:79], v[156:159]\n\t"
  "ds_read_b64_tr_b16 a[14:15], v31 offset:55296\n\t"
  "v_mfma_f32_16x16x32_bf16 v[160:163], a[52:55], a[60:63], v[160:163]\n\t"
  "ds_read_b64_tr_b16 a[16:17], v32 offset:49152\n\t"
  "ds_read_b64_tr_b16 a[18:19], v32 offset:55296\n\t"
  "s_add_u32 m0, s45, 20480\n\t"
  "s_add_u32 s40, s57, 10240\n\t"
  "buffer_load_dwordx4 v20, s[20:23], s40 offen offset:0 sc1 lds\n\t"
  "v_mfma_f32_16x16x32_bf16 v[164:167], a[52:55], a[64:67], v[164:167]\n\t"
  "v_mfma_f32_16x16x32_bf16 v[168:171], a[52:55], a[68:71], v[168:171]\n\t"
  "v_mfma_f32_16x16x32_bf16 v[172:175], a[52:55], a[72:75], v[172:175]\n\t"
  "buffer_load_dwordx4 v20, s[20:23], s40 offen offset:1024 sc1 lds\n\t"
  "v_mfma_f32_16x16x32_bf16 v[176:179], a[52:55], a[76:79], v[176:179]\n\t"
  "buffer_load_dwordx4 v20, s[20:23], s40 offen offset:2048 sc1 lds\n\t"
  "v_mfma_f32_16x16x32_bf16 v[180:183], a[56:59], a[60:63], v[180:183]\n\t"
  "buffer_load_dwordx4 v20, s[20:23], s40 offen offset:3072 sc1 lds\n\t"
  "v_mfma_f32_16x16x32_bf16 v[184:187], a[56:59], a[64:67], v[184:187]\n\t"
  "v_mfma_f32_16x16x32_bf16 v[188:191], a[56:59], a[68:71], v[188:191]\n\t"
  "s_add_u32 m0, s45, 24576\n\t"
  "s_nop 0\n\t"
  "buffer_load_dwordx4 v21, s[20:23], s40 offen offset:0 sc1 lds\n\t"
  "v_mfma_f32_16x16x32_bf16 v[192:195], a[56:59], a[72:75], v[192:195]\n\t"
  "v_mfma_f32_16x16x32_bf16 v[196:199], a[56:59], a[76:79], v[196:199]\n\t"
  "s_waitcnt lgkmcnt(0)\n\t"
  "v_mfma_f32_16x16x32_bf16 v[100:103], a[0:3], a[20:23], v[100:103]\n\t"
  "s_add_u32 m0, s46, 90112\n\t"
  "s_add_u32 s41, s53, 0\n\t"
  "buffer_load_dwordx4 v22, s[24:27], s41 offen offset:0 lds\n\t"
  "v_mfma_f32_16x16x32_bf16 v[104:107], a[0:3], a[24:27], v[104:107]\n\t"
  "v_mfma_f32_16x16x32_bf16 v[108:111], a[0:3], a[28:31], v[108:111]\n\t"
  "ds_read_b128 a[60:63], v25 offset:46080\n\t"
  "v_mfma_f32_16x16x32_bf16 v[112:115], a[0:3], a[32:35], v[112:115]\n\t"
  "ds_read_b128 a[64:67], v25 offset:47104\n\t"
  "ds_read_b128 a[68:71], v25 offset:48128\n\t"
  "v_mfma_f32_16x16x32_bf16 v[116:119], a[0:3], a[36:39], v[116:119]\n\t"
  "ds_read_b128 a[72:75], v25 offset:49152\n\t"
  "ds_read_b128 a[76:79], v25 offset:50176\n\t"
  "v_mfma_f32_16x16x32_bf16 v[120:123], a[4:7], a[20:23], v[120:123]\n\t"
  "ds_read_b64_tr_b16 a[40:41], v28 offset:61440\n\t"
  "buffer_load_dwordx4 v23, s[24:27], s41 offen offset:1024 lds\n\t"
  "v_mfma_f32_16x16x32_bf16 v[124:127], a[4:7], a[24:27], v[124:127]\n\t"
  "v_mfma_f32_16x16x32_bf16 v[128:131], a[4:7], a[28:31], v[128:131]\n\t"
  "ds_read_b64_tr_b16 a[42:43], v33 offset:18432\n\t"
  "v_mfma_f32_16x16x32_bf16 v[132:135], a[4:7], a[32:35], v[132:135]\n\t"
  "ds_read_b64_tr_b16 a[44:45], v29 offset:61440\n\t"
  "ds_read_b64_tr_b16 a[46:47], v34 offset:18432\n\t"
  "v_mfma_f32_16x16x32_bf16 v[136:139], a[4:7], a[36:39], v[136:139]\n\t"
  "ds_read_b64_tr_b16 a[48:49], v30 offset:61440\n\t"
  "buffer_load_dwordx4 v24, s[24:27], s41 offen offset:2048 lds\n\t"
  "v_mfma_f32_16x16x32_bf16 v[140:143], a[8:11], a[20:23], v[140:143]\n\t"
  "v_mfma_f32_16x16x32_bf16 v[144:147], a[8:11], a[24:27], v[144:147]\n\t"
  "ds_read_b64_tr_b16 a[50:51], v35 offset:18432\n\t"
  "ds_read_b64_tr_b16 a[52:53], v31 offset:61440\n\t"
  "v_mfma_f32_16x16x32_bf16 v[148:151], a[8:11], a[28:31], v[148:151]\n\t"
  "ds_read_b64_tr_b16 a[54:55], v36 offset:18432\n\t"
  "ds_read_b64_tr_b16 a[56:57], v32 offset:61440\n\t"
  "v_mfma_f32_16x16x32_bf16 v[152:155], a[8:11], a[32:35], v[152:155]\n\t"
  "s_add_u32 m0, s46, 102400\n\t"
  "s_add_u32 s41, s53, 40960\n\t"
  "buffer_load_dwordx4 v22, s[24:27], s41 offen offset:0 lds\n\t"
  "v_mfma_f32_16x16x32_bf16 v[156:159], a[8:11], a[36:39], v[156:159]\n\t"
  "v_mfma_f32_16x16x32_bf16 v[160:163], a[12:15], a[20:23], v[160:163]\n\t"
  "v_mfma_f32_16x16x32_bf16 v[164:167], a[12:15], a[24:27], v[164:167]\n\t"
  "ds_read_b64_tr_b16 a[58:59], v37 offset:18432\n\t"
  "buffer_load_dwordx4 v23, s[24:27], s41 offen offset:1024 lds\n\t"
  "v_mfma_f32_16x16x32_bf16 v[168:171], a[12:15], a[28:31], v[168:171]\n\t"
  "v_mfma_f32_16x16x32_bf16 v[172:175], a[12:15], a[32:35], v[172:175]\n\t"
  "buffer_load_dwordx4 v24, s[24:27], s41 offen offset:2048 lds\n\t"
  "v_mfma_f32_16x16x32_bf16 v[176:179], a[12:15], a[36:39], v[176:179]\n\t"
  "v_mfma_f32_16x16x32_bf16 v[180:183], a[16:19], a[20:23], v[180:183]\n\t"
  "v_mfma_f32_16x16x32_bf16 v[184:187], a[16:19], a[24:27], v[184:187]\n\t"
  "v_mfma_f32_16x16x32_bf16 v[188:191], a[16:19], a[28:31], v[188:191]\n\t"
  "v_mfma_f32_16x16x32_bf16 v[192:195], a[16:19], a[32:35], v[192:195]\n\t"
  "v_mfma_f32_16x16x32_bf16 v[196:199], a[16:19], a[36:39], v[196:199]\n\t"
  "s_waitcnt vmcnt(17) lgkmcnt(0)\n\t"
  "s_barrier\n\t"
  "v_mfma_f32_16x16x32_bf16 v[100:103], a[40:43], a[60:63], v[100:103]\n\t"
  "s_add_u32 m0, s45, 40960\n\t"
  "s_add_u32 s40, s57, 20480\n\t"
  "buffer_load_dwordx4 v20, s[20:23], s40 offen offset:0 sc1 lds\n\t"
  "v_mfma_f32_16x16x32_bf16 v[104:107], a[40:43], a[64:67], v[104:107]\n\t"
  "v_mfma_f32_16x16x32_bf16 v[108:111], a[40:43], a[68:71], v[108:111]\n\t"
  "ds_read_b128 a[20:23], v26 offset:0\n\t"
  "v_mfma_f32_16x16x32_bf16 v[112:115], a[40:43], a[72:75], v[112:115]\n\t"
  "ds_read_b128 a[24:27], v26 offset:1024\n\t"
  "ds_read_b128 a[28:31], v26 offset:2048\n\t"
  "v_mfma_f32_16x16x32_bf16 v[116:119], a[40:43], a[76:79], v[116:119]\n\t"
  "ds_read_b128 a[32:35], v26 offset:3072\n\t"
  "ds_read_b128 a[36:39], v26 offset:4096\n\t"
  "v_mfma_f32_16x16x32_bf16 v[120:123], a[44:47], a[60:63], v[120:123]\n\t"
  "ds_read_b64_tr_b16 a[0:1], v28 offset:0\n\t"
  "ds_read_b64_tr_b16 a[2:3], v28 offset:6144\n\t"
  "v_mfma_f32_16x16x32_bf16 v[124:127], a[44:47], a[64:67], v[124:127]\n\t"
  "buffer_load_dwordx4 v20, s[20:23], s40 offen offset:1024 sc1 lds\n\t"
  "v_mfma_f32_16x16x32_bf16 v[128:131], a[44:47], a[68:71], v[128:131]\n\t"
  "v_mfma_f32_16x16x32_bf16 v[132:135], a[44:47], a[72:75], v[132:135]\n\t"
  "ds_read_b64_tr_b16 a[4:5], v29 offset:0\n\t"
  "ds_read_b64_tr_b16 a[6:7], v29 offset:6144\n\t"
  "v_mfma_f32_16x16x32_bf16 v[136:139], a[44:47], a[76:79], v[136:139]\n\t"
  "ds_read_b64_tr_b16 a[8:9], v30 offset:0\n\t"
  "ds_read_b64_tr_b16 a[10:11], v30 offset:6144\n\t"
  "v_mfma_f32_16x16x32_bf16 v[140:143], a[48:51], a[60:63], v[140:143]\n\t"
  "ds_read_b64_tr_b16 a[12:13], v31 offset:0\n\t"
  "buffer_load_dwordx4 v20, s[20:23], s40 offen offset:2048 sc1 lds\n\t"
  "v_mfma_f32_16x16x32_bf16 v[144:147], a[48:51], a[64:67], v[144:147]\n\t"
  "v_mfma_f32_16x16x32_bf16 v[148:151], a[48:51], a[68:71], v[148:151]\n\t"
  "ds_read_b64_tr_b16 a[14:15], v31 offset:6144\n\t"
  "ds_read_b64_tr_b16 a[16:17], v32 offset:0\n\t"
  "v_mfma_f32_16x16x32_bf16 v[152:155], a[48:51], a[72:75], v[152:155]\n\t"
  "ds_read_b64_tr_b16 a[18:19], v32 offset:6144\n\t"
  "buffer_load_dwordx4 v20, s[20:23], s40 offen offset:3072 sc1 lds\n\t"
  "v_mfma_f32_16x16x32_bf16 v[156:159], a[48:51], a[76:79], v[156:159]\n\t"
  "v_mfma_f32_16x16x32_bf16 v[160:163], a[52:55], a[60:63], v[160:163]\n\t"
  "v_mfma_f32_16x16x32_bf16 v[164:167], a[52:55], a[64:67], v[164:167]\n\t"
  "s_add_u32 m0, s45, 45056\n\t"
  "s_nop 0\n\t"
  "buffer_load_dwordx4 v21, s[20:23], s40 offen offset:0 sc1 lds\n\t"
  "v_mfma_f32_16x16x32_bf16 v[168:171], a[52:55], a[68:71], v[168:171]\n\t"
  "v_mfma_f32_16x16x32_bf16 v[172:175], a[52:55], a[72:75], v[172:175]\n\t"
  "v_mfma_f32_16x16x32_bf16 v[176:179], a[52:55], a[76:79], v[176:179]\n\t"
  "v_mfma_f32_16x16x32_bf16 v[180:183], a[56:59], a[60:63], v[180:183]\n\t"
  "v_mfma_f32_16x16x32_bf16 v[184:187], a[56:59], a[64:67], v[184:187]\n\t"
  "v_mfma_f32_16x16x32_bf16 v[188:191], a[56:59], a[68:71], v[188:191]\n\t"
  "v_mfma_f32_16x16x32_bf16 v[192:195], a[56:59], a[72:75], v[192:195]\n\t"
  "v_mfma_f32_16x16x32_bf16 v[196:199], a[56:59], a[76:79], v[196:199]\n\t"
  "s_waitcnt lgkmcnt(0)\n\t"
  "v_mfma_f32_16x16x32_bf16 v[100:103], a[0:3], a[20:23], v[100:103]\n\t"
  "s_add_u32 m0, s46, 114688\n\t"
  "s_add_u32 s41, s53, 81920\n\t"
  "buffer_load_dwordx4 v22, s[24:27], s41 offen offset:0 lds\n\t"
  "v_mfma_f32_16x16x32_bf16 v[104:107], a[0:3], a[24:27], v[104:107]\n\t"
  "v_mfma_f32_16x16x32_bf16 v[108:111], a[0:3], a[28:31], v[108:111]\n\t"
  "ds_read_b128 a[60:63], v26 offset:5120\n\t"
  "v_mfma_f32_16x16x32_bf16 v[112:115], a[0:3], a[32:35], v[112:115]\n\t"
  "ds_read_b128 a[64:67], v26 offset:6144\n\t"
  "ds_read_b128 a[68:71], v26 offset:7168\n\t"
  "v_mfma_f32_16x16x32_bf16 v[116:119], a[0:3], a[36:39], v[116:119]\n\t"
  "ds_read_b128 a[72:75], v26 offset:8192\n\t"
  "ds_read_b128 a[76:79], v26 offset:9216\n\t"
  "v_mfma_f32_16x16x32_bf16 v[120:123], a[4:7], a[20:23], v[120:123]\n\t"
  "ds_read_b64_tr_b16 a[40:41], v28 offset:12288\n\t"
  "buffer_load_dwordx4 v23, s[24:27], s41 offen offset:1024 lds\n\t"
  "v_mfma_f32_16x16x32_bf16 v[124:127], a[4:7], a[24:27], v[124:127]\n\t"
  "v_mfma_f32_16x16x32_bf16 v[128:131], a[4:7], a[28:31], v[128:131]\n\t"
  "ds_read_b64_tr_b16 a[42:43], v28 offset:18432\n\t"
  "v_mfma_f32_16x16x32_bf16 v[132:135], a[4:7], a[32:35], v[132:135]\n\t"
  "ds_read_b64_tr_b16 a[44:45], v29 offset:12288\n\t"
  "ds_read_b64_tr_b16 a[46:47], v29 offset:18432\n\t"
  "v_mfma_f32_16x16x32_bf16 v[136:139], a[4:7], a[36:39], v[136:139]\n\t"
  "ds_read_b64_tr_b16 a[48:49], v30 offset:12288\n\t"
  "buffer_load_dwordx4 v24, s[24:27], s41 offen offset:2048 lds\n\t"
  "v_mfma_f32_16x16x32_bf16 v[140:143], a[8:11], a[20:23], v[140:143]\n\t"
  "v_mfma_f32_16x16x32_bf16 v[144:147], a[8:11], a[24:27], v[144:147]\n\t"
  "ds_read_b64_tr_b16 a[50:51], v30 offset:18432\n\t"
  "ds_read_b64_tr_b16 a[52:53], v31 offset:12288\n\t"
  "v_mfma_f32_16x16x32_bf16 v[148:151], a[8:11], a[28:31], v[148:151]\n\t"
  "ds_read_b64_tr_b16 a[54:55], v31 offset:18432\n\t"
  "ds_read_b64_tr_b16 a[56:57], v32 offset:12288\n\t"
  "v_mfma_f32_16x16x32_bf16 v[152:155], a[8:11], a[32:35], v[152:155]\n\t"
  "s_add_u32 m0, s46, 126976\n\t"
  "s_add_u32 s41, s53, 122880\n\t"
  "buffer_load_dwordx4 v22, s[24:27], s41 offen offset:0 lds\n\t"
  "v_mfma_f32_16x16x32_bf16 v[156:159], a[8:11], a[36:39], v[156:159]\n\t"
  "v_mfma_f32_16x16x32_bf16 v[160:163], a[12:15], a[20:23], v[160:163]\n\t"
  "v_mfma_f32_16x16x32_bf16 v[164:167], a[12:15], a[24:27], v[164:167]\n\t"
  "ds_read_b64_tr_b16 a[58:59], v32 offset:18432\n\t"
  "buffer_load_dwordx4 v23, s[24:27], s41 offen offset:1024 lds\n\t"
  "v_mfma_f32_16x16x32_bf16 v[168:171], a[12:15], a[28:31], v[168:171]\n\t"
  "v_mfma_f32_16x16x32_bf16 v[172:175], a[12:15], a[32:35], v[172:175]\n\t"
  "buffer_load_dwordx4 v24, s[24:27], s41 offen offset:2048 lds\n\t"
  "v_mfma_f32_16x16x32_bf16 v[176:179], a[12:15], a[36:39], v[176:179]\n\t"
  "v_mfma_f32_16x16x32_bf16 v[180:183], a[16:19], a[20:23], v[180:183]\n\t"
  "v_mfma_f32_16x16x32_bf16 v[184:187], a[16:19], a[24:27], v[184:187]\n\t"
  "v_mfma_f32_16x16x32_bf16 v[188:191], a[16:19], a[28:31], v[188:191]\n\t"
  "v_mfma_f32_16x16x32_bf16 v[192:195], a[16:19], a[32:35], v[192:195]\n\t"
  "v_mfma_f32_16x16x32_bf16 v[196:199], a[16:19], a[36:39], v[196:199]\n\t"
  "s_waitcnt vmcnt(11) lgkmcnt(0)\n\t"
  "s_barrier\n\t"
  "v_mfma_f32_16x16x32_bf16 v[100:103], a[40:43], a[60:63], v[100:103]\n\t"
  "s_add_u32 m0, s45, 139264\n\t"
  "s_add_u32 s40, s57, 30720\n\t"
  "buffer_load_dwordx4 v20, s[20:23], s40 offen offset:0 sc1 lds\n\t"
  "v_mfma_f32_16x16x32_bf16 v[104:107], a[40:43], a[64:67], v[104:107]\n\t"
  "v_mfma_f32_16x16x32_bf16 v[108:111], a[40:43], a[68:71], v[108:111]\n\t"
  "ds_read_b128 a[20:23], v25 offset:0\n\t"
  "v_mfma_f32_16x16x32_bf16 v[112:115], a[40:43], a[72:75], v[112:115]\n\t"
  "ds_read_b128 a[24:27], v25 offset:1024\n\t"
  "ds_read_b128 a[28:31], v25 offset:2048\n\t"
  "v_mfma_f32_16x16x32_bf16 v[116:119], a[40:43], a[76:79], v[116:119]\n\t"
  "ds_read_b128 a[32:35], v25 offset:3072\n\t"
  "ds_read_b128 a[36:39], v25 offset:4096\n\t"
  "v_mfma_f32_16x16x32_bf16 v[120:123], a[44:47], a[60:63], v[120:123]\n\t"
  "ds_read_b64_tr_b16 a[0:1], v28 offset:24576\n\t"
  "ds_read_b64_tr_b16 a[2:3], v28 offset:30720\n\t"
  "v_mfma_f32_16x16x32_bf16 v[124:127], a[44:47], a[64:67], v[124:127]\n\t"
  "buffer_load_dwordx4 v20, s[20:23], s40 offen offset:1024 sc1 lds\n\t"
  "v_mfma_f32_16x16x32_bf16 v[128:131], a[44:47], a[68:71], v[128:131]\n\t"
  "v_mfma_f32_16x16x32_bf16 v[132:135], a[44:47], a[72:75], v[132:135]\n\t"
  "ds_read_b64_tr_b16 a[4:5], v29 offset:24576\n\t"
  "ds_read_b64_tr_b16 a[6:7], v29 offset:30720\n\t"
  "v_mfma_f32_16x16x32_bf16 v[136:139], a[44:47], a[76:79], v[136:139]\n\t"
  "ds_read_b64_tr_b16 a[8:9], v30 offset:24576\n\t"
  "ds_read_b64_tr_b16 a[10:11], v30 offset:30720\n\t"
  "v_mfma_f32_16x16x32_bf16 v[140:143], a[48:51], a[60:63], v[140:143]\n\t"
  "ds_read_b64_tr_b16 a[12:13], v31 offset:24576\n\t"
  "buffer_load_dwordx4 v20, s[20:23], s40 offen offset:2048 sc1 lds\n\t"
  "v_mfma_f32_16x16x32_bf16 v[144:147], a[48:51], a[64:67], v[144:147]\n\t"
  "v_mfma_f32_16x16x32_bf16 v[148:151], a[48:51], a[68:71], v[148:151]\n\t"
  "ds_read_b64_tr_b16 a[14:15], v31 offset:30720\n\t"
  "ds_read_b64_tr_b16 a[16:17], v32 offset:24576\n\t"
  "v_mfma_f32_16x16x32_bf16 v[152:155], a[48:51], a[72:75], v[152:155]\n\t"
  "ds_read_b64_tr_b16 a[18:19], v32 offset:30720\n\t"
  "buffer_load_dwordx4 v20, s[20:23], s40 offen offset:3072 sc1 lds\n\t"
  "v_mfma_f32_16x16x32_bf16 v[156:159], a[48:51], a[76:79], v[156:159]\n\t"
  "v_mfma_f32_16x16x32_bf16 v[160:163], a[52:55], a[60:63], v[160:163]\n\t"
  "v_mfma_f32_16x16x32_bf16 v[164:167], a[52:55], a[64:67], v[164:167]\n\t"
  "s_add_u32 m0, s45, 143360\n\t"
  "s_nop 0\n\t"
  "buffer_load_dwordx4 v21, s[20:23], s40 offen offset:0 sc1 lds\n\t"
  "v_mfma_f32_16x16x32_bf16 v[168:171], a[52:55], a[68:71], v[168:171]\n\t"
  "v_mfma_f32_16x16x32_bf16 v[172:175], a[52:55], a[72:75], v[172:175]\n\t"
  "v_mfma_f32_16x16x32_bf16 v[176:179], a[52:55], a[76:79], v[176:179]\n\t"
  "v_mfma_f32_16x16x32_bf16 v[180:183], a[56:59], a[60:63], v[180:183]\n\t"
  "v_mfma_f32_16x16x32_bf16 v[184:187], a[56:59], a[64:67], v[184:187]\n\t"
  "v_mfma_f32_16x16x32_bf16 v[188:191], a[56:59], a[68:71], v[188:191]\n\t"
  "v_mfma_f32_16x16x32_bf16 v[192:195], a[56:59], a[72:75], v[192:195]\n\t"
  "v_mfma_f32_16x16x32_bf16 v[196:199], a[56:59], a[76:79], v[196:199]\n\t"
  "s_waitcnt lgkmcnt(0)\n\t"
  "v_mfma_f32_16x16x32_bf16 v[100:103], a[0:3], a[20:23], v[100:103]\n\t"
  "s_add_u32 m0, s46, 65536\n\t"
  "s_add_u32 s41, s53, 163840\n\t"
  "buffer_load_dwordx4 v22, s[24:27], s41 offen offset:0 lds\n\t"
  "v_mfma_f32_16x16x32_bf16 v[104:107], a[0:3], a[24:27], v[104:107]\n\t"
  "v_mfma_f32_16x16x32_bf16 v[108:111], a[0:3], a[28:31], v[108:111]\n\t"
  "ds_read_b128 a[60:63], v25 offset:5120\n\t"
  "v_mfma_f32_16x16x32_bf16 v[112:115], a[0:3], a[32:35], v[112:115]\n\t"
  "ds_read_b128 a[64:67], v25 offset:6144\n\t"
  "ds_read_b128 a[68:71], v25 offset:7168\n\t"
  "v_mfma_f32_16x16x32_bf16 v[116:119], a[0:3], a[36:39], v[116:119]\n\t"
  "ds_read_b128 a[72:75], v25 offset:8192\n\t"
  "ds_read_b128 a[76:79], v25 offset:9216\n\t"
  "v_mfma_f32_16x16x32_bf16 v[120:123], a[4:7], a[20:23], v[120:123]\n\t"
  "ds_read_b64_tr_b16 a[40:41], v28 offset:36864\n\t"
  "buffer_load_dwordx4 v23, s[24:27], s41 offen offset:1024 lds\n\t"
  "v_mfma_f32_16x16x32_bf16 v[124:127], a[4:7], a[24:27], v[124:127]\n\t"
  "v_mfma_f32_16x16x32_bf16 v[128:131], a[4:7], a[28:31], v[128:131]\n\t"
  "ds_read_b64_tr_b16 a[42:43], v28 offset:43008\n\t"
  "v_mfma_f32_16x16x32_bf16 v[132:135], a[4:7], a[32:35], v[132:135]\n\t"
  "ds_read_b64_tr_b16 a[44:45], v29 offset:36864\n\t"
  "ds_read_b64_tr_b16 a[46:47], v29 offset:43008\n\t"
  "v_mfma_f32_16x16x32_bf16 v[136:139], a[4:7], a[36:39], v[136:139]\n\t"
  "buffer_load_dwordx4 v24, s[24:27], s41 offen offset:2048 lds\n\t"
  "v_mfma_f32_16x16x32_bf16 v[140:143], a[8:11], a[20:23], v[140:143]\n\t"
  "ds_read_b64_tr_b16 a[48:49], v30 offset:36864\n\t"
  "v_mfma_f32_16x16x32_bf16 v[144:147], a[8:11], a[24:27], v[144:147]\n\t"
  "ds_read_b64_tr_b16 a[50:51], v30 offset:43008\n\t"
  "ds_read_b64_tr_b16 a[52:53], v31 offset:36864\n\t"
  "v_mfma_f32_16x16x32_bf16 v[148:151], a[8:11], a[28:31], v[148:151]\n\t"
  "ds_read_b64_tr_b16 a[54:55], v31 offset:43008\n\t"
  "s_add_u32 m0, s46, 77824\n\t"
  "s_add_u32 s41, s53, 204800\n\t"
  "buffer_load_dwordx4 v22, s[24:27], s41 offen offset:0 lds\n\t"
  "v_mfma_f32_16x16x32_bf16 v[152:155], a[8:11], a[32:35], v[152:155]\n\t"
  "v_mfma_f32_16x16x32_bf16 v[156:159], a[8:11], a[36:39], v[156:159]\n\t"
  "v_mfma_f32_16x16x32_bf16 v[160:163], a[12:15], a[20:23], v[160:163]\n\t"
  "ds_read_b64_tr_b16 a[56:57], v32 offset:36864\n\t"
  "v_mfma_f32_16x16x32_bf16 v[164:167], a[12:15], a[24:27], v[164:167]\n\t"
  "ds_read_b64_tr_b16 a[58:59], v32 offset:43008\n\t"
  "buffer_load_dwordx4 v23, s[24:27], s41 offen offset:1024 lds\n\t"
  "v_mfma_f32_16x16x32_bf16 v[168:171], a[12:15], a[28:31], v[168:171]\n\t"
  "v_mfma_f32_16x16x32_bf16 v[172:175], a[12:15], a[32:35], v[172:175]\n\t"
  "buffer_load_dwordx4 v24, s[24:27], s41 offen offset:2048 lds\n\t"
  "v_mfma_f32_16x16x32_bf16 v[176:179], a[12:15], a[36:39], v[176:179]\n\t"
  "v_mfma_f32_16x16x32_bf16 v[180:183], a[16:19], a[20:23], v[180:183]\n\t"
  "v_mfma_f32_16x16x32_bf16 v[184:187], a[16:19], a[24:27], v[184:187]\n\t"
  "s_waitcnt vmcnt(38)\n\t"
  "s_cmp_lg_u32 %7, 0\n\t"
  "s_cbranch_scc1 76f\n\t"
  "s_mov_b64 exec, 15\n\t"
  "global_store_dword v69, v74, %13 sc1\n\t"
  "s_mov_b64 exec, -1\n\t"
  "76:\n\t"
  "v_mfma_f32_16x16x32_bf16 v[188:191], a[16:19], a[28:31], v[188:191]\n\t"
  "v_mfma_f32_16x16x32_bf16 v[192:195], a[16:19], a[32:35], v[192:195]\n\t"
  "v_mfma_f32_16x16x32_bf16 v[196:199], a[16:19], a[36:39], v[196:199]\n\t"
  "s_waitcnt vmcnt(11) lgkmcnt(0)\n\t"
  "s_barrier\n\t"
  "v_mfma_f32_16x16x32_bf16 v[100:103], a[40:43], a[60:63], v[100:103]\n\t"
  "s_add_u32 m0, s45, 0\n\t"
  "s_add_u32 s40, s58, 0\n\t"
  "buffer_load_dwordx4 v20, s[20:23], s40 offen offset:0 sc1 lds\n\t"
  "v_mfma_f32_16x16x32_bf16 v[104:107], a[40:43], a[64:67], v[104:107]\n\t"
  "v_mfma_f32_16x16x32_bf16 v[108:111], a[40:43], a[68:71], v[108:111]\n\t"
  "ds_read_b128 a[20:23], v25 offset:20480\n\t"
  "v_mfma_f32_16x16x32_bf16 v[112:115], a[40:43], a[72:75], v[112:115]\n\t"
  "ds_read_b128 a[24:27], v25 offset:21504\n\t"
  "ds_read_b128 a[28:31], v25 offset:22528\n\t"
  "v_mfma_f32_16x16x32_bf16 v[116:119], a[40:43], a[76:79], v[116:119]\n\t"
  "ds_read_b128 a[32:35], v25 offset:23552\n\t"
  "ds_read_b128 a[36:39], v25 offset:24576\n\t"
  "v_mfma_f32_16x16x32_bf16 v[120:123], a[44:47], a[60:63], v[120:123]\n\t"
  "ds_read_b64_tr_b16 a[0:1], v28 offset:49152\n\t"
  "ds_read_b64_tr_b16 a[2:3], v28 offset:55296\n\t"
  "v_mfma_f32_16x16x32_bf16 v[124:127], a[44:47], a[64:67], v[124:127]\n\t"
  "buffer_load_dwordx4 v20, s[20:23], s40 offen offset:1024 sc1 lds\n\t"
  "v_mfma_f32_16x16x32_bf16 v[128:131], a[44:47], a[68:71], v[128:131]\n\t"
  "v_mfma_f32_16x16x32_bf16 v[132:135], a[44:47], a[72:75], v[132:135]\n\t"
  "ds_read_b64_tr_b16 a[4:5], v29 offset:49152\n\t"
  "ds_read_b64_tr_b16 a[6:7], v29 offset:55296\n\t"
  "v_mfma_f32_16x16x32_bf16 v[136:139], a[44:47], a[76:79], v[136:139]\n\t"
  "ds_read_b64_tr_b16 a[8:9], v30 offset:49152\n\t"
  "ds_read_b64_tr_b16 a[10:11], v30 offset:55296\n\t"
  "v_mfma_f32_16x16x32_bf16 v[140:143], a[48:51], a[60:63], v[140:143]\n\t"
  "ds_read_b64_tr_b16 a[12:13], v31 offset:49152\n\t"
  "buffer_load_dwordx4 v20, s[20:23], s40 offen offset:2048 sc1 lds\n\t"
  "v_mfma_f32_16x16x32_bf16 v[144:147], a[48:51], a[64:67], v[144:147]\n\t"
  "v_mfma_f32_16x16x32_bf16 v[148:151], a[48:51], a[68:71], v[148:151]\n\t"
  "ds_read_b64_tr_b16 a[14:15], v31 offset:55296\n\t"
  "ds_read_b64_tr_b16 a[16:17], v32 offset:49152\n\t"
  "v_mfma_f32_16x16x32_bf16 v[152:155], a[48:51], a[72:75], v[152:155]\n\t"
  "ds_read_b64_tr_b16 a[18:19], v32 offset:55296\n\t"
  "buffer_load_dwordx4 v20, s[20:23], s40 offen offset:3072 sc1 lds\n\t"
  "v_mfma_f32_16x16x32_bf16 v[156:159], a[48:51], a[76:79], v[156:159]\n\t"
  "v_mfma_f32_16x16x32_bf16 v[160:163], a[52:55], a[60:63], v[160:163]\n\t"
  "v_mfma_f32_16x16x32_bf16 v[164:167], a[52:55], a[64:67], v[164:167]\n\t"
  "s_add_u32 m0, s45, 4096\n\t"
  "s_nop 0\n\t"
  "buffer_load_dwordx4 v21, s[20:23], s40 offen offset:0 sc1 lds\n\t"
  "v_mfma_f32_16x16x32_bf16 v[168:171], a[52:55], a[68:71], v[168:171]\n\t"
  "v_mfma_f32_16x16x32_bf16 v[172:175], a[52:55], a[72:75], v[172:175]\n\t"
  "v_mfma_f32_16x16x32_bf16 v[176:179], a[52:55], a[76:79], v[176:179]\n\t"
  "v_mfma_f32_16x16x32_bf16 v[180:183], a[56:59], a[60:63], v[180:183]\n\t"
  "v_mfma_f32_16x16x32_bf16 v[184:187], a[56:59], a[64:67], v[184:187]\n\t"
  "v_mfma_f32_16x16x32_bf16 v[188:191], a[56:59], a[68:71], v[188:191]\n\t"
  "v_mfma_f32_16x16x32_bf16 v[192:195], a[56:59], a[72:75], v[192:195]\n\t"
  "v_mfma_f32_16x16x32_bf16 v[196:199], a[56:59], a[76:79], v[196:199]\n\t"
  "s_waitcnt lgkmcnt(0)\n\t"
  "v_mfma_f32_16x16x32_bf16 v[100:103], a[0:3], a[20:23], v[100:103]\n\t"
  "s_add_u32 m0, s46, 90112\n\t"
  "s_add_u32 s41, s53, 245760\n\t"
  "buffer_load_dwordx4 v22, s[24:27], s41 offen offset:0 lds\n\t"
  "v_mfma_f32_16x16x32_bf16 v[104:107], a[0:3], a[24:27], v[104:107]\n\t"
  "v_mfma_f32_16x16x32_bf16 v[108:111], a[0:3], a[28:31], v[108:111]\n\t"
  "ds_read_b128 a[60:63], v25 offset:25600\n\t"
  "v_mfma_f32_16x16x32_bf16 v[112:115], a[0:3], a[32:35], v[112:115]\n\t"
  "ds_read_b128 a[64:67], v25 offset:26624\n\t"
  "ds_read_b128 a[68:71], v25 offset:27648\n\t"
  "v_mfma_f32_16x16x32_bf16 v[116:119], a[0:3], a[36:39], v[116:119]\n\t"
  "ds_read_b128 a[72:75], v25 offset:28672\n\t"
  "ds_read_b128 a[76:79], v25 offset:29696\n\t"
  "v_mfma_f32_16x16x32_bf16 v[120:123], a[4:7], a[20:23], v[120:123]\n\t"
  "ds_read_b64_tr_b16 a[40:41], v28 offset:61440\n\t"
  "buffer_load_dwordx4 v23, s[24:27], s41 offen offset:1024 lds\n\t"
  "v_mfma_f32_16x16x32_bf16 v[124:127], a[4:7], a[24:27], v[124:127]\n\t"
  "v_mfma_f32_16x16x32_bf16 v[128:131], a[4:7], a[28:31], v[128:131]\n\t"
  "ds_read_b64_tr_b16 a[42:43], v33 offset:18432\n\t"
  "v_mfma_f32_16x16x32_bf16 v[132:135], a[4:7], a[32:35], v[132:135]\n\t"
  "ds_read_b64_tr_b16 a[44:45], v29 offset:61440\n\t"
  "ds_read_b64_tr_b16 a[46:47], v34 offset:18432\n\t"
  "v_mfma_f32_16x16x32_bf16 v[136:139], a[4:7], a[36:39], v[136:139]\n\t"
  "ds_read_b64_tr_b16 a[48:49], v30 offset:61440\n\t"
  "buffer_load_dwordx4 v24, s[24:27], s41 offen offset:2048 lds\n\t"
  "v_mfma_f32_16x16x32_bf16 v[140:143], a[8:11], a[20:23], v[140:143]\n\t"
  "v_mfma_f32_16x16x32_bf16 v[144:147], a[8:11], a[24:27], v[144:147]\n\t"
  "ds_read_b64_tr_b16 a[50:51], v35 offset:18432\n\t"
  "ds_read_b64_tr_b16 a[52:53], v31 offset:61440\n\t"
  "v_mfma_f32_16x16x32_bf16 v[148:151], a[8:11], a[28:31], v[148:151]\n\t"
  "ds_read_b64_tr_b16 a[54:55], v36 offset:18432\n\t"
  "ds_read_b64_tr_b16 a[56:57], v32 offset:61440\n\t"
  "v_mfma_f32_16x16x32_bf16 v[152:155], a[8:11], a[32:35], v[152:155]\n\t"
  "s_add_u32 m0, s46, 102400\n\t"
  "s_add_u32 s41, s53, 286720\n\t"
  "buffer_load_dwordx4 v22, s[24:27], s41 offen offset:0 lds\n\t"
  "v_mfma_f32_16x16x32_bf16 v[156:159], a[8:11], a[36:39], v[156:159]\n\t"
  "v_mfma_f32_16x16x32_bf16 v[160:163], a[12:15], a[20:23], v[160:163]\n\t"
  "v_mfma_f32_16x16x32_bf16 v[164:167], a[12:15], a[24:27], v[164:167]\n\t"
  "ds_read_b64_tr_b16 a[58:59], v37 offset:18432\n\t"
  "buffer_load_dwordx4 v23, s[24:27], s41 offen offset:1024 lds\n\t"
  "v_mfma_f32_16x16x32_bf16 v[168:171], a[12:15], a[28:31], v[168:171]\n\t"
  "v_mfma_f32_16x16x32_bf16 v[172:175], a[12:15], a[32:35], v[172:175]\n\t"
  "buffer_load_dwordx4 v24, s[24:27], s41 offen offset:2048 lds\n\t"
  "v_mfma_f32_16x16x32_bf16 v[176:179], a[12:15], a[36:39], v[176:179]\n\t"
  "v_mfma_f32_16x16x32_bf16 v[180:183], a[16:19], a[20:23], v[180:183]\n\t"
  "v_mfma_f32_16x16x32_bf16 v[184:187], a[16:19], a[24:27], v[184:187]\n\t"
  "v_mfma_f32_16x16x32_bf16 v[188:191], a[16:19], a[28:31], v[188:191]\n\t"
  "v_mfma_f32_16x16x32_bf16 v[192:195], a[16:19], a[32:35], v[192:195]\n\t"
  "v_mfma_f32_16x16x32_bf16 v[196:199], a[16:19], a[36:39], v[196:199]\n\t"
  "s_waitcnt vmcnt(11) lgkmcnt(0)\n\t"
  "s_barrier\n\t"
  "v_mfma_f32_16x16x32_bf16 v[100:103], a[40:43], a[60:63], v[100:103]\n\t"
  "s_add_u32 m0, s45, 20480\n\t"
  "s_add_u32 s40, s58, 10240\n\t"
  "buffer_load_dwordx4 v20, s[20:23], s40 offen offset:0 sc1 lds\n\t"
  "v_mfma_f32_16x16x32_bf16 v[104:107], a[40:43], a[64:67], v[104:107]\n\t"
  "v_mfma_f32_16x16x32_bf16 v[108:111], a[40:43], a[68:71], v[108:111]\n\t"
  "ds_read_b128 a[20:23], v25 offset:40960\n\t"
  "v_mfma_f32_16x16x32_bf16 v[112:115], a[40:43], a[72:75], v[112:115]\n\t"
  "ds_read_b128 a[24:27], v25 offset:41984\n\t"
  "ds_read_b128 a[28:31], v25 offset:43008\n\t"
  "v_mfma_f32_16x16x32_bf16 v[116:119], a[40:43], a[76:79], v[116:119]\n\t"
  "ds_read_b128 a[32:35], v25 offset:44032\n\t"
  "ds_read_b128 a[36:39], v25 offset:45056\n\t"
  "v_mfma_f32_16x16x32_bf16 v[120:123], a[44:47], a[60:63], v[120:123]\n\t"
  "ds_read_b64_tr_b16 a[0:1], v28 offset:0\n\t"
  "ds_read_b64_tr_b16 a[2:3], v28 offset:6144\n\t"
  "v_mfma_f32_16x16x32_bf16 v[124:127], a[44:47], a[64:67], v[124:127]\n\t"
  "buffer_load_dwordx4 v20, s[20:23], s40 offen offset:1024 sc1 lds\n\t"
  "v_mfma_f32_16x16x32_bf16 v[128:131], a[44:47], a[68:71], v[128:131]\n\t"
  "v_mfma_f32_16x16x32_bf16 v[132:135], a[44:47], a[72:75], v[132:135]\n\t"
  "ds_read_b64_tr_b16 a[4:5], v29 offset:0\n\t"
  "ds_read_b64_tr_b16 a[6:7], v29 offset:6144\n\t"
  "v_mfma_f32_16x16x32_bf16 v[136:139], a[44:47], a[76:79], v[136:139]\n\t"
  "ds_read_b64_tr_b16 a[8:9], v30 offset:0\n\t"
  "ds_read_b64_tr_b16 a[10:11], v30 offset:6144\n\t"
  "v_mfma_f32_16x16x32_bf16 v[140:143], a[48:51], a[60:63], v[140:143]\n\t"
  "ds_read_b64_tr_b16 a[12:13], v31 offset:0\n\t"
  "buffer_load_dwordx4 v20, s[20:23], s40 offen offset:2048 sc1 lds\n\t"
  "v_mfma_f32_16x16x32_bf16 v[144:147], a[48:51], a[64:67], v[144:147]\n\t"
  "v_mfma_f32_16x16x32_bf16 v[148:151], a[48:51], a[68:71], v[148:151]\n\t"
  "ds_read_b64_tr_b16 a[14:15], v31 offset:6144\n\t"
  "ds_read_b64_tr_b16 a[16:17], v32 offset:0\n\t"
  "v_mfma_f32_16x16x32_bf16 v[152:155], a[48:51], a[72:75], v[152:155]\n\t"
  "ds_read_b64_tr_b16 a[18:19], v32 offset:6144\n\t"
  "buffer_load_dwordx4 v20, s[20:23], s40 offen offset:3072 sc1 lds\n\t"
  "v_mfma_f32_16x16x32_bf16 v[156:159], a[48:51], a[76:79], v[156:159]\n\t"
  "v_mfma_f32_16x16x32_bf16 v[160:163], a[52:55], a[60:63], v[160:163]\n\t"
  "v_mfma_f32_16x16x32_bf16 v[164:167], a[52:55], a[64:67], v[164:167]\n\t"
  "s_add_u32 m0, s45, 24576\n\t"
  "s_nop 0\n\t"
  "buffer_load_dwordx4 v21, s[20:23], s40 offen offset:0 sc1 lds\n\t"
  "v_mfma_f32_16x16x32_bf16 v[168:171], a[52:55], a[68:71], v[168:171]\n\t"
  "v_mfma_f32_16x16x32_bf16 v[172:175], a[52:55], a[72:75], v[172:175]\n\t"
  "v_mfma_f32_16x16x32_bf16 v[176:179], a[52:55], a[76:79], v[176:179]\n\t"
  "v_mfma_f32_16x16x32_bf16 v[180:183], a[56:59], a[60:63], v[180:183]\n\t"
  "v_mfma_f32_16x16x32_bf16 v[184:187], a[56:59], a[64:67], v[184:187]\n\t"
  "v_mfma_f32_16x16x32_bf16 v[188:191], a[56:59], a[68:71], v[188:191]\n\t"
  "v_mfma_f32_16x16x32_bf16 v[192:195], a[56:59], a[72:75], v[192:195]\n\t"
  "v_mfma_f32_16x16x32_bf16 v[196:199], a[56:59], a[76:79], v[196:199]\n\t"
  "s_waitcnt lgkmcnt(0)\n\t"
  "v_mfma_f32_16x16x32_bf16 v[100:103], a[0:3], a[20:23], v[100:103]\n\t"
  "s_add_u32 m0, s46, 114688\n\t"
  "s_add_u32 s41, s54, 0\n\t"
  "buffer_load_dwordx4 v22, s[24:27], s41 offen offset:0 lds\n\t"
  "v_mfma_f32_16x16x32_bf16 v[104:107], a[0:3], a[24:27], v[104:107]\n\t"
  "v_mfma_f32_16x16x32_bf16 v[108:111], a[0:3], a[28:31], v[108:111]\n\t"
  "ds_read_b128 a[60:63], v25 offset:46080\n\t"
  "v_mfma_f32_16x16x32_bf16 v[112:115], a[0:3], a[32:35], v[112:115]\n\t"
  "ds_read_b128 a[64:67], v25 offset:47104\n\t"
  "ds_read_b128 a[68:71], v25 offset:48128\n\t"
  "v_mfma_f32_16x16x32_bf16 v[116:119], a[0:3], a[36:39], v[116:119]\n\t"
  "ds_read_b128 a[72:75], v25 offset:49152\n\t"
  "ds_read_b128 a[76:79], v25 offset:50176\n\t"
  "v_mfma_f32_16x16x32_bf16 v[120:123], a[4:7], a[20:23], v[120:123]\n\t"
  "ds_read_b64_tr_b16 a[40:41], v28 offset:12288\n\t"
  "buffer_load_dwordx4 v23, s[24:27], s41 offen offset:1024 lds\n\t"
  "v_mfma_f32_16x16x32_bf16 v[124:127], a[4:7], a[24:27], v[124:127]\n\t"
  "v_mfma_f32_16x16x32_bf16 v[128:131], a[4:7], a[28:31], v[128:131]\n\t"
  "ds_read_b64_tr_b16 a[42:43], v28 offset:18432\n\t"
  "v_mfma_f32_16x16x32_bf16 v[132:135], a[4:7], a[32:35], v[132:135]\n\t"
  "ds_read_b64_tr_b16 a[44:45], v29 offset:12288\n\t"
  "ds_read_b64_tr_b16 a[46:47], v29 offset:18432\n\t"
  "v_mfma_f32_16x16x32_bf16 v[136:139], a[4:7], a[36:39], v[136:139]\n\t"
  "ds_read_b64_tr_b16 a[48:49], v30 offset:12288\n\t"
  "buffer_load_dwordx4 v24, s[24:27], s41 offen offset:2048 lds\n\t"
  "v_mfma_f32_16x16x32_bf16 v[140:143], a[8:11], a[20:23], v[140:143]\n\t"
  "v_mfma_f32_16x16x32_bf16 v[144:147], a[8:11], a[24:27], v[144:147]\n\t"
  "ds_read_b64_tr_b16 a[50:51], v30 offset:18432\n\t"
  "ds_read_b64_tr_b16 a[52:53], v31 offset:12288\n\t"
  "v_mfma_f32_16x16x32_bf16 v[148:151], a[8:11], a[28:31], v[148:151]\n\t"
  "ds_read_b64_tr_b16 a[54:55], v31 offset:18432\n\t"
  "ds_read_b64_tr_b16 a[56:57], v32 offset:12288\n\t"
  "v_mfma_f32_16x16x32_bf16 v[152:155], a[8:11], a[32:35], v[152:155]\n\t"
  "s_add_u32 m0, s46, 126976\n\t"
  "s_add_u32 s41, s54, 40960\n\t"
  "buffer_load_dwordx4 v22, s[24:27], s41 offen offset:0 lds\n\t"
  "v_mfma_f32_16x16x32_bf16 v[156:159], a[8:11], a[36:39], v[156:159]\n\t"
  "v_mfma_f32_16x16x32_bf16 v[160:163], a[12:15], a[20:23], v[160:163]\n\t"
  "v_mfma_f32_16x16x32_bf16 v[164:167], a[12:15], a[24:27], v[164:167]\n\t"
  "ds_read_b64_tr_b16 a[58:59], v32 offset:18432\n\t"
  "buffer_load_dwordx4 v23, s[24:27], s41 offen offset:1024 lds\n\t"
  "v_mfma_f32_16x16x32_bf16 v[168:171], a[12:15], a[28:31], v[168:171]\n\t"
  "v_mfma_f32_16x16x32_bf16 v[172:175], a[12:15], a[32:35], v[172:175]\n\t"
  "buffer_load_dwordx4 v24, s[24:27], s41 offen offset:2048 lds\n\t"
  "v_mfma_f32_16x16x32_bf16 v[176:179], a[12:15], a[36:39], v[176:179]\n\t"
  "v_mfma_f32_16x16x32_bf16 v[180:183], a[16:19], a[20:23], v[180:183]\n\t"
  "v_mfma_f32_16x16x32_bf16 v[184:187], a[16:19], a[24:27], v[184:187]\n\t"
  "v_mfma_f32_16x16x32_bf16 v[188:191], a[16:19], a[28:31], v[188:191]\n\t"
  "v_mfma_f32_16x16x32_bf16 v[192:195], a[16:19], a[32:35], v[192:195]\n\t"
  "v_mfma_f32_16x16x32_bf16 v[196:199], a[16:19], a[36:39], v[196:199]\n\t"
  "s_waitcnt vmcnt(11) lgkmcnt(0)\n\t"
  "s_barrier\n\t"
  "v_mfma_f32_16x16x32_bf16 v[100:103], a[40:43], a[60:63], v[100:103]\n\t"
  "s_add_u32 m0, s45, 40960\n\t"
  "s_add_u32 s40, s58, 20480\n\t"
  "buffer_load_dwordx4 v20, s[20:23], s40 offen offset:0 sc1 lds\n\t"
  "v_mfma_f32_16x16x32_bf16 v[104:107], a[40:43], a[64:67], v[104:107]\n\t"
  "v_mfma_f32_16x16x32_bf16 v[108:111], a[40:43], a[68:71], v[108:111]\n\t"
  "ds_read_b128 a[20:23], v26 offset:0\n\t"
  "v_mfma_f32_16x16x32_bf16 v[112:115], a[40:43], a[72:75], v[112:115]\n\t"
  "ds_read_b128 a[24:27], v26 offset:1024\n\t"
  "ds_read_b128 a[28:31], v26 offset:2048\n\t"
  "v_mfma_f32_16x16x32_bf16 v[116:119], a[40:43], a[76:79], v[116:119]\n\t"
  "ds_read_b128 a[32:35], v26 offset:3072\n\t"
  "ds_read_b128 a[36:39], v26 offset:4096\n\t"
  "v_mfma_f32_16x16x32_bf16 v[120:123], a[44:47], a[60:63], v[120:123]\n\t"
  "ds_read_b64_tr_b16 a[0:1], v28 offset:24576\n\t"
  "ds_read_b64_tr_b16 a[2:3], v28 offset:30720\n\t"
  "v_mfma_f32_16x16x32_bf16 v[124:127], a[44:47], a[64:67], v[124:127]\n\t"
  "buffer_load_dwordx4 v20, s[20:23], s40 offen offset:1024 sc1 lds\n\t"
  "v_mfma_f32_16x16x32_bf16 v[128:131], a[44:47], a[68:71], v[128:131]\n\t"
  "v_mfma_f32_16x16x32_bf16 v[132:135], a[44:47], a[72:75], v[132:135]\n\t"
  "ds_read_b64_tr_b16 a[4:5], v29 offset:24576\n\t"
  "ds_read_b64_tr_b16 a[6:7], v29 offset:30720\n\t"
  "v_mfma_f32_16x16x32_bf16 v[136:139], a[44:47], a[76:79], v[136:139]\n\t"
  "ds_read_b64_tr_b16 a[8:9], v30 offset:24576\n\t"
  "ds_read_b64_tr_b16 a[10:11], v30 offset:30720\n\t"
  "v_mfma_f32_16x16x32_bf16 v[140:143], a[48:51], a[60:63], v[140:143]\n\t"
  "ds_read_b64_tr_b16 a[12:13], v31 offset:24576\n\t"
  "buffer_load_dwordx4 v20, s[20:23], s40 offen offset:2048 sc1 lds\n\t"
  "v_mfma_f32_16x16x32_bf16 v[144:147], a[48:51], a[64:67], v[144:147]\n\t"
  "v_mfma_f32_16x16x32_bf16 v[148:151], a[48:51], a[68:71], v[148:151]\n\t"
  "ds_read_b64_tr_b16 a[14:15], v31 offset:30720\n\t"
  "ds_read_b64_tr_b16 a[16:17], v32 offset:24576\n\t"
  "v_mfma_f32_16x16x32_bf16 v[152:155], a[48:51], a[72:75], v[152:155]\n\t"
  "ds_read_b64_tr_b16 a[18:19], v32 offset:30720\n\t"
  "buffer_load_dwordx4 v20, s[20:23], s40 offen offset:3072 sc1 lds\n\t"
  "v_mfma_f32_16x16x32_bf16 v[156:159], a[48:51], a[76:79], v[156:159]\n\t"
  "v_mfma_f32_16x16x32_bf16 v[160:163], a[52:55], a[60:63], v[160:163]\n\t"
  "v_mfma_f32_16x16x32_bf16 v[164:167], a[52:55], a[64:67], v[164:167]\n\t"
  "s_add_u32 m0, s45, 45056\n\t"
  "s_nop 0\n\t"
  "buffer_load_dwordx4 v21, s[20:23], s40 offen offset:0 sc1 lds\n\t"
  "v_mfma_f32_16x16x32_bf16 v[168:171], a[52:55], a[68:71], v[168:171]\n\t"
  "v_mfma_f32_16x16x32_bf16 v[172:175], a[52:55], a[72:75], v[172:175]\n\t"
  "v_mfma_f32_16x16x32_bf16 v[176:179], a[52:55], a[76:79], v[176:179]\n\t"
  "v_mfma_f32_16x16x32_bf16 v[180:183], a[56:59], a[60:63], v[180:183]\n\t"
  "v_mfma_f32_16x16x32_bf16 v[184:187], a[56:59], a[64:67], v[184:187]\n\t"
  "v_mfma_f32_16x16x32_bf16 v[188:191], a[56:59], a[68:71], v[188:191]\n\t"
  "v_mfma_f32_16x16x32_bf16 v[192:195], a[56:59], a[72:75], v[192:195]\n\t"
  "v_mfma_f32_16x16x32_bf16 v[196:199], a[56:59], a[76:79], v[196:199]\n\t"
  "s_waitcnt lgkmcnt(0)\n\t"
  "v_mfma_f32_16x16x32_bf16 v[100:103], a[0:3], a[20:23], v[100:103]\n\t"
  "s_add_u32 m0, s46, 65536\n\t"
  "s_add_u32 s41, s54, 81920\n\t"
  "buffer_load_dwordx4 v22, s[24:27], s41 offen offset:0 lds\n\t"
  "v_mfma_f32_16x16x32_bf16 v[104:107], a[0:3], a[24:27], v[104:107]\n\t"
  "v_mfma_f32_16x16x32_bf16 v[108:111], a[0:3], a[28:31], v[108:111]\n\t"
  "ds_read_b128 a[60:63], v26 offset:5120\n\t"
  "v_mfma_f32_16x16x32_bf16 v[112:115], a[0:3], a[32:35], v[112:115]\n\t"
  "ds_read_b128 a[64:67], v26 offset:6144\n\t"
  "ds_read_b128 a[68:71], v26 offset:7168\n\t"
  "v_mfma_f32_16x16x32_bf16 v[116:119], a[0:3], a[36:39], v[116:119]\n\t"
  "ds_read_b128 a[72:75], v26 offset:8192\n\t"
  "ds_read_b128 a[76:79], v26 offset:9216\n\t"
  "v_mfma_f32_16x16x32_bf16 v[120:123], a[4:7], a[20:23], v[120:123]\n\t"
  "ds_read_b64_tr_b16 a[40:41], v28 offset:36864\n\t"
  "buffer_load_dwordx4 v23, s[24:27], s41 offen offset:1024 lds\n\t"
  "v_mfma_f32_16x16x32_bf16 v[124:127], a[4:7], a[24:27], v[124:127]\n\t"
  "v_mfma_f32_16x16x32_bf16 v[128:131], a[4:7], a[28:31], v[128:131]\n\t"
  "ds_read_b64_tr_b16 a[42:43], v28 offset:43008\n\t"
  "v_mfma_f32_16x16x32_bf16 v[132:135], a[4:7], a[32:35], v[132:135]\n\t"
  "ds_read_b64_tr_b16 a[44:45], v29 offset:36864\n\t"
  "ds_read_b64_tr_b16 a[46:47], v29 offset:43008\n\t"
  "v_mfma_f32_16x16x32_bf16 v[136:139], a[4:7], a[36:39], v[136:139]\n\t"
  "ds_read_b64_tr_b16 a[48:49], v30 offset:36864\n\t"
  "buffer_load_dwordx4 v24, s[24:27], s41 offen offset:2048 lds\n\t"
  "v_mfma_f32_16x16x32_bf16 v[140:143], a[8:11], a[20:23], v[140:143]\n\t"
  "v_mfma_f32_16x16x32_bf16 v[144:147], a[8:11], a[24:27], v[144:147]\n\t"
  "ds_read_b64_tr_b16 a[50:51], v30 offset:43008\n\t"
  "ds_read_b64_tr_b16 a[52:53], v31 offset:36864\n\t"
  "v_mfma_f32_16x16x32_bf16 v[148:151], a[8:11], a[28:31], v[148:151]\n\t"
  "ds_read_b64_tr_b16 a[54:55], v31 offset:43008\n\t"
  "ds_read_b64_tr_b16 a[56:57], v32 offset:36864\n\t"
  "v_mfma_f32_16x16x32_bf16 v[152:155], a[8:11], a[32:35], v[152:155]\n\t"
  "s_add_u32 m0, s46, 77824\n\t"
  "s_add_u32 s41, s54, 122880\n\t"
  "buffer_load_dwordx4 v22, s[24:27], s41 offen offset:0 lds\n\t"
  "v_mfma_f32_16x16x32_bf16 v[156:159], a[8:11], a[36:39], v[156:159]\n\t"
  "v_mfma_f32_16x16x32_bf16 v[160:163], a[12:15], a[20:23], v[160:163]\n\t"
  "v_mfma_f32_16x16x32_bf16 v[164:167], a[12:15], a[24:27], v[164:167]\n\t"
  "ds_read_b64_tr_b16 a[58:59], v32 offset:43008\n\t"
  "buffer_load_dwordx4 v23, s[24:27], s41 offen offset:1024 lds\n\t"
  "v_mfma_f32_16x16x32_bf16 v[168:171], a[12:15], a[28:31], v[168:171]\n\t"
  "v_mfma_f32_16x16x32_bf16 v[172:175], a[12:15], a[32:35], v[172:175]\n\t"
  "buffer_load_dwordx4 v24, s[24:27], s41 offen offset:2048 lds\n\t"
  "v_mfma_f32_16x16x32_bf16 v[176:179], a[12:15], a[36:39], v[176:179]\n\t"
  "v_mfma_f32_16x16x32_bf16 v[180:183], a[16:19], a[20:23], v[180:183]\n\t"
  "v_mfma_f32_16x16x32_bf16 v[184:187], a[16:19], a[24:27], v[184:187]\n\t"
  "v_mfma_f32_16x16x32_bf16 v[188:191], a[16:19], a[28:31], v[188:191]\n\t"
  "v_mfma_f32_16x16x32_bf16 v[192:195], a[16:19], a[32:35], v[192:195]\n\t"
  "v_mfma_f32_16x16x32_bf16 v[196:199], a[16:19], a[36:39], v[196:199]\n\t"
  "s_waitcnt vmcnt(11) lgkmcnt(0)\n\t"
  "s_barrier\n\t"
  "v_mfma_f32_16x16x32_bf16 v[100:103], a[40:43], a[60:63], v[100:103]\n\t"
  "s_add_u32 m0, s45, 139264\n\t"
  "s_add_u32 s40, s58, 30720\n\t"
  "buffer_load_dwordx4 v20, s[20:23], s40 offen offset:0 sc1 lds\n\t"
  "v_mfma_f32_16x16x32_bf16 v[104:107], a[40:43], a[64:67], v[104:107]\n\t"
  "v_mfma_f32_16x16x32_bf16 v[108:111], a[40:43], a[68:71], v[108:111]\n\t"
  "ds_read_b128 a[20:23], v25 offset:0\n\t"
  "v_mfma_f32_16x16x32_bf16 v[112:115], a[40:43], a[72:75], v[112:115]\n\t"
  "ds_read_b128 a[24:27], v25 offset:1024\n\t"
  "ds_read_b128 a[28:31], v25 offset:2048\n\t"
  "v_mfma_f32_16x16x32_bf16 v[116:119], a[40:43], a[76:79], v[116:119]\n\t"
  "ds_read_b128 a[32:35], v25 offset:3072\n\t"
  "ds_read_b128 a[36:39], v25 offset:4096\n\t"
  "v_mfma_f32_16x16x32_bf16 v[120:123], a[44:47], a[60:63], v[120:123]\n\t"
  "ds_read_b64_tr_b16 a[0:1], v28 offset:49152\n\t"
  "ds_read_b64_tr_b16 a[2:3], v28 offset:55296\n\t"
  "v_mfma_f32_16x16x32_bf16 v[124:127], a[44:47], a[64:67], v[124:127]\n\t"
  "buffer_load_dwordx4 v20, s[20:23], s40 offen offset:1024 sc1 lds\n\t"
  "v_mfma_f32_16x16x32_bf16 v[128:131], a[44:47], a[68:71], v[128:131]\n\t"
  "v_mfma_f32_16x16x32_bf16 v[132:135], a[44:47], a[72:75], v[132:135]\n\t"
  "ds_read_b64_tr_b16 a[4:5], v29 offset:49152\n\t"
  "ds_read_b64_tr_b16 a[6:7], v29 offset:55296\n\t"
  "v_mfma_f32_16x16x32_bf16 v[136:139], a[44:47], a[76:79], v[136:139]\n\t"
  "ds_read_b64_tr_b16 a[8:9], v30 offset:49152\n\t"
  "ds_read_b64_tr_b16 a[10:11], v30 offset:55296\n\t"
  "v_mfma_f32_16x16x32_bf16 v[140:143], a[48:51], a[60:63], v[140:143]\n\t"
  "ds_read_b64_tr_b16 a[12:13], v31 offset:49152\n\t"
  "buffer_load_dwordx4 v20, s[20:23], s40 offen offset:2048 sc1 lds\n\t"
  "v_mfma_f32_16x16x32_bf16 v[144:147], a[48:51], a[64:67], v[144:147]\n\t"
  "v_mfma_f32_16x16x32_bf16 v[148:151], a[48:51], a[68:71], v[148:151]\n\t"
  "ds_read_b64_tr_b16 a[14:15], v31 offset:55296\n\t"
  "ds_read_b64_tr_b16 a[16:17], v32 offset:49152\n\t"
  "v_mfma_f32_16x16x32_bf16 v[152:155], a[48:51], a[72:75], v[152:155]\n\t"
  "ds_read_b64_tr_b16 a[18:19], v32 offset:55296\n\t"
  "buffer_load_dwordx4 v20, s[20:23], s40 offen offset:3072 sc1 lds\n\t"
  "v_mfma_f32_16x16x32_bf16 v[156:159], a[48:51], a[76:79], v[156:159]\n\t"
  "v_mfma_f32_16x16x32_bf16 v[160:163], a[52:55], a[60:63], v[160:163]\n\t"
  "v_mfma_f32_16x16x32_bf16 v[164:167], a[52:55], a[64:67], v[164:167]\n\t"
  "s_add_u32 m0, s45, 143360\n\t"
  "s_nop 0\n\t"
  "buffer_load_dwordx4 v21, s[20:23], s40 offen offset:0 sc1 lds\n\t"
  "v_mfma_f32_16x16x32_bf16 v[168:171], a[52:55], a[68:71], v[168:171]\n\t"
  "v_mfma_f32_16x16x32_bf16 v[172:175], a[52:55], a[72:75], v[172:175]\n\t"
  "v_mfma_f32_16x16x32_bf16 v[176:179], a[52:55], a[76:79], v[176:179]\n\t"
  "v_mfma_f32_16x16x32_bf16 v[180:183], a[56:59], a[60:63], v[180:183]\n\t"
  "v_mfma_f32_16x16x32_bf16 v[184:187], a[56:59], a[64:67], v[184:187]\n\t"
  "v_mfma_f32_16x16x32_bf16 v[188:191], a[56:59], a[68:71], v[188:191]\n\t"
  "v_mfma_f32_16x16x32_bf16 v[192:195], a[56:59], a[72:75], v[192:195]\n\t"
  "v_mfma_f32_16x16x32_bf16 v[196:199], a[56:59], a[76:79], v[196:199]\n\t"
  "s_waitcnt lgkmcnt(0)\n\t"
  "v_mfma_f32_16x16x32_bf16 v[100:103], a[0:3], a[20:23], v[100:103]\n\t"
  "s_add_u32 m0, s46, 90112\n\t"
  "s_add_u32 s41, s54, 163840\n\t"
  "buffer_load_dwordx4 v22, s[24:27], s41 offen offset:0 lds\n\t"
  "v_mfma_f32_16x16x32_bf16 v[104:107], a[0:3], a[24:27], v[104:107]\n\t"
  "v_mfma_f32_16x16x32_bf16 v[108:111], a[0:3], a[28:31], v[108:111]\n\t"
  "ds_read_b128 a[60:63], v25 offset:5120\n\t"
  "v_mfma_f32_16x16x32_bf16 v[112:115], a[0:3], a[32:35], v[112:115]\n\t"
  "ds_read_b128 a[64:67], v25 offset:6144\n\t"
  "ds_read_b128 a[68:71], v25 offset:7168\n\t"
  "v_mfma_f32_16x16x32_bf16 v[116:119], a[0:3], a[36:39], v[116:119]\n\t"
  "ds_read_b128 a[72:75], v25 offset:8192\n\t"
  "ds_read_b128 a[76:79], v25 offset:9216\n\t"
  "v_mfma_f32_16x16x32_bf16 v[120:123], a[4:7], a[20:23], v[120:123]\n\t"
  "buffer_load_dwordx4 v23, s[24:27], s41 offen offset:1024 lds\n\t"
  "v_mfma_f32_16x16x32_bf16 v[124:127], a[4:7], a[24:27], v[124:127]\n\t"
  "v_mfma_f32_16x16x32_bf16 v[128:131], a[4:7], a[28:31], v[128:131]\n\t"
  "ds_read_b64_tr_b16 a[40:41], v28 offset:61440\n\t"
  "ds_read_b64_tr_b16 a[42:43], v33 offset:18432\n\t"
  "v_mfma_f32_16x16x32_bf16 v[132:135], a[4:7], a[32:35], v[132:135]\n\t"
  "ds_read_b64_tr_b16 a[44:45], v29 offset:61440\n\t"
  "ds_read_b64_tr_b16 a[46:47], v34 offset:18432\n\t"
  "v_mfma_f32_16x16x32_bf16 v[136:139], a[4:7], a[36:39], v[136:139]\n\t"
  "buffer_load_dwordx4 v24, s[24:27], s41 offen offset:2048 lds\n\t"
  "v_mfma_f32_16x16x32_bf16 v[140:143], a[8:11], a[20:23], v[140:143]\n\t"
  "ds_read_b64_tr_b16 a[48:49], v30 offset:61440\n\t"
  "v_mfma_f32_16x16x32_bf16 v[144:147], a[8:11], a[24:27], v[144:147]\n\t"
  "ds_read_b64_tr_b16 a[50:51], v35 offset:18432\n\t"
  "ds_read_b64_tr_b16 a[52:53], v31 offset:61440\n\t"
  "v_mfma_f32_16x16x32_bf16 v[148:151], a[8:11], a[28:31], v[148:151]\n\t"
  "s_add_u32 m0, s46, 102400\n\t"
  "s_add_u32 s41, s54, 204800\n\t"
  "buffer_load_dwordx4 v22, s[24:27], s41 offen offset:0 lds\n\t"
  "v_mfma_f32_16x16x32_bf16 v[152:155], a[8:11], a[32:35], v[152:155]\n\t"
  "v_mfma_f32_16x16x32_bf16 v[156:159], a[8:11], a[36:39], v[156:159]\n\t"
  "v_mfma_f32_16x16x32_bf16 v[160:163], a[12:15], a[20:23], v[160:163]\n\t"
  "ds_read_b64_tr_b16 a[54:55], v36 offset:18432\n\t"
  "ds_read_b64_tr_b16 a[56:57], v32 offset:61440\n\t"
  "v_mfma_f32_16x16x32_bf16 v[164:167], a[12:15], a[24:27], v[164:167]\n\t"
  "ds_read_b64_tr_b16 a[58:59], v37 offset:18432\n\t"
  "buffer_load_dwordx4 v23, s[24:27], s41 offen offset:1024 lds\n\t"
  "v_mfma_f32_16x16x32_bf16 v[168:171], a[12:15], a[28:31], v[168:171]\n\t"
  "v_mfma_f32_16x16x32_bf16 v[172:175], a[12:15], a[32:35], v[172:175]\n\t"
  "buffer_load_dwordx4 v24, s[24:27], s41 offen offset:2048 lds\n\t"
  "v_mfma_f32_16x16x32_bf16 v[176:179], a[12:15], a[36:39], v[176:179]\n\t"
  "v_mfma_f32_16x16x32_bf16 v[180:183], a[16:19], a[20:23], v[180:183]\n\t"
  "global_load_dword v74, v71, %13 sc1\n\t"
  "v_mfma_f32_16x16x32_bf16 v[184:187], a[16:19], a[24:27], v[184:187]\n\t"
  "v_mfma_f32_16x16x32_bf16 v[188:191], a[16:19], a[28:31], v[188:191]\n\t"
  "v_mfma_f32_16x16x32_bf16 v[192:195], a[16:19], a[32:35], v[192:195]\n\t"
  "v_mfma_f32_16x16x32_bf16 v[196:199], a[16:19], a[36:39], v[196:199]\n\t"
  "s_waitcnt vmcnt(12) lgkmcnt(0)\n\t"
  "s_barrier\n\t"
  "v_mfma_f32_16x16x32_bf16 v[100:103], a[40:43], a[60:63], v[100:103]\n\t"
  "s_add_u32 m0, s45, 0\n\t"
  "s_add_u32 s40, s59, 0\n\t"
  "buffer_load_dwordx4 v20, s[20:23], s40 offen offset:0 sc1 lds\n\t"
  "v_mfma_f32_16x16x32_bf16 v[104:107], a[40:43], a[64:67], v[104:107]\n\t"
  "v_mfma_f32_16x16x32_bf16 v[108:111], a[40:43], a[68:71], v[108:111]\n\t"
  "ds_read_b128 a[20:23], v25 offset:20480\n\t"
  "v_mfma_f32_16x16x32_bf16 v[112:115], a[40:43], a[72:75], v[112:115]\n\t"
  "ds_read_b128 a[24:27], v25 offset:21504\n\t"
  "ds_read_b128 a[28:31], v25 offset:22528\n\t"
  "v_mfma_f32_16x16x32_bf16 v[116:119], a[40:43], a[76:79], v[116:119]\n\t"
  "ds_read_b128 a[32:35], v25 offset:23552\n\t"
  "ds_read_b128 a[36:39], v25 offset:24576\n\t"
  "v_mfma_f32_16x16x32_bf16 v[120:123], a[44:47], a[60:63], v[120:123]\n\t"
  "ds_read_b64_tr_b16 a[0:1], v28 offset:0\n\t"
  "ds_read_b64_tr_b16 a[2:3], v28 offset:6144\n\t"
  "v_mfma_f32_16x16x32_bf16 v[124:127], a[44:47], a[64:67], v[124:127]\n\t"
  "buffer_load_dwordx4 v20, s[20:23], s40 offen offset:1024 sc1 lds\n\t"
  "v_mfma_f32_16x16x32_bf16 v[128:131], a[44:47], a[68:71], v[128:131]\n\t"
  "v_mfma_f32_16x16x32_bf16 v[132:135], a[44:47], a[72:75], v[132:135]\n\t"
  "ds_read_b64_tr_b16 a[4:5], v29 offset:0\n\t"
  "ds_read_b64_tr_b16 a[6:7], v29 offset:6144\n\t"
  "v_mfma_f32_16x16x32_bf16 v[136:139], a[44:47], a[76:79], v[136:139]\n\t"
  "ds_read_b64_tr_b16 a[8:9], v30 offset:0\n\t"
  "ds_read_b64_tr_b16 a[10:11], v30 offset:6144\n\t"
  "v_mfma_f32_16x16x32_bf16 v[140:143], a[48:51], a[60:63], v[140:143]\n\t"
  "ds_read_b64_tr_b16 a[12:13], v31 offset:0\n\t"
  "buffer_load_dwordx4 v20, s[20:23], s40 offen offset:2048 sc1 lds\n\t"
  "v_mfma_f32_16x16x32_bf16 v[144:147], a[48:51], a[64:67], v[144:147]\n\t"
  "v_mfma_f32_16x16x32_bf16 v[148:151], a[48:51], a[68:71], v[148:151]\n\t"
  "ds_read_b64_tr_b16 a[14:15], v31 offset:6144\n\t"
  "ds_read_b64_tr_b16 a[16:17], v32 offset:0\n\t"
  "v_mfma_f32_16x16x32_bf16 v[152:155], a[48:51], a[72:75], v[152:155]\n\t"
  "ds_read_b64_tr_b16 a[18:19], v32 offset:6144\n\t"
  "buffer_load_dwordx4 v20, s[20:23], s40 offen offset:3072 sc1 lds\n\t"
  "v_mfma_f32_16x16x32_bf16 v[156:159], a[48:51], a[76:79], v[156:159]\n\t"
  "v_mfma_f32_16x16x32_bf16 v[160:163], a[52:55], a[60:63], v[160:163]\n\t"
  "v_mfma_f32_16x16x32_bf16 v[164:167], a[52:55], a[64:67], v[164:167]\n\t"
  "s_add_u32 m0, s45, 4096\n\t"
  "s_nop 0\n\t"
  "buffer_load_dwordx4 v21, s[20:23], s40 offen offset:0 sc1 lds\n\t"
  "v_mfma_f32_16x16x32_bf16 v[168:171], a[52:55], a[68:71], v[168:171]\n\t"
  "v_mfma_f32_16x16x32_bf16 v[172:175], a[52:55], a[72:75], v[172:175]\n\t"
  "v_mfma_f32_16x16x32_bf16 v[176:179], a[52:55], a[76:79], v[176:179]\n\t"
  "v_mfma_f32_16x16x32_bf16 v[180:183], a[56:59], a[60:63], v[180:183]\n\t"
  "v_mfma_f32_16x16x32_bf16 v[184:187], a[56:59], a[64:67], v[184:187]\n\t"
  "v_mfma_f32_16x16x32_bf16 v[188:191], a[56:59], a[68:71], v[188:191]\n\t"
  "v_mfma_f32_16x16x32_bf16 v[192:195], a[56:59], a[72:75], v[192:195]\n\t"
  "v_mfma_f32_16x16x32_bf16 v[196:199], a[56:59], a[76:79], v[196:199]\n\t"
  "s_waitcnt lgkmcnt(0)\n\t"
  "v_mfma_f32_16x16x32_bf16 v[100:103], a[0:3], a[20:23], v[100:103]\n\t"
  "s_add_u32 m0, s46, 114688\n\t"
  "s_add_u32 s41, s54, 245760\n\t"
  "buffer_load_dwordx4 v22, s[24:27], s41 offen offset:0 lds\n\t"
  "v_mfma_f32_16x16x32_bf16 v[104:107], a[0:3], a[24:27], v[104:107]\n\t"
  "v_mfma_f32_16x16x32_bf16 v[108:111], a[0:3], a[28:31], v[108:111]\n\t"
  "ds_read_b128 a[60:63], v25 offset:25600\n\t"
  "v_mfma_f32_16x16x32_bf16 v[112:115], a[0:3], a[32:35], v[112:115]\n\t"
  "ds_read_b128 a[64:67], v25 offset:26624\n\t"
  "ds_read_b128 a[68:71], v25 offset:27648\n\t"
  "v_mfma_f32_16x16x32_bf16 v[116:119], a[0:3], a[36:39], v[116:119]\n\t"
  "ds_read_b128 a[72:75], v25 offset:28672\n\t"
  "ds_read_b128 a[76:79], v25 offset:29696\n\t"
  "v_mfma_f32_16x16x32_bf16 v[120:123], a[4:7], a[20:23], v[120:123]\n\t"
  "ds_read_b64_tr_b16 a[40:41], v28 offset:12288\n\t"
  "buffer_load_dwordx4 v23, s[24:27], s41 offen offset:1024 lds\n\t"
  "v_mfma_f32_16x16x32_bf16 v[124:127], a[4:7], a[24:27], v[124:127]\n\t"
  "v_mfma_f32_16x16x32_bf16 v[128:131], a[4:7], a[28:31], v[128:131]\n\t"
  "ds_read_b64_tr_b16 a[42:43], v28 offset:18432\n\t"
  "v_mfma_f32_16x16x32_bf16 v[132:135], a[4:7], a[32:35], v[132:135]\n\t"
  "ds_read_b64_tr_b16 a[44:45], v29 offset:12288\n\t"
  "ds_read_b64_tr_b16 a[46:47], v29 offset:18432\n\t"
  "v_mfma_f32_16x16x32_bf16 v[136:139], a[4:7], a[36:39], v[136:139]\n\t"
  "ds_read_b64_tr_b16 a[48:49], v30 offset:12288\n\t"
  "buffer_load_dwordx4 v24, s[24:27], s41 offen offset:2048 lds\n\t"
  "v_mfma_f32_16x16x32_bf16 v[140:143], a[8:11], a[20:23], v[140:143]\n\t"
  "v_mfma_f32_16x16x32_bf16 v[144:147], a[8:11], a[24:27], v[144:147]\n\t"
  "ds_read_b64_tr_b16 a[50:51], v30 offset:18432\n\t"
  "ds_read_b64_tr_b16 a[52:53], v31 offset:12288\n\t"
  "v_mfma_f32_16x16x32_bf16 v[148:151], a[8:11], a[28:31], v[148:151]\n\t"
  "ds_read_b64_tr_b16 a[54:55], v31 offset:18432\n\t"
  "ds_read_b64_tr_b16 a[56:57], v32 offset:12288\n\t"
  "v_mfma_f32_16x16x32_bf16 v[152:155], a[8:11], a[32:35], v[152:155]\n\t"
  "s_add_u32 m0, s46, 126976\n\t"
  "s_add_u32 s41, s54, 286720\n\t"
  "buffer_load_dwordx4 v22, s[24:27], s41 offen offset:0 lds\n\t"
  "v_mfma_f32_16x16x32_bf16 v[156:159], a[8:11], a[36:39], v[156:159]\n\t"
  "v_mfma_f32_16x16x32_bf16 v[160:163], a[12:15], a[20:23], v[160:163]\n\t"
  "v_mfma_f32_16x16x32_bf16 v[164:167], a[12:15], a[24:27], v[164:167]\n\t"
  "ds_read_b64_tr_b16 a[58:59], v32 offset:18432\n\t"
  "buffer_load_dwordx4 v23, s[24:27], s41 offen offset:1024 lds\n\t"
  "v_mfma_f32_16x16x32_bf16 v[168:171], a[12:15], a[28:31], v[168:171]\n\t"
  "v_mfma_f32_16x16x32_bf16 v[172:175], a[12:15], a[32:35], v[172:175]\n\t"
  "buffer_load_dwordx4 v24, s[24:27], s41 offen offset:2048 lds\n\t"
  "v_mfma_f32_16x16x32_bf16 v[176:179], a[12:15], a[36:39], v[176:179]\n\t"
  "v_mfma_f32_16x16x32_bf16 v[180:183], a[16:19], a[20:23], v[180:183]\n\t"
  "v_mfma_f32_16x16x32_bf16 v[184:187], a[16:19], a[24:27], v[184:187]\n\t"
  "v_mfma_f32_16x16x32_bf16 v[188:191], a[16:19], a[28:31], v[188:191]\n\t"
  "v_mfma_f32_16x16x32_bf16 v[192:195], a[16:19], a[32:35], v[192:195]\n\t"
  "v_mfma_f32_16x16x32_bf16 v[196:199], a[16:19], a[36:39], v[196:199]\n\t"
  "s_waitcnt vmcnt(12) lgkmcnt(0)\n\t"
  "s_barrier\n\t"
  "v_mfma_f32_16x16x32_bf16 v[100:103], a[40:43], a[60:63], v[100:103]\n\t"
  "s_add_u32 m0, s45, 20480\n\t"
  "s_add_u32 s40, s59, 10240\n\t"
  "buffer_load_dwordx4 v20, s[20:23], s40 offen offset:0 sc1 lds\n\t"
  "v_mfma_f32_16x16x32_bf16 v[104:107], a[40:43], a[64:67], v[104:107]\n\t"
  "v_mfma_f32_16x16x32_bf16 v[108:111], a[40:43], a[68:71], v[108:111]\n\t"
  "ds_read_b128 a[20:23], v25 offset:40960\n\t"
  "v_mfma_f32_16x16x32_bf16 v[112:115], a[40:43], a[72:75], v[112:115]\n\t"
  "ds_read_b128 a[24:27], v25 offset:41984\n\t"
  "ds_read_b128 a[28:31], v25 offset:43008\n\t"
  "v_mfma_f32_16x16x32_bf16 v[116:119], a[40:43], a[76:79], v[116:119]\n\t"
  "ds_read_b128 a[32:35], v25 offset:44032\n\t"
  "ds_read_b128 a[36:39], v25 offset:45056\n\t"
  "v_mfma_f32_16x16x32_bf16 v[120:123], a[44:47], a[60:63], v[120:123]\n\t"
  "ds_read_b64_tr_b16 a[0:1], v28 offset:24576\n\t"
  "ds_read_b64_tr_b16 a[2:3], v28 offset:30720\n\t"
  "v_mfma_f32_16x16x32_bf16 v[124:127], a[44:47], a[64:67], v[124:127]\n\t"
  "buffer_load_dwordx4 v20, s[20:23], s40 offen offset:1024 sc1 lds\n\t"
  "v_mfma_f32_16x16x32_bf16 v[128:131], a[44:47], a[68:71], v[128:131]\n\t"
  "v_mfma_f32_16x16x32_bf16 v[132:135], a[44:47], a[72:75], v[132:135]\n\t"
  "ds_read_b64_tr_b16 a[4:5], v29 offset:24576\n\t"
  "ds_read_b64_tr_b16 a[6:7], v29 offset:30720\n\t"
  "v_mfma_f32_16x16x32_bf16 v[136:139], a[44:47], a[76:79], v[136:139]\n\t"
  "ds_read_b64_tr_b16 a[8:9], v30 offset:24576\n\t"
  "ds_read_b64_tr_b16 a[10:11], v30 offset:30720\n\t"
  "v_mfma_f32_16x16x32_bf16 v[140:143], a[48:51], a[60:63], v[140:143]\n\t"
  "ds_read_b64_tr_b16 a[12:13], v31 offset:24576\n\t"
  "buffer_load_dwordx4 v20, s[20:23], s40 offen offset:2048 sc1 lds\n\t"
  "v_mfma_f32_16x16x32_bf16 v[144:147], a[48:51], a[64:67], v[144:147]\n\t"
  "v_mfma_f32_16x16x32_bf16 v[148:151], a[48:51], a[68:71], v[148:151]\n\t"
  "ds_read_b64_tr_b16 a[14:15], v31 offset:30720\n\t"
  "ds_read_b64_tr_b16 a[16:17], v32 offset:24576\n\t"
  "v_mfma_f32_16x16x32_bf16 v[152:155], a[48:51], a[72:75], v[152:155]\n\t"
  "ds_read_b64_tr_b16 a[18:19], v32 offset:30720\n\t"
  "buffer_load_dwordx4 v20, s[20:23], s40 offen offset:3072 sc1 lds\n\t"
  "v_mfma_f32_16x16x32_bf16 v[156:159], a[48:51], a[76:79], v[156:159]\n\t"
  "v_mfma_f32_16x16x32_bf16 v[160:163], a[52:55], a[60:63], v[160:163]\n\t"
  "v_mfma_f32_16x16x32_bf16 v[164:167], a[52:55], a[64:67], v[164:167]\n\t"
  "s_add_u32 m0, s45, 24576\n\t"
  "s_nop 0\n\t"
  "buffer_load_dwordx4 v21, s[20:23], s40 offen offset:0 sc1 lds\n\t"
  "v_mfma_f32_16x16x32_bf16 v[168:171], a[52:55], a[68:71], v[168:171]\n\t"
  "v_mfma_f32_16x16x32_bf16 v[172:175], a[52:55], a[72:75], v[172:175]\n\t"
  "v_mfma_f32_16x16x32_bf16 v[176:179], a[52:55], a[76:79], v[176:179]\n\t"
  "v_mfma_f32_16x16x32_bf16 v[180:183], a[56:59], a[60:63], v[180:183]\n\t"
  "v_mfma_f32_16x16x32_bf16 v[184:187], a[56:59], a[64:67], v[184:187]\n\t"
  "v_mfma_f32_16x16x32_bf16 v[188:191], a[56:59], a[68:71], v[188:191]\n\t"
  "v_mfma_f32_16x16x32_bf16 v[192:195], a[56:59], a[72:75], v[192:195]\n\t"
  "v_mfma_f32_16x16x32_bf16 v[196:199], a[56:59], a[76:79], v[196:199]\n\t"
  "s_waitcnt lgkmcnt(0)\n\t"
  "v_mfma_f32_16x16x32_bf16 v[100:103], a[0:3], a[20:23], v[100:103]\n\t"
  "s_add_u32 m0, s46, 65536\n\t"
  "s_add_u32 s41, s55, 0\n\t"
  "buffer_load_dwordx4 v22, s[24:27], s41 offen offset:0 lds\n\t"
  "v_mfma_f32_16x16x32_bf16 v[104:107], a[0:3], a[24:27], v[104:107]\n\t"
  "v_mfma_f32_16x16x32_bf16 v[108:111], a[0:3], a[28:31], v[108:111]\n\t"
  "ds_read_b128 a[60:63], v25 offset:46080\n\t"
  "v_mfma_f32_16x16x32_bf16 v[112:115], a[0:3], a[32:35], v[112:115]\n\t"
  "ds_read_b128 a[64:67], v25 offset:47104\n\t"
  "ds_read_b128 a[68:71], v25 offset:48128\n\t"
  "v_mfma_f32_16x16x32_bf16 v[116:119], a[0:3], a[36:39], v[116:119]\n\t"
  "ds_read_b128 a[72:75], v25 offset:49152\n\t"
  "ds_read_b128 a[76:79], v25 offset:50176\n\t"
  "v_mfma_f32_16x16x32_bf16 v[120:123], a[4:7], a[20:23], v[120:123]\n\t"
  "ds_read_b64_tr_b16 a[40:41], v28 offset:36864\n\t"
  "buffer_load_dwordx4 v23, s[24:27], s41 offen offset:1024 lds\n\t"
  "v_mfma_f32_16x16x32_bf16 v[124:127], a[4:7], a[24:27], v[124:127]\n\t"
  "v_mfma_f32_16x16x32_bf16 v[128:131], a[4:7], a[28:31], v[128:131]\n\t"
  "ds_read_b64_tr_b16 a[42:43], v28 offset:43008\n\t"
  "v_mfma_f32_16x16x32_bf16 v[132:135], a[4:7], a[32:35], v[132:135]\n\t"
  "ds_read_b64_tr_b16 a[44:45], v29 offset:36864\n\t"
  "ds_read_b64_tr_b16 a[46:47], v29 offset:43008\n\t"
  "v_mfma_f32_16x16x32_bf16 v[136:139], a[4:7], a[36:39], v[136:139]\n\t"
  "ds_read_b64_tr_b16 a[48:49], v30 offset:36864\n\t"
  "buffer_load_dwordx4 v24, s[24:27], s41 offen offset:2048 lds\n\t"
  "v_mfma_f32_16x16x32_bf16 v[140:143], a[8:11], a[20:23], v[140:143]\n\t"
  "v_mfma_f32_16x16x32_bf16 v[144:147], a[8:11], a[24:27], v[144:147]\n\t"
  "ds_read_b64_tr_b16 a[50:51], v30 offset:43008\n\t"
  "ds_read_b64_tr_b16 a[52:53], v31 offset:36864\n\t"
  "v_mfma_f32_16x16x32_bf16 v[148:151], a[8:11], a[28:31], v[148:151]\n\t"
  "ds_read_b64_tr_b16 a[54:55], v31 offset:43008\n\t"
  "ds_read_b64_tr_b16 a[56:57], v32 offset:36864\n\t"
  "v_mfma_f32_16x16x32_bf16 v[152:155], a[8:11], a[32:35], v[152:155]\n\t"
  "s_add_u32 m0, s46, 77824\n\t"
  "s_add_u32 s41, s55, 40960\n\t"
  "buffer_load_dwordx4 v22, s[24:27], s41 offen offset:0 lds\n\t"
  "v_mfma_f32_16x16x32_bf16 v[156:159], a[8:11], a[36:39], v[156:159]\n\t"
  "v_mfma_f32_16x16x32_bf16 v[160:163], a[12:15], a[20:23], v[160:163]\n\t"
  "v_mfma_f32_16x16x32_bf16 v[164:167], a[12:15], a[24:27], v[164:167]\n\t"
  "ds_read_b64_tr_b16 a[58:59], v32 offset:43008\n\t"
  "buffer_load_dwordx4 v23, s[24:27], s41 offen offset:1024 lds\n\t"
  "v_mfma_f32_16x16x32_bf16 v[168:171], a[12:15], a[28:31], v[168:171]\n\t"
  "v_mfma_f32_16x16x32_bf16 v[172:175], a[12:15], a[32:35], v[172:175]\n\t"
  "buffer_load_dwordx4 v24, s[24:27], s41 offen offset:2048 lds\n\t"
  "v_mfma_f32_16x16x32_bf16 v[176:179], a[12:15], a[36:39], v[176:179]\n\t"
  "v_mfma_f32_16x16x32_bf16 v[180:183], a[16:19], a[20:23], v[180:183]\n\t"
  "v_mfma_f32_16x16x32_bf16 v[184:187], a[16:19], a[24:27], v[184:187]\n\t"
  "v_mfma_f32_16x16x32_bf16 v[188:191], a[16:19], a[28:31], v[188:191]\n\t"
  "v_mfma_f32_16x16x32_bf16 v[192:195], a[16:19], a[32:35], v[192:195]\n\t"
  "v_mfma_f32_16x16x32_bf16 v[196:199], a[16:19], a[36:39], v[196:199]\n\t"
  "s_waitcnt vmcnt(11) lgkmcnt(0)\n\t"
  "s_barrier\n\t"
  "v_mfma_f32_16x16x32_bf16 v[100:103], a[40:43], a[60:63], v[100:103]\n\t"
  "s_add_u32 m0, s45, 40960\n\t"
  "s_add_u32 s40, s59, 20480\n\t"
  "buffer_load_dwordx4 v20, s[20:23], s40 offen offset:0 sc1 lds\n\t"
  "v_mfma_f32_16x16x32_bf16 v[104:107], a[40:43], a[64:67], v[104:107]\n\t"
  "v_mfma_f32_16x16x32_bf16 v[108:111], a[40:43], a[68:71], v[108:111]\n\t"
  "ds_read_b128 a[20:23], v26 offset:0\n\t"
  "v_mfma_f32_16x16x32_bf16 v[112:115], a[40:43], a[72:75], v[112:115]\n\t"
  "ds_read_b128 a[24:27], v26 offset:1024\n\t"
  "ds_read_b128 a[28:31], v26 offset:2048\n\t"
  "v_mfma_f32_16x16x32_bf16 v[116:119], a[40:43], a[76:79], v[116:119]\n\t"
  "ds_read_b128 a[32:35], v26 offset:3072\n\t"
  "ds_read_b128 a[36:39], v26 offset:4096\n\t"
  "v_mfma_f32_16x16x32_bf16 v[120:123], a[44:47], a[60:63], v[120:123]\n\t"
  "ds_read_b64_tr_b16 a[0:1], v28 offset:49152\n\t"
  "ds_read_b64_tr_b16 a[2:3], v28 offset:55296\n\t"
  "v_mfma_f32_16x16x32_bf16 v[124:127], a[44:47], a[64:67], v[124:127]\n\t"
  "buffer_load_dwordx4 v20, s[20:23], s40 offen offset:1024 sc1 lds\n\t"
  "v_mfma_f32_16x16x32_bf16 v[128:131], a[44:47], a[68:71], v[128:131]\n\t"
  "v_mfma_f32_16x16x32_bf16 v[132:135], a[44:47], a[72:75], v[132:135]\n\t"
  "ds_read_b64_tr_b16 a[4:5], v29 offset:49152\n\t"
  "ds_read_b64_tr_b16 a[6:7], v29 offset:55296\n\t"
  "v_mfma_f32_16x16x32_bf16 v[136:139], a[44:47], a[76:79], v[136:139]\n\t"
  "ds_read_b64_tr_b16 a[8:9], v30 offset:49152\n\t"
  "ds_read_b64_tr_b16 a[10:11], v30 offset:55296\n\t"
  "v_mfma_f32_16x16x32_bf16 v[140:143], a[48:51], a[60:63], v[140:143]\n\t"
  "ds_read_b64_tr_b16 a[12:13], v31 offset:49152\n\t"
  "buffer_load_dwordx4 v20, s[20:23], s40 offen offset:2048 sc1 lds\n\t"
  "v_mfma_f32_16x16x32_bf16 v[144:147], a[48:51], a[64:67], v[144:147]\n\t"
  "v_mfma_f32_16x16x32_bf16 v[148:151], a[48:51], a[68:71], v[148:151]\n\t"
  "ds_read_b64_tr_b16 a[14:15], v31 offset:55296\n\t"
  "ds_read_b64_tr_b16 a[16:17], v32 offset:49152\n\t"
  "v_mfma_f32_16x16x32_bf16 v[152:155], a[48:51], a[72:75], v[152:155]\n\t"
  "ds_read_b64_tr_b16 a[18:19], v32 offset:55296\n\t"
  "buffer_load_dwordx4 v20, s[20:23], s40 offen offset:3072 sc1 lds\n\t"
  "v_mfma_f32_16x16x32_bf16 v[156:159], a[48:51], a[76:79], v[156:159]\n\t"
  "v_mfma_f32_16x16x32_bf16 v[160:163], a[52:55], a[60:63], v[160:163]\n\t"
  "v_mfma_f32_16x16x32_bf16 v[164:167], a[52:55], a[64:67], v[164:167]\n\t"
  "s_add_u32 m0, s45, 45056\n\t"
  "s_nop 0\n\t"
  "buffer_load_dwordx4 v21, s[20:23], s40 offen offset:0 sc1 lds\n\t"
  "v_mfma_f32_16x16x32_bf16 v[168:171], a[52:55], a[68:71], v[168:171]\n\t"
  "v_mfma_f32_16x16x32_bf16 v[172:175], a[52:55], a[72:75], v[172:175]\n\t"
  "v_mfma_f32_16x16x32_bf16 v[176:179], a[52:55], a[76:79], v[176:179]\n\t"
  "v_mfma_f32_16x16x32_bf16 v[180:183], a[56:59], a[60:63], v[180:183]\n\t"
  "v_mfma_f32_16x16x32_bf16 v[184:187], a[56:59], a[64:67], v[184:187]\n\t"
  "v_mfma_f32_16x16x32_bf16 v[188:191], a[56:59], a[68:71], v[188:191]\n\t"
  "v_mfma_f32_16x16x32_bf16 v[192:195], a[56:59], a[72:75], v[192:195]\n\t"
  "v_mfma_f32_16x16x32_bf16 v[196:199], a[56:59], a[76:79], v[196:199]\n\t"
  "s_waitcnt lgkmcnt(0)\n\t"
  "v_mfma_f32_16x16x32_bf16 v[100:103], a[0:3], a[20:23], v[100:103]\n\t"
  "s_add_u32 m0, s46, 90112\n\t"
  "s_add_u32 s41, s55, 81920\n\t"
  "buffer_load_dwordx4 v22, s[24:27], s41 offen offset:0 lds\n\t"
  "v_mfma_f32_16x16x32_bf16 v[104:107], a[0:3], a[24:27], v[104:107]\n\t"
  "v_mfma_f32_16x16x32_bf16 v[108:111], a[0:3], a[28:31], v[108:111]\n\t"
  "ds_read_b128 a[60:63], v26 offset:5120\n\t"
  "v_mfma_f32_16x16x32_bf16 v[112:115], a[0:3], a[32:35], v[112:115]\n\t"
  "ds_read_b128 a[64:67], v26 offset:6144\n\t"
  "ds_read_b128 a[68:71], v26 offset:7168\n\t"
  "v_mfma_f32_16x16x32_bf16 v[116:119], a[0:3], a[36:39], v[116:119]\n\t"
  "ds_read_b128 a[72:75], v26 offset:8192\n\t"
  "ds_read_b128 a[76:79], v26 offset:9216\n\t"
  "v_mfma_f32_16x16x32_bf16 v[120:123], a[4:7], a[20:23], v[120:123]\n\t"
  "ds_read_b64_tr_b16 a[40:41], v28 offset:61440\n\t"
  "buffer_load_dwordx4 v23, s[24:27], s41 offen offset:1024 lds\n\t"
  "v_mfma_f32_16x16x32_bf16 v[124:127], a[4:7], a[24:27], v[124:127]\n\t"
  "v_mfma_f32_16x16x32_bf16 v[128:131], a[4:7], a[28:31], v[128:131]\n\t"
  "ds_read_b64_tr_b16 a[42:43], v33 offset:18432\n\t"
  "v_mfma_f32_16x16x32_bf16 v[132:135], a[4:7], a[32:35], v[132:135]\n\t"
  "ds_read_b64_tr_b16 a[44:45], v29 offset:61440\n\t"
  "ds_read_b64_tr_b16 a[46:47], v34 offset:18432\n\t"
  "v_mfma_f32_16x16x32_bf16 v[136:139], a[4:7], a[36:39], v[136:139]\n\t"
  "ds_read_b64_tr_b16 a[48:49], v30 offset:61440\n\t"
  "buffer_load_dwordx4 v24, s[24:27], s41 offen offset:2048 lds\n\t"
  "v_mfma_f32_16x16x32_bf16 v[140:143], a[8:11], a[20:23], v[140:143]\n\t"
  "v_mfma_f32_16x16x32_bf16 v[144:147], a[8:11], a[24:27], v[144:147]\n\t"
  "ds_read_b64_tr_b16 a[50:51], v35 offset:18432\n\t"
  "ds_read_b64_tr_b16 a[52:53], v31 offset:61440\n\t"
  "v_mfma_f32_16x16x32_bf16 v[148:151], a[8:11], a[28:31], v[148:151]\n\t"
  "ds_read_b64_tr_b16 a[54:55], v36 offset:18432\n\t"
  "ds_read_b64_tr_b16 a[56:57], v32 offset:61440\n\t"
  "v_mfma_f32_16x16x32_bf16 v[152:155], a[8:11], a[32:35], v[152:155]\n\t"
  "s_add_u32 m0, s46, 102400\n\t"
  "s_add_u32 s41, s55, 122880\n\t"
  "buffer_load_dwordx4 v22, s[24:27], s41 offen offset:0 lds\n\t"
  "v_mfma_f32_16x16x32_bf16 v[156:159], a[8:11], a[36:39], v[156:159]\n\t"
  "v_mfma_f32_16x16x32_bf16 v[160:163], a[12:15], a[20:23], v[160:163]\n\t"
  "v_mfma_f32_16x16x32_bf16 v[164:167], a[12:15], a[24:27], v[164:167]\n\t"
  "ds_read_b64_tr_b16 a[58:59], v37 offset:18432\n\t"
  "buffer_load_dwordx4 v23, s[24:27], s41 offen offset:1024 lds\n\t"
  "v_mfma_f32_16x16x32_bf16 v[168:171], a[12:15], a[28:31], v[168:171]\n\t"
  "v_mfma_f32_16x16x32_bf16 v[172:175], a[12:15], a[32:35], v[172:175]\n\t"
  "buffer_load_dwordx4 v24, s[24:27], s41 offen offset:2048 lds\n\t"
  "v_mfma_f32_16x16x32_bf16 v[176:179], a[12:15], a[36:39], v[176:179]\n\t"
  "v_mfma_f32_16x16x32_bf16 v[180:183], a[16:19], a[20:23], v[180:183]\n\t"
  "v_mfma_f32_16x16x32_bf16 v[184:187], a[16:19], a[24:27], v[184:187]\n\t"
  "v_mfma_f32_16x16x32_bf16 v[188:191], a[16:19], a[28:31], v[188:191]\n\t"
  "v_mfma_f32_16x16x32_bf16 v[192:195], a[16:19], a[32:35], v[192:195]\n\t"
  "v_mfma_f32_16x16x32_bf16 v[196:199], a[16:19], a[36:39], v[196:199]\n\t"
  "s_waitcnt vmcnt(11) lgkmcnt(0)\n\t"
  "s_barrier\n\t"
  "v_mfma_f32_16x16x32_bf16 v[100:103], a[40:43], a[60:63], v[100:103]\n\t"
  "s_add_u32 m0, s45, 139264\n\t"
  "s_add_u32 s40, s59, 30720\n\t"
  "buffer_load_dwordx4 v20, s[20:23], s40 offen offset:0 sc1 lds\n\t"
  "v_mfma_f32_16x16x32_bf16 v[104:107], a[40:43], a[64:67], v[104:107]\n\t"
  "v_mfma_f32_16x16x32_bf16 v[108:111], a[40:43], a[68:71], v[108:111]\n\t"
  "ds_read_b128 a[20:23], v25 offset:0\n\t"
  "v_mfma_f32_16x16x32_bf16 v[112:115], a[40:43], a[72:75], v[112:115]\n\t"
  "ds_read_b128 a[24:27], v25 offset:1024\n\t"
  "ds_read_b128 a[28:31], v25 offset:2048\n\t"
  "v_mfma_f32_16x16x32_bf16 v[116:119], a[40:43], a[76:79], v[116:119]\n\t"
  "ds_read_b128 a[32:35], v25 offset:3072\n\t"
  "ds_read_b128 a[36:39], v25 offset:4096\n\t"
  "v_mfma_f32_16x16x32_bf16 v[120:123], a[44:47], a[60:63], v[120:123]\n\t"
  "ds_read_b64_tr_b16 a[0:1], v28 offset:0\n\t"
  "ds_read_b64_tr_b16 a[2:3], v28 offset:6144\n\t"
  "v_mfma_f32_16x16x32_bf16 v[124:127], a[44:47], a[64:67], v[124:127]\n\t"
  "buffer_load_dwordx4 v20, s[20:23], s40 offen offset:1024 sc1 lds\n\t"
  "v_mfma_f32_16x16x32_bf16 v[128:131], a[44:47], a[68:71], v[128:131]\n\t"
  "v_mfma_f32_16x16x32_bf16 v[132:135], a[44:47], a[72:75], v[132:135]\n\t"
  "ds_read_b64_tr_b16 a[4:5], v29 offset:0\n\t"
  "ds_read_b64_tr_b16 a[6:7], v29 offset:6144\n\t"
  "v_mfma_f32_16x16x32_bf16 v[136:139], a[44:47], a[76:79], v[136:139]\n\t"
  "ds_read_b64_tr_b16 a[8:9], v30 offset:0\n\t"
  "ds_read_b64_tr_b16 a[10:11], v30 offset:6144\n\t"
  "v_mfma_f32_16x16x32_bf16 v[140:143], a[48:51], a[60:63], v[140:143]\n\t"
  "ds_read_b64_tr_b16 a[12:13], v31 offset:0\n\t"
  "buffer_load_dwordx4 v20, s[20:23], s40 offen offset:2048 sc1 lds\n\t"
  "v_mfma_f32_16x16x32_bf16 v[144:147], a[48:51], a[64:67], v[144:147]\n\t"
  "v_mfma_f32_16x16x32_bf16 v[148:151], a[48:51], a[68:71], v[148:151]\n\t"
  "ds_read_b64_tr_b16 a[14:15], v31 offset:6144\n\t"
  "ds_read_b64_tr_b16 a[16:17], v32 offset:0\n\t"
  "v_mfma_f32_16x16x32_bf16 v[152:155], a[48:51], a[72:75], v[152:155]\n\t"
  "ds_read_b64_tr_b16 a[18:19], v32 offset:6144\n\t"
  "buffer_load_dwordx4 v20, s[20:23], s40 offen offset:3072 sc1 lds\n\t"
  "v_mfma_f32_16x16x32_bf16 v[156:159], a[48:51], a[76:79], v[156:159]\n\t"
  "v_mfma_f32_16x16x32_bf16 v[160:163], a[52:55], a[60:63], v[160:163]\n\t"
  "v_mfma_f32_16x16x32_bf16 v[164:167], a[52:55], a[64:67], v[164:167]\n\t"
  "s_add_u32 m0, s45, 143360\n\t"
  "s_nop 0\n\t"
  "buffer_load_dwordx4 v21, s[20:23], s40 offen offset:0 sc1 lds\n\t"
  "v_mfma_f32_16x16x32_bf16 v[168:171], a[52:55], a[68:71], v[168:171]\n\t"
  "v_mfma_f32_16x16x32_bf16 v[172:175], a[52:55], a[72:75], v[172:175]\n\t"
  "v_mfma_f32_16x16x32_bf16 v[176:179], a[52:55], a[76:79], v[176:179]\n\t"
  "v_mfma_f32_16x16x32_bf16 v[180:183], a[56:59], a[60:63], v[180:183]\n\t"
  "v_mfma_f32_16x16x32_bf16 v[184:187], a[56:59], a[64:67], v[184:187]\n\t"
  "v_mfma_f32_16x16x32_bf16 v[188:191], a[56:59], a[68:71], v[188:191]\n\t"
  "v_mfma_f32_16x16x32_bf16 v[192:195], a[56:59], a[72:75], v[192:195]\n\t"
  "v_mfma_f32_16x16x32_bf16 v[196:199], a[56:59], a[76:79], v[196:199]\n\t"
  "s_waitcnt lgkmcnt(0)\n\t"
  "v_mfma_f32_16x16x32_bf16 v[100:103], a[0:3], a[20:23], v[100:103]\n\t"
  "s_add_u32 m0, s46, 114688\n\t"
  "s_add_u32 s41, s55, 163840\n\t"
  "buffer_load_dwordx4 v22, s[24:27], s41 offen offset:0 lds\n\t"
  "v_mfma_f32_16x16x32_bf16 v[104:107], a[0:3], a[24:27], v[104:107]\n\t"
  "v_mfma_f32_16x16x32_bf16 v[108:111], a[0:3], a[28:31], v[108:111]\n\t"
  "ds_read_b128 a[60:63], v25 offset:5120\n\t"
  "v_mfma_f32_16x16x32_bf16 v[112:115], a[0:3], a[32:35], v[112:115]\n\t"
  "ds_read_b128 a[64:67], v25 offset:6144\n\t"
  "ds_read_b128 a[68:71], v25 offset:7168\n\t"
  "v_mfma_f32_16x16x32_bf16 v[116:119], a[0:3], a[36:39], v[116:119]\n\t"
  "ds_read_b128 a[72:75], v25 offset:8192\n\t"
  "ds_read_b128 a[76:79], v25 offset:9216\n\t"
  "v_mfma_f32_16x16x32_bf16 v[120:123], a[4:7], a[20:23], v[120:123]\n\t"
  "ds_read_b64_tr_b16 a[40:41], v28 offset:12288\n\t"
  "buffer_load_dwordx4 v23, s[24:27], s41 offen offset:1024 lds\n\t"
  "v_mfma_f32_16x16x32_bf16 v[124:127], a[4:7], a[24:27], v[124:127]\n\t"
  "v_mfma_f32_16x16x32_bf16 v[128:131], a[4:7], a[28:31], v[128:131]\n\t"
  "ds_read_b64_tr_b16 a[42:43], v28 offset:18432\n\t"
  "v_mfma_f32_16x16x32_bf16 v[132:135], a[4:7], a[32:35], v[132:135]\n\t"
  "ds_read_b64_tr_b16 a[44:45], v29 offset:12288\n\t"
  "ds_read_b64_tr_b16 a[46:47], v29 offset:18432\n\t"
  "v_mfma_f32_16x16x32_bf16 v[136:139], a[4:7], a[36:39], v[136:139]\n\t"
  "ds_read_b64_tr_b16 a[48:49], v30 offset:12288\n\t"
  "buffer_load_dwordx4 v24, s[24:27], s41 offen offset:2048 lds\n\t"
  "v_mfma_f32_16x16x32_bf16 v[140:143], a[8:11], a[20:23], v[140:143]\n\t"
  "v_mfma_f32_16x16x32_bf16 v[144:147], a[8:11], a[24:27], v[144:147]\n\t"
  "ds_read_b64_tr_b16 a[50:51], v30 offset:18432\n\t"
  "ds_read_b64_tr_b16 a[52:53], v31 offset:12288\n\t"
  "v_mfma_f32_16x16x32_bf16 v[148:151], a[8:11], a[28:31], v[148:151]\n\t"
  "ds_read_b64_tr_b16 a[54:55], v31 offset:18432\n\t"
  "ds_read_b64_tr_b16 a[56:57], v32 offset:12288\n\t"
  "v_mfma_f32_16x16x32_bf16 v[152:155], a[8:11], a[32:35], v[152:155]\n\t"
  "s_add_u32 m0, s46, 126976\n\t"
  "s_add_u32 s41, s55, 204800\n\t"
  "buffer_load_dwordx4 v22, s[24:27], s41 offen offset:0 lds\n\t"
  "v_mfma_f32_16x16x32_bf16 v[156:159], a[8:11], a[36:39], v[156:159]\n\t"
  "v_mfma_f32_16x16x32_bf16 v[160:163], a[12:15], a[20:23], v[160:163]\n\t"
  "v_mfma_f32_16x16x32_bf16 v[164:167], a[12:15], a[24:27], v[164:167]\n\t"
  "ds_read_b64_tr_b16 a[58:59], v32 offset:18432\n\t"
  "buffer_load_dwordx4 v23, s[24:27], s41 offen offset:1024 lds\n\t"
  "v_mfma_f32_16x16x32_bf16 v[168:171], a[12:15], a[28:31], v[168:171]\n\t"
  "v_mfma_f32_16x16x32_bf16 v[172:175], a[12:15], a[32:35], v[172:175]\n\t"
  "buffer_load_dwordx4 v24, s[24:27], s41 offen offset:2048 lds\n\t"
  "v_mfma_f32_16x16x32_bf16 v[176:179], a[12:15], a[36:39], v[176:179]\n\t"
  "v_mfma_f32_16x16x32_bf16 v[180:183], a[16:19], a[20:23], v[180:183]\n\t"
  "v_mfma_f32_16x16x32_bf16 v[184:187], a[16:19], a[24:27], v[184:187]\n\t"
  "v_mfma_f32_16x16x32_bf16 v[188:191], a[16:19], a[28:31], v[188:191]\n\t"
  "v_mfma_f32_16x16x32_bf16 v[192:195], a[16:19], a[32:35], v[192:195]\n\t"
  "v_mfma_f32_16x16x32_bf16 v[196:199], a[16:19], a[36:39], v[196:199]\n\t"
  "s_waitcnt vmcnt(11) lgkmcnt(0)\n\t"
  "s_barrier\n\t"
  "v_mfma_f32_16x16x32_bf16 v[100:103], a[40:43], a[60:63], v[100:103]\n\t"
  "ds_read_b128 a[20:23], v25 offset:20480\n\t"
  "v_mfma_f32_16x16x32_bf16 v[104:107], a[40:43], a[64:67], v[104:107]\n\t"
  "ds_read_b128 a[24:27], v25 offset:21504\n\t"
  "v_mfma_f32_16x16x32_bf16 v[108:111], a[40:43], a[68:71], v[108:111]\n\t"
  "ds_read_b128 a[28:31], v25 offset:22528\n\t"
  "v_mfma_f32_16x16x32_bf16 v[112:115], a[40:43], a[72:75], v[112:115]\n\t"
  "ds_read_b128 a[32:35], v25 offset:23552\n\t"
  "ds_read_b128 a[36:39], v25 offset:24576\n\t"
  "v_mfma_f32_16x16x32_bf16 v[116:119], a[40:43], a[76:79], v[116:119]\n\t"
  "ds_read_b64_tr_b16 a[0:1], v28 offset:24576\n\t"
  "v_mfma_f32_16x16x32_bf16 v[120:123], a[44:47], a[60:63], v[120:123]\n\t"
  "ds_read_b64_tr_b16 a[2:3], v28 offset:30720\n\t"
  "v_mfma_f32_16x16x32_bf16 v[124:127], a[44:47], a[64:67], v[124:127]\n\t"
  "ds_read_b64_tr_b16 a[4:5], v29 offset:24576\n\t"
  "ds_read_b64_tr_b16 a[6:7], v29 offset:30720\n\t"
  "v_mfma_f32_16x16x32_bf16 v[128:131], a[44:47], a[68:71], v[128:131]\n\t"
  "ds_read_b64_tr_b16 a[8:9], v30 offset:24576\n\t"
  "v_mfma_f32_16x16x32_bf16 v[132:135], a[44:47], a[72:75], v[132:135]\n\t"
  "ds_read_b64_tr_b16 a[10:11], v30 offset:30720\n\t"
  "v_mfma_f32_16x16x32_bf16 v[136:139], a[44:47], a[76:79], v[136:139]\n\t"
  "ds_read_b64_tr_b16 a[12:13], v31 offset:24576\n\t"
  "ds_read_b64_tr_b16 a[14:15], v31 offset:30720\n\t"
  "v_mfma_f32_16x16x32_bf16 v[140:143], a[48:51], a[60:63], v[140:143]\n\t"
  "ds_read_b64_tr_b16 a[16:17], v32 offset:24576\n\t"
  "v_mfma_f32_16x16x32_bf16 v[144:147], a[48:51], a[64:67], v[144:147]\n\t"
  "ds_read_b64_tr_b16 a[18:19], v32 offset:30720\n\t"
  "v_mfma_f32_16x16x32_bf16 v[148:151], a[48:51], a[68:71], v[148:151]\n\t"
  "v_mfma_f32_16x16x32_bf16 v[152:155], a[48:51], a[72:75], v[152:155]\n\t"
  "v_mfma_f32_16x16x32_bf16 v[156:159], a[48:51], a[76:79], v[156:159]\n\t"
  "v_mfma_f32_16x16x32_bf16 v[160:163], a[52:55], a[60:63], v[160:163]\n\t"
  "v_mfma_f32_16x16x32_bf16 v[164:167], a[52:55], a[64:67], v[164:167]\n\t"
  "v_mfma_f32_16x16x32_bf16 v[168:171], a[52:55], a[68:71], v[168:171]\n\t"
  "v_mfma_f32_16x16x32_bf16 v[172:175], a[52:55], a[72:75], v[172:175]\n\t"
  "v_mfma_f32_16x16x32_bf16 v[176:179], a[52:55], a[76:79], v[176:179]\n\t"
  "v_mfma_f32_16x16x32_bf16 v[180:183], a[56:59], a[60:63], v[180:183]\n\t"
  "v_mfma_f32_16x16x32_bf16 v[184:187], a[56:59], a[64:67], v[184:187]\n\t"
  "v_mfma_f32_16x16x32_bf16 v[188:191], a[56:59], a[68:71], v[188:191]\n\t"
  "v_mfma_f32_16x16x32_bf16 v[192:195], a[56:59], a[72:75], v[192:195]\n\t"
  "v_mfma_f32_16x16x32_bf16 v[196:199], a[56:59], a[76:79], v[196:199]\n\t"
  "s_waitcnt lgkmcnt(0)\n\t"
  "v_mfma_f32_16x16x32_bf16 v[100:103], a[0:3], a[20:23], v[100:103]\n\t"
  "s_add_u32 m0, s46, 65536\n\t"
  "s_add_u32 s41, s55, 245760\n\t"
  "buffer_load_dwordx4 v22, s[24:27], s41 offen offset:0 lds\n\t"
  "v_mfma_f32_16x16x32_bf16 v[104:107], a[0:3], a[24:27], v[104:107]\n\t"
  "v_mfma_f32_16x16x32_bf16 v[108:111], a[0:3], a[28:31], v[108:111]\n\t"
  "ds_read_b128 a[60:63], v25 offset:25600\n\t"
  "v_mfma_f32_16x16x32_bf16 v[112:115], a[0:3], a[32:35], v[112:115]\n\t"
  "ds_read_b128 a[64:67], v25 offset:26624\n\t"
  "ds_read_b128 a[68:71], v25 offset:27648\n\t"
  "v_mfma_f32_16x16x32_bf16 v[116:119], a[0:3], a[36:39], v[116:119]\n\t"
  "ds_read_b128 a[72:75], v25 offset:28672\n\t"
  "ds_read_b128 a[76:79], v25 offset:29696\n\t"
  "v_mfma_f32_16x16x32_bf16 v[120:123], a[4:7], a[20:23], v[120:123]\n\t"
  "ds_read_b64_tr_b16 a[40:41], v28 offset:36864\n\t"
  "buffer_load_dwordx4 v23, s[24:27], s41 offen offset:1024 lds\n\t"
  "v_mfma_f32_16x16x32_bf16 v[124:127], a[4:7], a[24:27], v[124:127]\n\t"
  "v_mfma_f32_16x16x32_bf16 v[128:131], a[4:7], a[28:31], v[128:131]\n\t"
  "ds_read_b64_tr_b16 a[42:43], v28 offset:43008\n\t"
  "v_mfma_f32_16x16x32_bf16 v[132:135], a[4:7], a[32:35], v[132:135]\n\t"
  "ds_read_b64_tr_b16 a[44:45], v29 offset:36864\n\t"
  "ds_read_b64_tr_b16 a[46:47], v29 offset:43008\n\t"
  "v_mfma_f32_16x16x32_bf16 v[136:139], a[4:7], a[36:39], v[136:139]\n\t"
  "ds_read_b64_tr_b16 a[48:49], v30 offset:36864\n\t"
  "buffer_load_dwordx4 v24, s[24:27], s41 offen offset:2048 lds\n\t"
  "v_mfma_f32_16x16x32_bf16 v[140:143], a[8:11], a[20:23], v[140:143]\n\t"
  "v_mfma_f32_16x16x32_bf16 v[144:147], a[8:11], a[24:27], v[144:147]\n\t"
  "ds_read_b64_tr_b16 a[50:51], v30 offset:43008\n\t"
  "ds_read_b64_tr_b16 a[52:53], v31 offset:36864\n\t"
  "v_mfma_f32_16x16x32_bf16 v[148:151], a[8:11], a[28:31], v[148:151]\n\t"
  "ds_read_b64_tr_b16 a[54:55], v31 offset:43008\n\t"
  "ds_read_b64_tr_b16 a[56:57], v32 offset:36864\n\t"
  "v_mfma_f32_16x16x32_bf16 v[152:155], a[8:11], a[32:35], v[152:155]\n\t"
  "s_add_u32 m0, s46, 77824\n\t"
  "s_add_u32 s41, s55, 286720\n\t"
  "buffer_load_dwordx4 v22, s[24:27], s41 offen offset:0 lds\n\t"
  "v_mfma_f32_16x16x32_bf16 v[156:159], a[8:11], a[36:39], v[156:159]\n\t"
  "v_mfma_f32_16x16x32_bf16 v[160:163], a[12:15], a[20:23], v[160:163]\n\t"
  "v_mfma_f32_16x16x32_bf16 v[164:167], a[12:15], a[24:27], v[164:167]\n\t"
  "ds_read_b64_tr_b16 a[58:59], v32 offset:43008\n\t"
  "buffer_load_dwordx4 v23, s[24:27], s41 offen offset:1024 lds\n\t"
  "v_mfma_f32_16x16x32_bf16 v[168:171], a[12:15], a[28:31], v[168:171]\n\t"
  "v_mfma_f32_16x16x32_bf16 v[172:175], a[12:15], a[32:35], v[172:175]\n\t"
  "buffer_load_dwordx4 v24, s[24:27], s41 offen offset:2048 lds\n\t"
  "v_mfma_f32_16x16x32_bf16 v[176:179], a[12:15], a[36:39], v[176:179]\n\t"
  "v_mfma_f32_16x16x32_bf16 v[180:183], a[16:19], a[20:23], v[180:183]\n\t"
  "v_mfma_f32_16x16x32_bf16 v[184:187], a[16:19], a[24:27], v[184:187]\n\t"
  "v_mfma_f32_16x16x32_bf16 v[188:191], a[16:19], a[28:31], v[188:191]\n\t"
  "v_mfma_f32_16x16x32_bf16 v[192:195], a[16:19], a[32:35], v[192:195]\n\t"
  "v_mfma_f32_16x16x32_bf16 v[196:199], a[16:19], a[36:39], v[196:199]\n\t"
  "s_waitcnt vmcnt(6) lgkmcnt(0)\n\t"
  "s_barrier\n\t"
  "v_mfma_f32_16x16x32_bf16 v[100:103], a[40:43], a[60:63], v[100:103]\n\t"
  "ds_read_b128 a[20:23], v25 offset:40960\n\t"
  "v_mfma_f32_16x16x32_bf16 v[104:107], a[40:43], a[64:67], v[104:107]\n\t"
  "ds_read_b128 a[24:27], v25 offset:41984\n\t"
  "v_mfma_f32_16x16x32_bf16 v[108:111], a[40:43], a[68:71], v[108:111]\n\t"
  "ds_read_b128 a[28:31], v25 offset:43008\n\t"
  "v_mfma_f32_16x16x32_bf16 v[112:115], a[40:43], a[72:75], v[112:115]\n\t"
  "ds_read_b128 a[32:35], v25 offset:44032\n\t"
  "ds_read_b128 a[36:39], v25 offset:45056\n\t"
  "v_mfma_f32_16x16x32_bf16 v[116:119], a[40:43], a[76:79], v[116:119]\n\t"
  "ds_read_b64_tr_b16 a[0:1], v28 offset:49152\n\t"
  "v_mfma_f32_16x16x32_bf16 v[120:123], a[44:47], a[60:63], v[120:123]\n\t"
  "ds_read_b64_tr_b16 a[2:3], v28 offset:55296\n\t"
  "v_mfma_f32_16x16x32_bf16 v[124:127], a[44:47], a[64:67], v[124:127]\n\t"
  "ds_read_b64_tr_b16 a[4:5], v29 offset:49152\n\t"
  "ds_read_b64_tr_b16 a[6:7], v29 offset:55296\n\t"
  "v_mfma_f32_16x16x32_bf16 v[128:131], a[44:47], a[68:71], v[128:131]\n\t"
  "ds_read_b64_tr_b16 a[8:9], v30 offset:49152\n\t"
  "v_mfma_f32_16x16x32_bf16 v[132:135], a[44:47], a[72:75], v[132:135]\n\t"
  "ds_read_b64_tr_b16 a[10:11], v30 offset:55296\n\t"
  "v_mfma_f32_16x16x32_bf16 v[136:139], a[44:47], a[76:79], v[136:139]\n\t"
  "ds_read_b64_tr_b16 a[12:13], v31 offset:49152\n\t"
  "ds_read_b64_tr_b16 a[14:15], v31 offset:55296\n\t"
  "v_mfma_f32_16x16x32_bf16 v[140:143], a[48:51], a[60:63], v[140:143]\n\t"
  "ds_read_b64_tr_b16 a[16:17], v32 offset:49152\n\t"
  "v_mfma_f32_16x16x32_bf16 v[144:147], a[48:51], a[64:67], v[144:147]\n\t"
  "ds_read_b64_tr_b16 a[18:19], v32 offset:55296\n\t"
  "v_mfma_f32_16x16x32_bf16 v[148:151], a[48:51], a[68:71], v[148:151]\n\t"
  "v_mfma_f32_16x16x32_bf16 v[152:155], a[48:51], a[72:75], v[152:155]\n\t"
  "v_mfma_f32_16x16x32_bf16 v[156:159], a[48:51], a[76:79], v[156:159]\n\t"
  "v_mfma_f32_16x16x32_bf16 v[160:163], a[52:55], a[60:63], v[160:163]\n\t"
  "v_mfma_f32_16x16x32_bf16 v[164:167], a[52:55], a[64:67], v[164:167]\n\t"
  "v_mfma_f32_16x16x32_bf16 v[168:171], a[52:55], a[68:71], v[168:171]\n\t"
  "v_mfma_f32_16x16x32_bf16 v[172:175], a[52:55], a[72:75], v[172:175]\n\t"
  "v_mfma_f32_16x16x32_bf16 v[176:179], a[52:55], a[76:79], v[176:179]\n\t"
  "v_mfma_f32_16x16x32_bf16 v[180:183], a[56:59], a[60:63], v[180:183]\n\t"
  "v_mfma_f32_16x16x32_bf16 v[184:187], a[56:59], a[64:67], v[184:187]\n\t"
  "v_mfma_f32_16x16x32_bf16 v[188:191], a[56:59], a[68:71], v[188:191]\n\t"
  "v_mfma_f32_16x16x32_bf16 v[192:195], a[56:59], a[72:75], v[192:195]\n\t"
  "v_mfma_f32_16x16x32_bf16 v[196:199], a[56:59], a[76:79], v[196:199]\n\t"
  "s_waitcnt lgkmcnt(0)\n\t"
  "v_mfma_f32_16x16x32_bf16 v[100:103], a[0:3], a[20:23], v[100:103]\n\t"
  "ds_read_b128 a[60:63], v25 offset:46080\n\t"
  "v_mfma_f32_16x16x32_bf16 v[104:107], a[0:3], a[24:27], v[104:107]\n\t"
  "ds_read_b128 a[64:67], v25 offset:47104\n\t"
  "v_mfma_f32_16x16x32_bf16 v[108:111], a[0:3], a[28:31], v[108:111]\n\t"
  "ds_read_b128 a[68:71], v25 offset:48128\n\t"
  "v_mfma_f32_16x16x32_bf16 v[112:115], a[0:3], a[32:35], v[112:115]\n\t"
  "ds_read_b128 a[72:75], v25 offset:49152\n\t"
  "ds_read_b128 a[76:79], v25 offset:50176\n\t"
  "v_mfma_f32_16x16x32_bf16 v[116:119], a[0:3], a[36:39], v[116:119]\n\t"
  "ds_read_b64_tr_b16 a[40:41], v28 offset:61440\n\t"
  "v_mfma_f32_16x16x32_bf16 v[120:123], a[4:7], a[20:23], v[120:123]\n\t"
  "ds_read_b64_tr_b16 a[42:43], v33 offset:18432\n\t"
  "v_mfma_f32_16x16x32_bf16 v[124:127], a[4:7], a[24:27], v[124:127]\n\t"
  "ds_read_b64_tr_b16 a[44:45], v29 offset:61440\n\t"
  "ds_read_b64_tr_b16 a[46:47], v34 offset:18432\n\t"
  "v_mfma_f32_16x16x32_bf16 v[128:131], a[4:7], a[28:31], v[128:131]\n\t"
  "ds_read_b64_tr_b16 a[48:49], v30 offset:61440\n\t"
  "v_mfma_f32_16x16x32_bf16 v[132:135], a[4:7], a[32:35], v[132:135]\n\t"
  "ds_read_b64_tr_b16 a[50:51], v35 offset:18432\n\t"
  "v_mfma_f32_16x16x32_bf16 v[136:139], a[4:7], a[36:39], v[136:139]\n\t"
  "ds_read_b64_tr_b16 a[52:53], v31 offset:61440\n\t"
  "ds_read_b64_tr_b16 a[54:55], v36 offset:18432\n\t"
  "v_mfma_f32_16x16x32_bf16 v[140:143], a[8:11], a[20:23], v[140:143]\n\t"
  "ds_read_b64_tr_b16 a[56:57], v32 offset:61440\n\t"
  "v_mfma_f32_16x16x32_bf16 v[144:147], a[8:11], a[24:27], v[144:147]\n\t"
  "ds_read_b64_tr_b16 a[58:59], v37 offset:18432\n\t"
  "v_mfma_f32_16x16x32_bf16 v[148:151], a[8:11], a[28:31], v[148:151]\n\t"
  "v_mfma_f32_16x16x32_bf16 v[152:155], a[8:11], a[32:35], v[152:155]\n\t"
  "v_mfma_f32_16x16x32_bf16 v[156:159], a[8:11], a[36:39], v[156:159]\n\t"
  "v_mfma_f32_16x16x32_bf16 v[160:163], a[12:15], a[20:23], v[160:163]\n\t"
  "v_mfma_f32_16x16x32_bf16 v[164:167], a[12:15], a[24:27], v[164:167]\n\t"
  "v_mfma_f32_16x16x32_bf16 v[168:171], a[12:15], a[28:31], v[168:171]\n\t"
  "v_mfma_f32_16x16x32_bf16 v[172:175], a[12:15], a[32:35], v[172:175]\n\t"
  "v_mfma_f32_16x16x32_bf16 v[176:179], a[12:15], a[36:39], v[176:179]\n\t"
  "v_mfma_f32_16x16x32_bf16 v[180:183], a[16:19], a[20:23], v[180:183]\n\t"
  "v_mfma_f32_16x16x32_bf16 v[184:187], a[16:19], a[24:27], v[184:187]\n\t"
  "v_mfma_f32_16x16x32_bf16 v[188:191], a[16:19], a[28:31], v[188:191]\n\t"
  "v_mfma_f32_16x16x32_bf16 v[192:195], a[16:19], a[32:35], v[192:195]\n\t"
  "v_mfma_f32_16x16x32_bf16 v[196:199], a[16:19], a[36:39], v[196:199]\n\t"
  "s_waitcnt vmcnt(0) lgkmcnt(0)\n\t"
  "s_barrier\n\t"
  "v_mfma_f32_16x16x32_bf16 v[100:103], a[40:43], a[60:63], v[100:103]\n\t"
  "ds_read_b128 a[20:23], v26 offset:0\n\t"
  "v_mfma_f32_16x16x32_bf16 v[104:107], a[40:43], a[64:67], v[104:107]\n\t"
  "ds_read_b128 a[24:27], v26 offset:1024\n\t"
  "v_mfma_f32_16x16x32_bf16 v[108:111], a[40:43], a[68:71], v[108:111]\n\t"
  "ds_read_b128 a[28:31], v26 offset:2048\n\t"
  "v_mfma_f32_16x16x32_bf16 v[112:115], a[40:43], a[72:75], v[112:115]\n\t"
  "ds_read_b128 a[32:35], v26 offset:3072\n\t"
  "ds_read_b128 a[36:39], v26 offset:4096\n\t"
  "v_mfma_f32_16x16x32_bf16 v[116:119], a[40:43], a[76:79], v[116:119]\n\t"
  "ds_read_b64_tr_b16 a[0:1], v28 offset:0\n\t"
  "v_mfma_f32_16x16x32_bf16 v[120:123], a[44:47], a[60:63], v[120:123]\n\t"
  "ds_read_b64_tr_b16 a[2:3], v28 offset:6144\n\t"
  "v_mfma_f32_16x16x32_bf16 v[124:127], a[44:47], a[64:67], v[124:127]\n\t"
  "ds_read_b64_tr_b16 a[4:5], v29 offset:0\n\t"
  "ds_read_b64_tr_b16 a[6:7], v29 offset:6144\n\t"
  "v_mfma_f32_16x16x32_bf16 v[128:131], a[44:47], a[68:71], v[128:131]\n\t"
  "ds_read_b64_tr_b16 a[8:9], v30 offset:0\n\t"
  "v_mfma_f32_16x16x32_bf16 v[132:135], a[44:47], a[72:75], v[132:135]\n\t"
  "ds_read_b64_tr_b16 a[10:11], v30 offset:6144\n\t"
  "v_mfma_f32_16x16x32_bf16 v[136:139], a[44:47], a[76:79], v[136:139]\n\t"
  "ds_read_b64_tr_b16 a[12:13], v31 offset:0\n\t"
  "ds_read_b64_tr_b16 a[14:15], v31 offset:6144\n\t"
  "v_mfma_f32_16x16x32_bf16 v[140:143], a[48:51], a[60:63], v[140:143]\n\t"
  "ds_read_b64_tr_b16 a[16:17], v32 offset:0\n\t"
  "v_mfma_f32_16x16x32_bf16 v[144:147], a[48:51], a[64:67], v[144:147]\n\t"
  "ds_read_b64_tr_b16 a[18:19], v32 offset:6144\n\t"
  "v_mfma_f32_16x16x32_bf16 v[148:151], a[48:51], a[68:71], v[148:151]\n\t"
  "v_mfma_f32_16x16x32_bf16 v[152:155], a[48:51], a[72:75], v[152:155]\n\t"
  "v_mfma_f32_16x16x32_bf16 v[156:159], a[48:51], a[76:79], v[156:159]\n\t"
  "v_mfma_f32_16x16x32_bf16 v[160:163], a[52:55], a[60:63], v[160:163]\n\t"
  "v_mfma_f32_16x16x32_bf16 v[164:167], a[52:55], a[64:67], v[164:167]\n\t"
  "v_mfma_f32_16x16x32_bf16 v[168:171], a[52:55], a[68:71], v[168:171]\n\t"
  "v_mfma_f32_16x16x32_bf16 v[172:175], a[52:55], a[72:75], v[172:175]\n\t"
  "v_mfma_f32_16x16x32_bf16 v[176:179], a[52:55], a[76:79], v[176:179]\n\t"
  "v_mfma_f32_16x16x32_bf16 v[180:183], a[56:59], a[60:63], v[180:183]\n\t"
  "v_mfma_f32_16x16x32_bf16 v[184:187], a[56:59], a[64:67], v[184:187]\n\t"
  "v_mfma_f32_16x16x32_bf16 v[188:191], a[56:59], a[68:71], v[188:191]\n\t"
  "v_mfma_f32_16x16x32_bf16 v[192:195], a[56:59], a[72:75], v[192:195]\n\t"
  "v_mfma_f32_16x16x32_bf16 v[196:199], a[56:59], a[76:79], v[196:199]\n\t"
  "s_waitcnt lgkmcnt(0)\n\t"
  "v_mfma_f32_16x16x32_bf16 v[100:103], a[0:3], a[20:23], v[100:103]\n\t"
  "ds_read_b128 a[60:63], v26 offset:5120\n\t"
  "v_mfma_f32_16x16x32_bf16 v[104:107], a[0:3], a[24:27], v[104:107]\n\t"
  "ds_read_b128 a[64:67], v26 offset:6144\n\t"
  "v_mfma_f32_16x16x32_bf16 v[108:111], a[0:3], a[28:31], v[108:111]\n\t"
  "ds_read_b128 a[68:71], v26 offset:7168\n\t"
  "v_mfma_f32_16x16x32_bf16 v[112:115], a[0:3], a[32:35], v[112:115]\n\t"
  "ds_read_b128 a[72:75], v26 offset:8192\n\t"
  "ds_read_b128 a[76:79], v26 offset:9216\n\t"
  "v_mfma_f32_16x16x32_bf16 v[116:119], a[0:3], a[36:39], v[116:119]\n\t"
  "ds_read_b64_tr_b16 a[40:41], v28 offset:12288\n\t"
  "v_mfma_f32_16x16x32_bf16 v[120:123], a[4:7], a[20:23], v[120:123]\n\t"
  "ds_read_b64_tr_b16 a[42:43], v28 offset:18432\n\t"
  "v_mfma_f32_16x16x32_bf16 v[124:127], a[4:7], a[24:27], v[124:127]\n\t"
  "ds_read_b64_tr_b16 a[44:45], v29 offset:12288\n\t"
  "ds_read_b64_tr_b16 a[46:47], v29 offset:18432\n\t"
  "v_mfma_f32_16x16x32_bf16 v[128:131], a[4:7], a[28:31], v[128:131]\n\t"
  "ds_read_b64_tr_b16 a[48:49], v30 offset:12288\n\t"
  "v_mfma_f32_16x16x32_bf16 v[132:135], a[4:7], a[32:35], v[132:135]\n\t"
  "ds_read_b64_tr_b16 a[50:51], v30 offset:18432\n\t"
  "v_mfma_f32_16x16x32_bf16 v[136:139], a[4:7], a[36:39], v[136:139]\n\t"
  "ds_read_b64_tr_b16 a[52:53], v31 offset:12288\n\t"
  "ds_read_b64_tr_b16 a[54:55], v31 offset:18432\n\t"
  "v_mfma_f32_16x16x32_bf16 v[140:143], a[8:11], a[20:23], v[140:143]\n\t"
  "ds_read_b64_tr_b16 a[56:57], v32 offset:12288\n\t"
  "v_mfma_f32_16x16x32_bf16 v[144:147], a[8:11], a[24:27], v[144:147]\n\t"
  "ds_read_b64_tr_b16 a[58:59], v32 offset:18432\n\t"
  "v_mfma_f32_16x16x32_bf16 v[148:151], a[8:11], a[28:31], v[148:151]\n\t"
  "v_mfma_f32_16x16x32_bf16 v[152:155], a[8:11], a[32:35], v[152:155]\n\t"
  "v_mfma_f32_16x16x32_bf16 v[156:159], a[8:11], a[36:39], v[156:159]\n\t"
  "v_mfma_f32_16x16x32_bf16 v[160:163], a[12:15], a[20:23], v[160:163]\n\t"
  "v_mfma_f32_16x16x32_bf16 v[164:167], a[12:15], a[24:27], v[164:167]\n\t"
  "v_mfma_f32_16x16x32_bf16 v[168:171], a[12:15], a[28:31], v[168:171]\n\t"
  "v_mfma_f32_16x16x32_bf16 v[172:175], a[12:15], a[32:35], v[172:175]\n\t"
  "v_mfma_f32_16x16x32_bf16 v[176:179], a[12:15], a[36:39], v[176:179]\n\t"
  "v_mfma_f32_16x16x32_bf16 v[180:183], a[16:19], a[20:23], v[180:183]\n\t"
  "v_mfma_f32_16x16x32_bf16 v[184:187], a[16:19], a[24:27], v[184:187]\n\t"
  "v_mfma_f32_16x16x32_bf16 v[188:191], a[16:19], a[28:31], v[188:191]\n\t"
  "v_mfma_f32_16x16x32_bf16 v[192:195], a[16:19], a[32:35], v[192:195]\n\t"
  "v_mfma_f32_16x16x32_bf16 v[196:199], a[16:19], a[36:39], v[196:199]\n\t"
  "s_waitcnt lgkmcnt(0)\n\t"
  "v_mfma_f32_16x16x32_bf16 v[100:103], a[40:43], a[60:63], v[100:103]\n\t"
  "v_mfma_f32_16x16x32_bf16 v[104:107], a[40:43], a[64:67], v[104:107]\n\t"
  "v_mfma_f32_16x16x32_bf16 v[108:111], a[40:43], a[68:71], v[108:111]\n\t"
  "v_mfma_f32_16x16x32_bf16 v[112:115], a[40:43], a[72:75], v[112:115]\n\t"
  "v_mfma_f32_16x16x32_bf16 v[116:119], a[40:43], a[76:79], v[116:119]\n\t"
  "v_mfma_f32_16x16x32_bf16 v[120:123], a[44:47], a[60:63], v[120:123]\n\t"
  "v_mfma_f32_16x16x32_bf16 v[124:127], a[44:47], a[64:67], v[124:127]\n\t"
  "v_mfma_f32_16x16x32_bf16 v[128:131], a[44:47], a[68:71], v[128:131]\n\t"
  "v_mfma_f32_16x16x32_bf16 v[132:135], a[44:47], a[72:75], v[132:135]\n\t"
  "v_mfma_f32_16x16x32_bf16 v[136:139], a[44:47], a[76:79], v[136:139]\n\t"
  "v_mfma_f32_16x16x32_bf16 v[140:143], a[48:51], a[60:63], v[140:143]\n\t"
  "v_mfma_f32_16x16x32_bf16 v[144:147], a[48:51], a[64:67], v[144:147]\n\t"
  "v_mfma_f32_16x16x32_bf16 v[148:151], a[48:51], a[68:71], v[148:151]\n\t"
  "v_mfma_f32_16x16x32_bf16 v[152:155], a[48:51], a[72:75], v[152:155]\n\t"
  "v_mfma_f32_16x16x32_bf16 v[156:159], a[48:51], a[76:79], v[156:159]\n\t"
  "v_mfma_f32_16x16x32_bf16 v[160:163], a[52:55], a[60:63], v[160:163]\n\t"
  "v_mfma_f32_16x16x32_bf16 v[164:167], a[52:55], a[64:67], v[164:167]\n\t"
  "v_mfma_f32_16x16x32_bf16 v[168:171], a[52:55], a[68:71], v[168:171]\n\t"
  "v_mfma_f32_16x16x32_bf16 v[172:175], a[52:55], a[72:75], v[172:175]\n\t"
  "v_mfma_f32_16x16x32_bf16 v[176:179], a[52:55], a[76:79], v[176:179]\n\t"
  "v_mfma_f32_16x16x32_bf16 v[180:183], a[56:59], a[60:63], v[180:183]\n\t"
  "v_mfma_f32_16x16x32_bf16 v[184:187], a[56:59], a[64:67], v[184:187]\n\t"
  "v_mfma_f32_16x16x32_bf16 v[188:191], a[56:59], a[68:71], v[188:191]\n\t"
  "v_mfma_f32_16x16x32_bf16 v[192:195], a[56:59], a[72:75], v[192:195]\n\t"
  "v_mfma_f32_16x16x32_bf16 v[196:199], a[56:59], a[76:79], v[196:199]\n\t"
  "s_lshl_b32 s42, s47, 1\n\t"
  "s_sub_u32 s51, %8, s42\n\t"
  "s_and_b32 s51, s51, 3\n\t"
  "s_mul_i32 s43, %8, 20480\n\t"
  "s_cmp_eq_u32 s51, 0\n\t"
  "s_cbranch_scc1 40f\n\t"
  "s_add_u32 s44, s42, 0\n\t"
  "s_and_b32 s44, s44, 3\n\t"
  "s_mul_i32 s44, s44, 81920\n\t"
  "s_add_u32 s44, s44, s43\n\t"
  "s_nop 2\n\t"
  "v_cvt_pk_bf16_f32 v100, v100, v101\n\t"
  "v_cvt_pk_bf16_f32 v101, v102, v103\n\t"
  "v_cvt_pk_bf16_f32 v102, v120, v121\n\t"
  "v_cvt_pk_bf16_f32 v103, v122, v123\n\t"
  "buffer_store_dwordx4 v[100:103], v39, s[36:39], s44 offen offset:0 sc1\n\t"
  "v_cvt_pk_bf16_f32 v140, v140, v141\n\t"
  "v_cvt_pk_bf16_f32 v141, v142, v143\n\t"
  "v_cvt_pk_bf16_f32 v142, v160, v161\n\t"
  "v_cvt_pk_bf16_f32 v143, v162, v163\n\t"
  "buffer_store_dwordx4 v[140:143], v39, s[36:39], s44 offen offset:1024 sc1\n\t"
  "v_cvt_pk_bf16_f32 v180, v180, v181\n\t"
  "v_cvt_pk_bf16_f32 v181, v182, v183\n\t"
  "v_cvt_pk_bf16_f32 v182, v116, v117\n\t"
  "v_cvt_pk_bf16_f32 v183, v118, v119\n\t"
  "buffer_store_dwordx4 v[180:183], v39, s[36:39], s44 offen offset:2048 sc1\n\t"
  "v_cvt_pk_bf16_f32 v136, v136, v137\n\t"
  "v_cvt_pk_bf16_f32 v137, v138, v139\n\t"
  "v_cvt_pk_bf16_f32 v138, v156, v157\n\t"
  "v_cvt_pk_bf16_f32 v139, v158, v159\n\t"
  "buffer_store_dwordx4 v[136:139], v39, s[36:39], s44 offen offset:3072 sc1\n\t"
  "v_cvt_pk_bf16_f32 v176, v176, v177\n\t"
  "v_cvt_pk_bf16_f32 v177, v178, v179\n\t"
  "v_cvt_pk_bf16_f32 v178, v196, v197\n\t"
  "v_cvt_pk_bf16_f32 v179, v198, v199\n\t"
  "s_add_u32 s44, s44, 0x1000\n\t"
  "buffer_store_dwordx4 v[176:179], v39, s[36:39], s44 offen offset:0 sc1\n\t"
  "40:\n\t"
  "s_cmp_eq_u32 s51, 1\n\t"
  "s_cbranch_scc1 41f\n\t"
  "s_add_u32 s44, s42, 1\n\t"
  "s_and_b32 s44, s44, 3\n\t"
  "s_mul_i32 s44, s44, 81920\n\t"
  "s_add_u32 s44, s44, s43\n\t"
  "v_cvt_pk_bf16_f32 v104, v104, v105\n\t"
  "v_cvt_pk_bf16_f32 v105, v106, v107\n\t"
  "v_cvt_pk_bf16_f32 v106, v124, v125\n\t"
  "v_cvt_pk_bf16_f32 v107, v126, v127\n\t"
  "buffer_store_dwordx4 v[104:107], v39, s[36:39], s44 offen offset:0 sc1\n\t"
  "v_cvt_pk_bf16_f32 v144, v144, v145\n\t"
  "v_cvt_pk_bf16_f32 v145, v146, v147\n\t"
  "v_cvt_pk_bf16_f32 v146, v164, v165\n\t"
  "v_cvt_pk_bf16_f32 v147, v166, v167\n\t"
  "buffer_store_dwordx4 v[144:147], v39, s[36:39], s44 offen offset:1024 sc1\n\t"
  "v_cvt_pk_bf16_f32 v184, v184, v185\n\t"
  "v_cvt_pk_bf16_f32 v185, v186, v187\n\t"
  "buffer_store_dwordx4 v[184:187], v39, s[36:39], s44 offen offset:2048 sc1\n\t"
  "41:\n\t"
  "s_cmp_eq_u32 s51, 2\n\t"
  "s_cbranch_scc1 42f\n\t"
  "s_add_u32 s44, s42, 2\n\t"
  "s_and_b32 s44, s44, 3\n\t"
  "s_mul_i32 s44, s44, 81920\n\t"
  "s_add_u32 s44, s44, s43\n\t"
  "v_cvt_pk_bf16_f32 v108, v108, v109\n\t"
  "v_cvt_pk_bf16_f32 v109, v110, v111\n\t"
  "v_cvt_pk_bf16_f32 v110, v128, v129\n\t"
  "v_cvt_pk_bf16_f32 v111, v130, v131\n\t"
  "buffer_store_dwordx4 v[108:111], v39, s[36:39], s44 offen offset:0 sc1\n\t"
  "v_cvt_pk_bf16_f32 v148, v148, v149\n\t"
  "v_cvt_pk_bf16_f32 v149, v150, v151\n\t"
  "v_cvt_pk_bf16_f32 v150, v168, v169\n\t"
  "v_cvt_pk_bf16_f32 v151, v170, v171\n\t"
  "buffer_store_dwordx4 v[148:151], v39, s[36:39], s44 offen offset:1024 sc1\n\t"
  "v_cvt_pk_bf16_f32 v188, v188, v189\n\t"
  "v_cvt_pk_bf16_f32 v189, v190, v191\n\t"
  "buffer_store_dwordx4 v[188:191], v39, s[36:39], s44 offen offset:2048 sc1\n\t"
  "42:\n\t"
  "s_cmp_eq_u32 s51, 3\n\t"
  "s_cbranch_scc1 43f\n\t"
  "s_add_u32 s44, s42, 3\n\t"
  "s_and_b32 s44, s44, 3\n\t"
  "s_mul_i32 s44, s44, 81920\n\t"
  "s_add_u32 s44, s44, s43\n\t"
  "v_cvt_pk_bf16_f32 v112, v112, v113\n\t"
  "v_cvt_pk_bf16_f32 v113, v114, v115\n\t"
  "v_cvt_pk_bf16_f32 v114, v132, v133\n\t"
  "v_cvt_pk_bf16_f32 v115, v134, v135\n\t"
  "buffer_store_dwordx4 v[112:115], v39, s[36:39], s44 offen offset:0 sc1\n\t"
  "v_cvt_pk_bf16_f32 v152, v152, v153\n\t"
  "v_cvt_pk_bf16_f32 v153, v154, v155\n\t"
  "v_cvt_pk_bf16_f32 v154, v172, v173\n\t"
  "v_cvt_pk_bf16_f32 v155, v174, v175\n\t"
  "buffer_store_dwordx4 v[152:155], v39, s[36:39], s44 offen offset:1024 sc1\n\t"
  "v_cvt_pk_bf16_f32 v192, v192, v193\n\t"
  "v_cvt_pk_bf16_f32 v193, v194, v195\n\t"
  "buffer_store_dwordx4 v[192:195], v39, s[36:39], s44 offen offset:2048 sc1\n\t"
  "43:\n\t"
  "s_cmp_lg_u32 s51, 0\n\t"
  "s_cbranch_scc1 50f\n\t"
  "v_mov_b32 v200, v100\n\t"
  "v_mov_b32 v201, v101\n\t"
  "v_mov_b32 v202, v102\n\t"
  "v_mov_b32 v203, v103\n\t"
  "v_mov_b32 v204, v120\n\t"
  "v_mov_b32 v205, v121\n\t"
  "v_mov_b32 v206, v122\n\t"
  "v_mov_b32 v207, v123\n\t"
  "v_mov_b32 v208, v140\n\t"
  "v_mov_b32 v209, v141\n\t"
  "v_mov_b32 v210, v142\n\t"
  "v_mov_b32 v211, v143\n\t"
  "v_mov_b32 v212, v160\n\t"
  "v_mov_b32 v213, v161\n\t"
  "v_mov_b32 v214, v162\n\t"
  "v_mov_b32 v215, v163\n\t"
  "v_mov_b32 v216, v180\n\t"
  "v_mov_b32 v217, v181\n\t"
  "v_mov_b32 v218, v182\n\t"
  "v_mov_b32 v219, v183\n\t"
  "v_mov_b32 v220, v116\n\t"
  "v_mov_b32 v221, v117\n\t"
  "v_mov_b32 v222, v118\n\t"
  "v_mov_b32 v223, v119\n\t"
  "v_mov_b32 v224, v136\n\t"
  "v_mov_b32 v225, v137\n\t"
  "v_mov_b32 v226, v138\n\t"
  "v_mov_b32 v227, v139\n\t"
  "v_mov_b32 v228, v156\n\t"
  "v_mov_b32 v229, v157\n\t"
  "v_mov_b32 v230, v158\n\t"
  "v_mov_b32 v231, v159\n\t"
  "v_mov_b32 v232, v176\n\t"
  "v_mov_b32 v233, v177\n\t"
  "v_mov_b32 v234, v178\n\t"
  "v_mov_b32 v235, v179\n\t"
  "v_mov_b32 v236, v196\n\t"
  "v_mov_b32 v237, v197\n\t"
  "v_mov_b32 v238, v198\n\t"
  "v_mov_b32 v239, v199\n\t"
  "s_branch 59f\n\t"
  "50:\n\t"
  "s_cmp_lg_u32 s51, 1\n\t"
  "s_cbranch_scc1 51f\n\t"
  "v_mov_b32 v200, v104\n\t"
  "v_mov_b32 v201, v105\n\t"
  "v_mov_b32 v202, v106\n\t"
  "v_mov_b32 v203, v107\n\t"
  "v_mov_b32 v204, v124\n\t"
  "v_mov_b32 v205, v125\n\t"
  "v_mov_b32 v206, v126\n\t"
  "v_mov_b32 v207, v127\n\t"
  "v_mov_b32 v208, v144\n\t"
  "v_mov_b32 v209, v145\n\t"
  "v_mov_b32 v210, v146\n\t"
  "v_mov_b32 v211, v147\n\t"
  "v_mov_b32 v212, v164\n\t"
  "v_mov_b32 v213, v165\n\t"
  "v_mov_b32 v214, v166\n\t"
  "v_mov_b32 v215, v167\n\t"
  "v_mov_b32 v216, v184\n\t"
  "v_mov_b32 v217, v185\n\t"
  "v_mov_b32 v218, v186\n\t"
  "v_mov_b32 v219, v187\n\t"
  "s_branch 59f\n\t"
  "51:\n\t"
  "s_cmp_lg_u32 s51, 2\n\t"
  "s_cbranch_scc1 52f\n\t"
  "v_mov_b32 v200, v108\n\t"
  "v_mov_b32 v201, v109\n\t"
  "v_mov_b32 v202, v110\n\t"
  "v_mov_b32 v203, v111\n\t"
  "v_mov_b32 v204, v128\n\t"
  "v_mov_b32 v205, v129\n\t"
  "v_mov_b32 v206, v130\n\t"
  "v_mov_b32 v207, v131\n\t"
  "v_mov_b32 v208, v148\n\t"
  "v_mov_b32 v209, v149\n\t"
  "v_mov_b32 v210, v150\n\t"
  "v_mov_b32 v211, v151\n\t"
  "v_mov_b32 v212, v168\n\t"
  "v_mov_b32 v213, v169\n\t"
  "v_mov_b32 v214, v170\n\t"
  "v_mov_b32 v215, v171\n\t"
  "v_mov_b32 v216, v188\n\t"
  "v_mov_b32 v217, v189\n\t"
  "v_mov_b32 v218, v190\n\t"
  "v_mov_b32 v219, v191\n\t"
  "s_branch 59f\n\t"
  "52:\n\t"
  "v_mov_b32 v200, v112\n\t"
  "v_mov_b32 v201, v113\n\t"
  "v_mov_b32 v202, v114\n\t"
  "v_mov_b32 v203, v115\n\t"
  "v_mov_b32 v204, v132\n\t"
  "v_mov_b32 v205, v133\n\t"
  "v_mov_b32 v206, v134\n\t"
  "v_mov_b32 v207, v135\n\t"
  "v_mov_b32 v208, v152\n\t"
  "v_mov_b32 v209, v153\n\t"
  "v_mov_b32 v210, v154\n\t"
  "v_mov_b32 v211, v155\n\t"
  "v_mov_b32 v212, v172\n\t"
  "v_mov_b32 v213, v173\n\t"
  "v_mov_b32 v214, v174\n\t"
  "v_mov_b32 v215, v175\n\t"
  "v_mov_b32 v216, v192\n\t"
  "v_mov_b32 v217, v193\n\t"
  "v_mov_b32 v218, v194\n\t"
  "v_mov_b32 v219, v195\n\t"
  "59:\n\t"
  "s_mul_i32 s43, %8, 81920\n\t"
  "s_add_u32 s42, %8, 1\n\t"
  "s_and_b32 s42, s42, 3\n\t"
  "s_mul_i32 s42, s42, 20480\n\t"
  "s_add_u32 s52, s42, s43\n\t"
  "s_add_u32 s56, s52, 0x1000\n\t"
  "s_add_u32 s42, %8, 2\n\t"
  "s_and_b32 s42, s42, 3\n\t"
  "s_mul_i32 s42, s42, 20480\n\t"
  "s_add_u32 s53, s42, s43\n\t"
  "s_add_u32 s57, s53, 0x1000\n\t"
  "s_add_u32 s42, %8, 3\n\t"
  "s_and_b32 s42, s42, 3\n\t"
  "s_mul_i32 s42, s42, 20480\n\t"
  "s_add_u32 s54, s42, s43\n\t"
  "s_add_u32 s58, s54, 0x1000\n\t"
  "s_mul_i32 s40, s51, 0xa000\n\t"
  "s_mov_b32 s41, 163840\n\t"
  "v_mov_b32 v19, 0xffff0000\n\t"
  "s_waitcnt vmcnt(0)\n\t"
  "s_barrier\n\t"
  "s_cmp_lg_u32 %7, 0\n\t"
  "s_cbranch_scc1 77f\n\t"
  "s_mov_b64 exec, 15\n\t"
  "global_store_dword v72, v74, %13 sc1\n\t"
  "s_mov_b64 exec, -1\n\t"
  "77:\n\t"
  "s_mov_b32 s50, 0x400000\n\t"
  "78:\n\t"
  "global_load_dword v75, v73, %13 sc1\n\t"
  "s_and_b32 s44, s50, 15\n\t"
  "s_cmp_lg_u32 s44, 1\n\t"
  "s_cbranch_scc1 81f\n\t"
  "global_load_dword v74, v71, %13 sc1\n\t"
  "s_waitcnt vmcnt(0)\n\t"
  "s_cmp_lg_u32 %7, 0\n\t"
  "s_cbranch_scc1 82f\n\t"
  "s_mov_b64 exec, 15\n\t"
  "global_store_dword v72, v74, %13 sc1\n\t"
  "s_mov_b64 exec, -1\n\t"
  "82:\n\t"
  "81:\n\t"
  "s_waitcnt vmcnt(0)\n\t"
  "s_sub_u32 s50, s50, 1\n\t"
  "s_cbranch_scc1 80f\n\t"
  "79:\n\t"
  "v_cmp_ne_u32 vcc, %12, v75\n\t"
  "s_cbranch_vccnz 78b\n\t"
  "80:\n\t"
  "s_cmp_eq_u32 s51, 0\n\t"
  "s_cbranch_scc1 63f\n\t"
  "buffer_load_dwordx4 v[100:103], v39, s[36:39], s52 offen offset:0 sc1\n\t"
  "buffer_load_dwordx4 v[120:123], v39, s[36:39], s53 offen offset:0 sc1\n\t"
  "buffer_load_dwordx4 v[140:143], v39, s[36:39], s54 offen offset:0 sc1\n\t"
  "buffer_load_dwordx4 v[104:107], v39, s[36:39], s52 offen offset:1024 sc1\n\t"
  "buffer_load_dwordx4 v[124:127], v39, s[36:39], s53 offen offset:1024 sc1\n\t"
  "buffer_load_dwordx4 v[144:147], v39, s[36:39], s54 offen offset:1024 sc1\n\t"
  "buffer_load_dwordx4 v[108:111], v39, s[36:39], s52 offen offset:2048 sc1\n\t"
  "buffer_load_dwordx4 v[128:131], v39, s[36:39], s53 offen offset:2048 sc1\n\t"
  "buffer_load_dwordx4 v[148:151], v39, s[36:39], s54 offen offset:2048 sc1\n\t"
  "global_load_dword v74, v71, %13 sc1\n\t"
  "s_waitcnt vmcnt(7)\n\t"
  "v_lshlrev_b32 v17, 16, v100\n\t"
  "v_and_b32 v18, v19, v100\n\t"
  "v_add_f32 v200, v200, v17\n\t"
  "v_add_f32 v201, v201, v18\n\t"
  "v_lshlrev_b32 v17, 16, v101\n\t"
  "v_and_b32 v18, v19, v101\n\t"
  "v_add_f32 v202, v202, v17\n\t"
  "v_add_f32 v203, v203, v18\n\t"
  "v_lshlrev_b32 v17, 16, v120\n\t"
  "v_and_b32 v18, v19, v120\n\t"
  "v_add_f32 v200, v200, v17\n\t"
  "v_add_f32 v201, v201, v18\n\t"
  "v_lshlrev_b32 v17, 16, v121\n\t"
  "v_and_b32 v18, v19, v121\n\t"
  "v_add_f32 v202, v202, v17\n\t"
  "v_add_f32 v203, v203, v18\n\t"
  "v_lshlrev_b32 v17, 16, v140\n\t"
  "v_and_b32 v18, v19, v140\n\t"
  "v_add_f32 v200, v200, v17\n\t"
  "v_add_f32 v201, v201, v18\n\t"
  "v_lshlrev_b32 v17, 16, v141\n\t"
  "v_and_b32 v18, v19, v141\n\t"
  "v_add_f32 v202, v202, v17\n\t"
  "v_add_f32 v203, v203, v18\n\t"
  "v_cvt_pk_bf16_f32 v200, v200, v201\n\t"
  "v_cvt_pk_bf16_f32 v201, v202, v203\n\t"
  "buffer_store_dwordx2 v[200:201], v38, s[28:31], s40 offen offset:0\n\t"
  "v_lshlrev_b32 v17, 16, v102\n\t"
  "v_and_b32 v18, v19, v102\n\t"
  "v_add_f32 v204, v204, v17\n\t"
  "v_add_f32 v205, v205, v18\n\t"
  "v_lshlrev_b32 v17, 16, v103\n\t"
  "v_and_b32 v18, v19, v103\n\t"
  "v_add_f32 v206, v206, v17\n\t"
  "v_add_f32 v207, v207, v18\n\t"
  "v_lshlrev_b32 v17, 16, v122\n\t"
  "v_and_b32 v18, v19, v122\n\t"
  "v_add_f32 v204, v204, v17\n\t"
  "v_add_f32 v205, v205, v18\n\t"
  "v_lshlrev_b32 v17, 16, v123\n\t"
  "v_and_b32 v18, v19, v123\n\t"
  "v_add_f32 v206, v206, v17\n\t"
  "v_add_f32 v207, v207, v18\n\t"
  "v_lshlrev_b32 v17, 16, v142\n\t"
  "v_and_b32 v18, v19, v142\n\t"
  "v_add_f32 v204, v204, v17\n\t"
  "v_add_f32 v205, v205, v18\n\t"
  "v_lshlrev_b32 v17, 16, v143\n\t"
  "v_and_b32 v18, v19, v143\n\t"
  "v_add_f32 v206, v206, v17\n\t"
  "v_add_f32 v207, v207, v18\n\t"
  "v_cvt_pk_bf16_f32 v204, v204, v205\n\t"
  "v_cvt_pk_bf16_f32 v205, v206, v207\n\t"
  "buffer_store_dwordx2 v[204:205], v38, s[28:31], s40 offen offset:32\n\t"
  "s_waitcnt vmcnt(6)\n\t"
  "v_lshlrev_b32 v17, 16, v104\n\t"
  "v_and_b32 v18, v19, v104\n\t"
  "v_add_f32 v208, v208, v17\n\t"
  "v_add_f32 v209, v209, v18\n\t"
  "v_lshlrev_b32 v17, 16, v105\n\t"
  "v_and_b32 v18, v19, v105\n\t"
  "v_add_f32 v210, v210, v17\n\t"
  "v_add_f32 v211, v211, v18\n\t"
  "v_lshlrev_b32 v17, 16, v124\n\t"
  "v_and_b32 v18, v19, v124\n\t"
  "v_add_f32 v208, v208, v17\n\t"
  "v_add_f32 v209, v209, v18\n\t"
  "v_lshlrev_b32 v17, 16, v125\n\t"
  "v_and_b32 v18, v19, v125\n\t"
  "v_add_f32 v210, v210, v17\n\t"
  "v_add_f32 v211, v211, v18\n\t"
  "v_lshlrev_b32 v17, 16, v144\n\t"
  "v_and_b32 v18, v19, v144\n\t"
  "v_add_f32 v208, v208, v17\n\t"
  "v_add_f32 v209, v209, v18\n\t"
  "v_lshlrev_b32 v17, 16, v145\n\t"
  "v_and_b32 v18, v19, v145\n\t"
  "v_add_f32 v210, v210, v17\n\t"
  "v_add_f32 v211, v211, v18\n\t"
  "v_cvt_pk_bf16_f32 v208, v208, v209\n\t"
  "v_cvt_pk_bf16_f32 v209, v210, v211\n\t"
  "buffer_store_dwordx2 v[208:209], v38, s[28:31], s40 offen offset:64\n\t"
  "v_lshlrev_b32 v17, 16, v106\n\t"
  "v_and_b32 v18, v19, v106\n\t"
  "v_add_f32 v212, v212, v17\n\t"
  "v_add_f32 v213, v213, v18\n\t"
  "v_lshlrev_b32 v17, 16, v107\n\t"
  "v_and_b32 v18, v19, v107\n\t"
  "v_add_f32 v214, v214, v17\n\t"
  "v_add_f32 v215, v215, v18\n\t"
  "v_lshlrev_b32 v17, 16, v126\n\t"
  "v_and_b32 v18, v19, v126\n\t"
  "v_add_f32 v212, v212, v17\n\t"
  "v_add_f32 v213, v213, v18\n\t"
  "v_lshlrev_b32 v17, 16, v127\n\t"
  "v_and_b32 v18, v19, v127\n\t"
  "v_add_f32 v214, v214, v17\n\t"
  "v_add_f32 v215, v215, v18\n\t"
  "v_lshlrev_b32 v17, 16, v146\n\t"
  "v_and_b32 v18, v19, v146\n\t"
  "v_add_f32 v212, v212, v17\n\t"
  "v_add_f32 v213, v213, v18\n\t"
  "v_lshlrev_b32 v17, 16, v147\n\t"
  "v_and_b32 v18, v19, v147\n\t"
  "v_add_f32 v214, v214, v17\n\t"
  "v_add_f32 v215, v215, v18\n\t"
  "v_cvt_pk_bf16_f32 v212, v212, v213\n\t"
  "v_cvt_pk_bf16_f32 v213, v214, v215\n\t"
  "buffer_store_dwordx2 v[212:213], v38, s[28:31], s40 offen offset:96\n\t"
  "s_waitcnt vmcnt(5)\n\t"
  "v_lshlrev_b32 v17, 16, v108\n\t"
  "v_and_b32 v18, v19, v108\n\t"
  "v_add_f32 v216, v216, v17\n\t"
  "v_add_f32 v217, v217, v18\n\t"
  "v_lshlrev_b32 v17, 16, v109\n\t"
  "v_and_b32 v18, v19, v109\n\t"
  "v_add_f32 v218, v218, v17\n\t"
  "v_add_f32 v219, v219, v18\n\t"
  "v_lshlrev_b32 v17, 16, v128\n\t"
  "v_and_b32 v18, v19, v128\n\t"
  "v_add_f32 v216, v216, v17\n\t"
  "v_add_f32 v217, v217, v18\n\t"
  "v_lshlrev_b32 v17, 16, v129\n\t"
  "v_and_b32 v18, v19, v129\n\t"
  "v_add_f32 v218, v218, v17\n\t"
  "v_add_f32 v219, v219, v18\n\t"
  "v_lshlrev_b32 v17, 16, v148\n\t"
  "v_and_b32 v18, v19, v148\n\t"
  "v_add_f32 v216, v216, v17\n\t"
  "v_add_f32 v217, v217, v18\n\t"
  "v_lshlrev_b32 v17, 16, v149\n\t"
  "v_and_b32 v18, v19, v149\n\t"
  "v_add_f32 v218, v218, v17\n\t"
  "v_add_f32 v219, v219, v18\n\t"
  "v_cvt_pk_bf16_f32 v216, v216, v217\n\t"
  "v_cvt_pk_bf16_f32 v217, v218, v219\n\t"
  "buffer_store_dwordx2 v[216:217], v38, s[28:31], s40 offen offset:128\n\t"
  "s_waitcnt vmcnt(5)\n\t"
  "s_cmp_lg_u32 %7, 0\n\t"
  "s_cbranch_scc1 83f\n\t"
  "s_mov_b64 exec, 15\n\t"
  "global_store_dword v72, v74, %13 sc1\n\t"
  "s_mov_b64 exec, -1\n\t"
  "83:\n\t"
  "s_branch 64f\n\t"
  "63:\n\t"
  "buffer_load_dwordx4 v[100:103], v39, s[36:39], s52 offen offset:0 sc1\n\t"
  "buffer_load_dwordx4 v[120:123], v39, s[36:39], s53 offen offset:0 sc1\n\t"
  "buffer_load_dwordx4 v[140:143], v39, s[36:39], s54 offen offset:0 sc1\n\t"
  "buffer_load_dwordx4 v[104:107], v39, s[36:39], s52 offen offset:1024 sc1\n\t"
  "buffer_load_dwordx4 v[124:127], v39, s[36:39], s53 offen offset:1024 sc1\n\t"
  "buffer_load_dwordx4 v[144:147], v39, s[36:39], s54 offen offset:1024 sc1\n\t"
  "buffer_load_dwordx4 v[108:111], v39, s[36:39], s52 offen offset:2048 sc1\n\t"
  "buffer_load_dwordx4 v[128:131], v39, s[36:39], s53 offen offset:2048 sc1\n\t"
  "buffer_load_dwordx4 v[148:151], v39, s[36:39], s54 offen offset:2048 sc1\n\t"
  "buffer_load_dwordx4 v[112:115], v39, s[36:39], s52 offen offset:3072 sc1\n\t"
  "buffer_load_dwordx4 v[132:135], v39, s[36:39], s53 offen offset:3072 sc1\n\t"
  "buffer_load_dwordx4 v[152:155], v39, s[36:39], s54 offen offset:3072 sc1\n\t"
  "buffer_load_dwordx4 v[116:119], v39, s[36:39], s56 offen offset:0 sc1\n\t"
  "buffer_load_dwordx4 v[136:139], v39, s[36:39], s57 offen offset:0 sc1\n\t"
  "buffer_load_dwordx4 v[156:159], v39, s[36:39], s58 offen offset:0 sc1\n\t"
  "global_load_dword v74, v71, %13 sc1\n\t"
  "s_waitcnt vmcnt(13)\n\t"
  "v_lshlrev_b32 v17, 16, v100\n\t"
  "v_and_b32 v18, v19, v100\n\t"
  "v_add_f32 v200, v200, v17\n\t"
  "v_add_f32 v201, v201, v18\n\t"
  "v_lshlrev_b32 v17, 16, v101\n\t"
  "v_and_b32 v18, v19, v101\n\t"
  "v_add_f32 v202, v202, v17\n\t"
  "v_add_f32 v203, v203, v18\n\t"
  "v_lshlrev_b32 v17, 16, v120\n\t"
  "v_and_b32 v18, v19, v120\n\t"
  "v_add_f32 v200, v200, v17\n\t"
  "v_add_f32 v201, v201, v18\n\t"
  "v_lshlrev_b32 v17, 16, v121\n\t"
  "v_and_b32 v18, v19, v121\n\t"
  "v_add_f32 v202, v202, v17\n\t"
  "v_add_f32 v203, v203, v18\n\t"
  "v_lshlrev_b32 v17, 16, v140\n\t"
  "v_and_b32 v18, v19, v140\n\t"
  "v_add_f32 v200, v200, v17\n\t"
  "v_add_f32 v201, v201, v18\n\t"
  "v_lshlrev_b32 v17, 16, v141\n\t"
  "v_and_b32 v18, v19, v141\n\t"
  "v_add_f32 v202, v202, v17\n\t"
  "v_add_f32 v203, v203, v18\n\t"
  "v_cvt_pk_bf16_f32 v200, v200, v201\n\t"
  "v_cvt_pk_bf16_f32 v201, v202, v203\n\t"
  "buffer_store_dwordx2 v[200:201], v38, s[28:31], s40 offen offset:0\n\t"
  "v_lshlrev_b32 v17, 16, v102\n\t"
  "v_and_b32 v18, v19, v102\n\t"
  "v_add_f32 v204, v204, v17\n\t"
  "v_add_f32 v205, v205, v18\n\t"
  "v_lshlrev_b32 v17, 16, v103\n\t"
  "v_and_b32 v18, v19, v103\n\t"
  "v_add_f32 v206, v206, v17\n\t"
  "v_add_f32 v207, v207, v18\n\t"
  "v_lshlrev_b32 v17, 16, v122\n\t"
  "v_and_b32 v18, v19, v122\n\t"
  "v_add_f32 v204, v204, v17\n\t"
  "v_add_f32 v205, v205, v18\n\t"
  "v_lshlrev_b32 v17, 16, v123\n\t"
  "v_and_b32 v18, v19, v123\n\t"
  "v_add_f32 v206, v206, v17\n\t"
  "v_add_f32 v207, v207, v18\n\t"
  "v_lshlrev_b32 v17, 16, v142\n\t"
  "v_and_b32 v18, v19, v142\n\t"
  "v_add_f32 v204, v204, v17\n\t"
  "v_add_f32 v205, v205, v18\n\t"
  "v_lshlrev_b32 v17, 16, v143\n\t"
  "v_and_b32 v18, v19, v143\n\t"
  "v_add_f32 v206, v206, v17\n\t"
  "v_add_f32 v207, v207, v18\n\t"
  "v_cvt_pk_bf16_f32 v204, v204, v205\n\t"
  "v_cvt_pk_bf16_f32 v205, v206, v207\n\t"
  "buffer_store_dwordx2 v[204:205], v38, s[28:31], s40 offen offset:32\n\t"
  "s_waitcnt vmcnt(12)\n\t"
  "v_lshlrev_b32 v17, 16, v104\n\t"
  "v_and_b32 v18, v19, v104\n\t"
  "v_add_f32 v208, v208, v17\n\t"
  "v_add_f32 v209, v209, v18\n\t"
  "v_lshlrev_b32 v17, 16, v105\n\t"
  "v_and_b32 v18, v19, v105\n\t"
  "v_add_f32 v210, v210, v17\n\t"
  "v_add_f32 v211, v211, v18\n\t"
  "v_lshlrev_b32 v17, 16, v124\n\t"
  "v_and_b32 v18, v19, v124\n\t"
  "v_add_f32 v208, v208, v17\n\t"
  "v_add_f32 v209, v209, v18\n\t"
  "v_lshlrev_b32 v17, 16, v125\n\t"
  "v_and_b32 v18, v19, v125\n\t"
  "v_add_f32 v210, v210, v17\n\t"
  "v_add_f32 v211, v211, v18\n\t"
  "v_lshlrev_b32 v17, 16, v144\n\t"
  "v_and_b32 v18, v19, v144\n\t"
  "v_add_f32 v208, v208, v17\n\t"
  "v_add_f32 v209, v209, v18\n\t"
  "v_lshlrev_b32 v17, 16, v145\n\t"
  "v_and_b32 v18, v19, v145\n\t"
  "v_add_f32 v210, v210, v17\n\t"
  "v_add_f32 v211, v211, v18\n\t"
  "v_cvt_pk_bf16_f32 v208, v208, v209\n\t"
  "v_cvt_pk_bf16_f32 v209, v210, v211\n\t"
  "buffer_store_dwordx2 v[208:209], v38, s[28:31], s40 offen offset:64\n\t"
  "v_lshlrev_b32 v17, 16, v106\n\t"
  "v_and_b32 v18, v19, v106\n\t"
  "v_add_f32 v212, v212, v17\n\t"
  "v_add_f32 v213, v213, v18\n\t"
  "v_lshlrev_b32 v17, 16, v107\n\t"
  "v_and_b32 v18, v19, v107\n\t"
  "v_add_f32 v214, v214, v17\n\t"
  "v_add_f32 v215, v215, v18\n\t"
  "v_lshlrev_b32 v17, 16, v126\n\t"
  "v_and_b32 v18, v19, v126\n\t"
  "v_add_f32 v212, v212, v17\n\t"
  "v_add_f32 v213, v213, v18\n\t"
  "v_lshlrev_b32 v17, 16, v127\n\t"
  "v_and_b32 v18, v19, v127\n\t"
  "v_add_f32 v214, v214, v17\n\t"
  "v_add_f32 v215, v215, v18\n\t"
  "v_lshlrev_b32 v17, 16, v146\n\t"
  "v_and_b32 v18, v19, v146\n\t"
  "v_add_f32 v212, v212, v17\n\t"
  "v_add_f32 v213, v213, v18\n\t"
  "v_lshlrev_b32 v17, 16, v147\n\t"
  "v_and_b32 v18, v19, v147\n\t"
  "v_add_f32 v214, v214, v17\n\t"
  "v_add_f32 v215, v215, v18\n\t"
  "v_cvt_pk_bf16_f32 v212, v212, v213\n\t"
  "v_cvt_pk_bf16_f32 v213, v214, v215\n\t"
  "buffer_store_dwordx2 v[212:213], v38, s[28:31], s40 offen offset:96\n\t"
  "s_waitcnt vmcnt(11)\n\t"
  "v_lshlrev_b32 v17, 16, v108\n\t"
  "v_and_b32 v18, v19, v108\n\t"
  "v_add_f32 v216, v216, v17\n\t"
  "v_add_f32 v217, v217, v18\n\t"
  "v_lshlrev_b32 v17, 16, v109\n\t"
  "v_and_b32 v18, v19, v109\n\t"
  "v_add_f32 v218, v218, v17\n\t"
  "v_add_f32 v219, v219, v18\n\t"
  "v_lshlrev_b32 v17, 16, v128\n\t"
  "v_and_b32 v18, v19, v128\n\t"
  "v_add_f32 v216, v216, v17\n\t"
  "v_add_f32 v217, v217, v18\n\t"
  "v_lshlrev_b32 v17, 16, v129\n\t"
  "v_and_b32 v18, v19, v129\n\t"
  "v_add_f32 v218, v218, v17\n\t"
  "v_add_f32 v219, v219, v18\n\t"
  "v_lshlrev_b32 v17, 16, v148\n\t"
  "v_and_b32 v18, v19, v148\n\t"
  "v_add_f32 v216, v216, v17\n\t"
  "v_add_f32 v217, v217, v18\n\t"
  "v_lshlrev_b32 v17, 16, v149\n\t"
  "v_and_b32 v18, v19, v149\n\t"
  "v_add_f32 v218, v218, v17\n\t"
  "v_add_f32 v219, v219, v18\n\t"
  "v_cvt_pk_bf16_f32 v216, v216, v217\n\t"
  "v_cvt_pk_bf16_f32 v217, v218, v219\n\t"
  "buffer_store_dwordx2 v[216:217], v38, s[28:31], s40 offen offset:128\n\t"
  "v_lshlrev_b32 v17, 16, v110\n\t"
  "v_and_b32 v18, v19, v110\n\t"
  "v_add_f32 v220, v220, v17\n\t"
  "v_add_f32 v221, v221, v18\n\t"
  "v_lshlrev_b32 v17, 16, v111\n\t"
  "v_and_b32 v18, v19, v111\n\t"
  "v_add_f32 v222, v222, v17\n\t"
  "v_add_f32 v223, v223, v18\n\t"
  "v_lshlrev_b32 v17, 16, v130\n\t"
  "v_and_b32 v18, v19, v130\n\t"
  "v_add_f32 v220, v220, v17\n\t"
  "v_add_f32 v221, v221, v18\n\t"
  "v_lshlrev_b32 v17, 16, v131\n\t"
  "v_and_b32 v18, v19, v131\n\t"
  "v_add_f32 v222, v222, v17\n\t"
  "v_add_f32 v223, v223, v18\n\t"
  "v_lshlrev_b32 v17, 16, v150\n\t"
  "v_and_b32 v18, v19, v150\n\t"
  "v_add_f32 v220, v220, v17\n\t"
  "v_add_f32 v221, v221, v18\n\t"
  "v_lshlrev_b32 v17, 16, v151\n\t"
  "v_and_b32 v18, v19, v151\n\t"
  "v_add_f32 v222, v222, v17\n\t"
  "v_add_f32 v223, v223, v18\n\t"
  "v_cvt_pk_bf16_f32 v220, v220, v221\n\t"
  "v_cvt_pk_bf16_f32 v221, v222, v223\n\t"
  "buffer_store_dwordx2 v[220:221], v38, s[28:31], s41 offen offset:0\n\t"
  "s_waitcnt vmcnt(10)\n\t"
  "v_lshlrev_b32 v17, 16, v112\n\t"
  "v_and_b32 v18, v19, v112\n\t"
  "v_add_f32 v224, v224, v17\n\t"
  "v_add_f32 v225, v225, v18\n\t"
  "v_lshlrev_b32 v17, 16, v113\n\t"
  "v_and_b32 v18, v19, v113\n\t"
  "v_add_f32 v226, v226, v17\n\t"
  "v_add_f32 v227, v227, v18\n\t"
  "v_lshlrev_b32 v17, 16, v132\n\t"
  "v_and_b32 v18, v19, v132\n\t"
  "v_add_f32 v224, v224, v17\n\t"
  "v_add_f32 v225, v225, v18\n\t"
  "v_lshlrev_b32 v17, 16, v133\n\t"
  "v_and_b32 v18, v19, v133\n\t"
  "v_add_f32 v226, v226, v17\n\t"
  "v_add_f32 v227, v227, v18\n\t"
  "v_lshlrev_b32 v17, 16, v152\n\t"
  "v_and_b32 v18, v19, v152\n\t"
  "v_add_f32 v224, v224, v17\n\t"
  "v_add_f32 v225, v225, v18\n\t"
  "v_lshlrev_b32 v17, 16, v153\n\t"
  "v_and_b32 v18, v19, v153\n\t"
  "v_add_f32 v226, v226, v17\n\t"
  "v_add_f32 v227, v227, v18\n\t"
  "v_cvt_pk_bf16_f32 v224, v224, v225\n\t"
  "v_cvt_pk_bf16_f32 v225, v226, v227\n\t"
  "buffer_store_dwordx2 v[224:225], v38, s[28:31], s41 offen offset:32\n\t"
  "v_lshlrev_b32 v17, 16, v114\n\t"
  "v_and_b32 v18, v19, v114\n\t"
  "v_add_f32 v228, v228, v17\n\t"
  "v_add_f32 v229, v229, v18\n\t"
  "v_lshlrev_b32 v17, 16, v115\n\t"
  "v_and_b32 v18, v19, v115\n\t"
  "v_add_f32 v230, v230, v17\n\t"
  "v_add_f32 v231, v231, v18\n\t"
  "v_lshlrev_b32 v17, 16, v134\n\t"
  "v_and_b32 v18, v19, v134\n\t"
  "v_add_f32 v228, v228, v17\n\t"
  "v_add_f32 v229, v229, v18\n\t"
  "v_lshlrev_b32 v17, 16, v135\n\t"
  "v_and_b32 v18, v19, v135\n\t"
  "v_add_f32 v230, v230, v17\n\t"
  "v_add_f32 v231, v231, v18\n\t"
  "v_lshlrev_b32 v17, 16, v154\n\t"
  "v_and_b32 v18, v19, v154\n\t"
  "v_add_f32 v228, v228, v17\n\t"
  "v_add_f32 v229, v229, v18\n\t"
  "v_lshlrev_b32 v17, 16, v155\n\t"
  "v_and_b32 v18, v19, v155\n\t"
  "v_add_f32 v230, v230, v17\n\t"
  "v_add_f32 v231, v231, v18\n\t"
  "v_cvt_pk_bf16_f32 v228, v228, v229\n\t"
  "v_cvt_pk_bf16_f32 v229, v230, v231\n\t"
  "buffer_store_dwordx2 v[228:229], v38, s[28:31], s41 offen offset:64\n\t"
  "s_waitcnt vmcnt(9)\n\t"
  "v_lshlrev_b32 v17, 16, v116\n\t"
  "v_and_b32 v18, v19, v116\n\t"
  "v_add_f32 v232, v232, v17\n\t"
  "v_add_f32 v233, v233, v18\n\t"
  "v_lshlrev_b32 v17, 16, v117\n\t"
  "v_and_b32 v18, v19, v117\n\t"
  "v_add_f32 v234, v234, v17\n\t"
  "v_add_f32 v235, v235, v18\n\t"
  "v_lshlrev_b32 v17, 16, v136\n\t"
  "v_and_b32 v18, v19, v136\n\t"
  "v_add_f32 v232, v232, v17\n\t"
  "v_add_f32 v233, v233, v18\n\t"
  "v_lshlrev_b32 v17, 16, v137\n\t"
  "v_and_b32 v18, v19, v137\n\t"
  "v_add_f32 v234, v234, v17\n\t"
  "v_add_f32 v235, v235, v18\n\t"
  "v_lshlrev_b32 v17, 16, v156\n\t"
  "v_and_b32 v18, v19, v156\n\t"
  "v_add_f32 v232, v232, v17\n\t"
  "v_add_f32 v233, v233, v18\n\t"
  "v_lshlrev_b32 v17, 16, v157\n\t"
  "v_and_b32 v18, v19, v157\n\t"
  "v_add_f32 v234, v234, v17\n\t"
  "v_add_f32 v235, v235, v18\n\t"
  "v_cvt_pk_bf16_f32 v232, v232, v233\n\t"
  "v_cvt_pk_bf16_f32 v233, v234, v235\n\t"
  "buffer_store_dwordx2 v[232:233], v38, s[28:31], s41 offen offset:96\n\t"
  "v_lshlrev_b32 v17, 16, v118\n\t"
  "v_and_b32 v18, v19, v118\n\t"
  "v_add_f32 v236, v236, v17\n\t"
  "v_add_f32 v237, v237, v18\n\t"
  "v_lshlrev_b32 v17, 16, v119\n\t"
  "v_and_b32 v18, v19, v119\n\t"
  "v_add_f32 v238, v238, v17\n\t"
  "v_add_f32 v239, v239, v18\n\t"
  "v_lshlrev_b32 v17, 16, v138\n\t"
  "v_and_b32 v18, v19, v138\n\t"
  "v_add_f32 v236, v236, v17\n\t"
  "v_add_f32 v237, v237, v18\n\t"
  "v_lshlrev_b32 v17, 16, v139\n\t"
  "v_and_b32 v18, v19, v139\n\t"
  "v_add_f32 v238, v238, v17\n\t"
  "v_add_f32 v239, v239, v18\n\t"
  "v_lshlrev_b32 v17, 16, v158\n\t"
  "v_and_b32 v18, v19, v158\n\t"
  "v_add_f32 v236, v236, v17\n\t"
  "v_add_f32 v237, v237, v18\n\t"
  "v_lshlrev_b32 v17, 16, v159\n\t"
  "v_and_b32 v18, v19, v159\n\t"
  "v_add_f32 v238, v238, v17\n\t"
  "v_add_f32 v239, v239, v18\n\t"
  "v_cvt_pk_bf16_f32 v236, v236, v237\n\t"
  "v_cvt_pk_bf16_f32 v237, v238, v239\n\t"
  "buffer_store_dwordx2 v[236:237], v38, s[28:31], s41 offen offset:128\n\t"
  "s_waitcnt vmcnt(10)\n\t"
  "s_cmp_lg_u32 %7, 0\n\t"
  "s_cbranch_scc1 84f\n\t"
  "s_mov_b64 exec, 15\n\t"
  "global_store_dword v72, v74, %13 sc1\n\t"
  "s_mov_b64 exec, -1\n\t"
  "84:\n\t"
  "64:\n\t"
  :: "v"(lane), "s"(ob0), "s"(ob1), "s"(wb0), "s"(wb1), "s"(cb0), "s"(cb1), "s"(w), "s"(kq), "s"(nq),
     "s"(pb0), "s"(pb1), "s"(nonce), "s"(syn), "s"(bid), "s"(syn), "v"(tid)
  : "v16","v17","v18","v19","v20","v21","v22","v23","v24","v25","v26","v27","v28","v29","v30","v31","v32","v33","v34","v35","v36","v37","v38","v39","v40","v41","v42","v43","v44","v45","v46","v47","v48","v49","v50","v51","v52","v53","v54","v55","v56","v57","v58","v59","v60","v61","v62","v63","v64","v65","v66","v67","v68","v69","v70","v71","v72","v73","v74","v75","v76","v77","v78","v79","v80","v81","v82","v83","v84","v85","v86","v87","v88","v89","v90","v91","v92","v93","v94","v95","v96","v97","v98","v99","v100","v101","v102","v103","v104","v105","v106","v107","v108","v109","v110","v111","v112","v113","v114","v115","v116","v117","v118","v119","v120","v121","v122","v123","v124","v125","v126","v127","v128","v129","v130","v131","v132","v133","v134","v135","v136","v137","v138","v139","v140","v141","v142","v143","v144","v145","v146","v147","v148","v149","v150","v151","v152","v153","v154","v155","v156","v157","v158","v159","v160","v161","v162","v163","v164","v165","v166","v167","v168","v169","v170","v171","v172","v173","v174","v175","v176","v177","v178","v179","v180","v181","v182","v183","v184","v185","v186","v187","v188","v189","v190","v191","v192","v193","v194","v195","v196","v197","v198","v199","v200","v201","v202","v203","v204","v205","v206","v207","v208","v209","v210","v211","v212","v213","v214","v215","v216","v217","v218","v219","v220","v221","v222","v223","v224","v225","v226","v227","v228","v229","v230","v231","v232","v233","v234","v235","v236","v237","v238","v239","a0","a1","a2","a3","a4","a5","a6","a7","a8","a9","a10","a11","a12","a13","a14","a15","a16","a17","a18","a19","a20","a21","a22","a23","a24","a25","a26","a27","a28","a29","a30","a31","a32","a33","a34","a35","a36","a37","a38","a39","a40","a41","a42","a43","a44","a45","a46","a47","a48","a49","a50","a51","a52","a53","a54","a55","a56","a57","a58","a59","a60","a61","a62","a63","a64","a65","a66","a67","a68","a69","a70","a71","a72","a73","a74","a75","a76","a77","a78","a79","a80","a81","a82","a83","a84","a85","a86","a87","a88","a89","a90","a91","a92","a93","a94","a95","a96","a97","a98","a99","a100","a101","a102","a103","a104","a105","a106","a107","a108","a109","a110","a111","a112","a113","a114","a115","a116","a117","a118","a119","a120","a121","a122","a123","a124","a125","a126","a127","a128","a129","a130","a131","a132","a133","a134","a135","a136","a137","a138","a139","a140","a141","a142","a143","a144","a145","a146","a147","a148","a149","a150","a151","a152","a153","a154","a155","a156","a157","a158","a159","a160","a161","a162","a163","a164","a165","a166","a167","a168","a169","a170","a171","a172","a173","a174","a175","a176","a177","a178","a179","a180","a181","a182","a183","a184","a185","a186","a187","a188","a189","a190","a191","a192","a193","a194","a195","a196","a197","a198","a199","a200","a201","a202","a203","a204","a205","a206","a207","a208","a209","a210","a211","a212","a213","a214","a215","a216","a217","a218","a219","a220","a221","a222","a223","a224","a225","a226","a227","a228","a229","a230","a231","a232","a233","a234","a235","a236","a237","a238","a239","s20","s21","s22","s23","s24","s25","s26","s27","s28","s29","s30","s31","s36","s37","s38","s39","s40","s41","s42","s43","s44","s45","s46","s47","s48","s49","s50","s51","s52","s53","s54","s55","s56","s57","s58","s59","s60","s61","s62","s63","vcc","scc","memory");
  }
}
extern "C" void kernel_launch(void* const* d_in, const int* in_sizes, int n_in, void* d_out, int out_size, void* d_ws, size_t ws_size, hipStream_t stream){
  (void)hipFuncSetAttribute((const void*)fused_kernel,hipFuncAttributeMaxDynamicSharedMemorySize,163840);
  fused_kernel<<<256,256,163840,stream>>>((const u16*)d_in[0],(const u16*)d_in[1],(const u16*)d_in[2],(const u16*)d_in[3],(const u16*)d_in[4],
                                          (const u16*)d_in[5],(const u16*)d_in[6],(const u16*)d_in[7],(const u16*)d_in[8],(const u16*)d_in[9],(char*)d_ws,(u16*)d_out);
}
```
